```python
import torch

def _softmax(x, axis=-1):
    return torch.softmax(x, dim=axis)

def _gelu(x):
    return torch.nn.functional.gelu(x, approximate='tanh')

def _silu(x):
    return torch.nn.functional.silu(x)

def _sigmoid(x):
    return torch.sigmoid(x)

def _layernorm(x, axis=-1, eps=1e-05):
    m = x.mean(dim=axis, keepdim=True)
    v = x.var(dim=axis, keepdim=True, unbiased=False)
    return (x - m) / torch.sqrt(v + eps)

def _scatter_add(tgt, idx, src, axis=0):
    return tgt.clone().index_add_(axis, idx, src)

def _segment_sum(src, seg, n):
    out = torch.zeros((n,) + src.shape[1:], dtype=src.dtype)
    return out.index_add_(0, seg, src)

def _pad(x, pw):
    flat = tuple((v for pair in reversed(pw) for v in pair))
    return torch.nn.functional.pad(x, flat)
kernel_name = 'adaLN_cond__spectral_fnet__mixer_gmlp__prompt'

def setup_inputs():

    _w = lambda a: (lambda t: t.to(torch.bfloat16) if t.is_floating_point() else t)(torch.as_tensor(a))

    def _A_setup():
        B, S, D = (1, 2048, 640)
        ns = torch.arange(S)[:, None] * torch.arange(S)[None, :]
        nd = torch.arange(D)[:, None] * torch.arange(D)[None, :]
        Ws_re = torch.cos(-2.0 * torch.pi * ns / S).to(float)
        Ws_im = torch.sin(-2.0 * torch.pi * ns / S).to(float)
        Wd_re = torch.cos(-2.0 * torch.pi * nd / D).to(float)
        Wd_im = torch.sin(-2.0 * torch.pi * nd / D).to(float)
        return {'x': _w((lambda *s: torch.randn(*s, dtype=torch.bfloat16) * 0.1)(B, S, D)), 'dft_seq_re': _w(Ws_re), 'dft_seq_im': _w(Ws_im), 'dft_hid_re': _w(Wd_re), 'dft_hid_im': _w(Wd_im)}
    _A_in = _A_setup()

    def _B_setup():
        B, S, D, F = (1, 2048, 640, 1728)
        Fh = F // 2
        return {'x': _w((lambda *s: torch.randn(*s, dtype=torch.bfloat16) * 0.1)(B, S, D)), 'w_in': _w((lambda *s: torch.randn(*s, dtype=torch.bfloat16) * 3.2)(D, 2 * Fh)), 'w_sp': _w(0.01 * (lambda *s: torch.randn(*s, dtype=torch.bfloat16) * 3.2)(S, S)), 'b_sp': _w(1.0 + 0.01 * (lambda *s: torch.randn(*s, dtype=torch.bfloat16) * 1.6)(S)), 'w_out': _w((lambda *s: torch.randn(*s, dtype=torch.bfloat16) * 1.6)(Fh, D))}
    _B_in = _B_setup()
    return {'A_x': _A_in['x'], 'A_dft_seq_re': _A_in['dft_seq_re'], 'A_dft_seq_im': _A_in['dft_seq_im'], 'A_dft_hid_re': _A_in['dft_hid_re'], 'A_dft_hid_im': _A_in['dft_hid_im'], 'B_x': _B_in['x'], 'B_w_in': _B_in['w_in'], 'B_w_sp': _B_in['w_sp'], 'B_b_sp': _B_in['b_sp'], 'B_w_out': _B_in['w_out'], 'c': _w((lambda *s: torch.randn(*s, dtype=torch.bfloat16) * 0.2)(1, 640) * 8.0), 'w_mod': _w((lambda *s: torch.randn(*s, dtype=torch.bfloat16) * 0.2)(640, 6 * 640) * 640 ** (-0.5) * 8.0)}

def reference(A_x, A_dft_seq_re, A_dft_seq_im, A_dft_hid_re, A_dft_hid_im, B_x, B_w_in, B_w_sp, B_b_sp, B_w_out, c, w_mod):

    def _A_ref(x, dft_seq_re, dft_seq_im, dft_hid_re, dft_hid_im):
        a = torch.einsum('bsd,ed->bse', x, dft_hid_re)
        b = torch.einsum('bsd,ed->bse', x, dft_hid_im)
        y = torch.einsum('ts,bse->bte', dft_seq_re, a) - torch.einsum('ts,bse->bte', dft_seq_im, b)
        return y

    def _B_ref(x, w_in, w_sp, b_sp, w_out):
        B, S, D = x.shape
        F = w_in.shape[-1]
        Fh = F // 2
        z = _gelu(torch.einsum('bsd,df->bsf', x, w_in))
        z1 = z[..., :Fh].clone()
        z2 = _layernorm(z[..., Fh:], axis=-1)
        g = torch.einsum('ts,bsf->btf', w_sp, z2) + b_sp[None, :, None]
        return torch.einsum('bsf,fd->bsd', z1 * g, w_out)
    x = A_x
    mod = (_silu(c) @ w_mod).reshape(c.shape[0], 6, 1, -1)
    sa, ca, ga, sb, cb, gb = (mod[:, 0].clone(), mod[:, 1].clone(), mod[:, 2].clone(), mod[:, 3].clone(), mod[:, 4].clone(), mod[:, 5].clone())
    h = _layernorm(x, axis=-1) * (1 + ca) + sa
    x = x + ga * _A_ref(h, A_dft_seq_re, A_dft_seq_im, A_dft_hid_re, A_dft_hid_im)
    h = _layernorm(x, axis=-1) * (1 + cb) + sb
    x = x + gb * _B_ref(h, B_w_in, B_w_sp, B_b_sp, B_w_out)
    return x
```

```cpp
#include <hip/hip_runtime.h>
#include <hip/hip_bf16.h>
typedef unsigned short u16;
typedef short bf16x8 __attribute__((ext_vector_type(8)));
typedef float f32x4 __attribute__((ext_vector_type(4)));
__device__ inline float b2f(u16 b) { return __uint_as_float(((unsigned)b) << 16); }
__device__ inline u16 f2b(float f) { unsigned u = __float_as_uint(f); u += 0x7FFFu + ((u >> 16) & 1u); return (u16)(u >> 16); }
constexpr int S = 2048, D = 640, F = 1728, FH = 864;

__global__ void k_mod(const u16* c, const u16* wmod, float* mod) {
  int j = blockIdx.x * blockDim.x + threadIdx.x;
  if (j >= 6 * D) return;
  float acc = 0.f;
  for (int i = 0; i < D; ++i) { float cv = b2f(c[i]); float s = cv / (1.f + __expf(-cv)); s = b2f(f2b(s)); acc += s * b2f(wmod[(size_t)i * 6 * D + j]); }
  mod[j] = b2f(f2b(acc));
}
__global__ void k_ln(const u16* x, int ld, int n, const float* sc, const float* sh, u16* out, int ldo) {
  int row = blockIdx.x * 4 + (threadIdx.x >> 6), lane = threadIdx.x & 63;
  float s = 0.f;
  for (int i = lane; i < n; i += 64) s += b2f(x[(size_t)row * ld + i]);
  for (int o = 32; o; o >>= 1) s += __shfl_xor(s, o);
  float m = s / n, q = 0.f;
  for (int i = lane; i < n; i += 64) { float d = b2f(x[(size_t)row * ld + i]) - m; q += d * d; }
  for (int o = 32; o; o >>= 1) q += __shfl_xor(q, o);
  float r = rsqrtf(q / n + 1e-5f);
  for (int i = lane; i < n; i += 64) { float v = (b2f(x[(size_t)row * ld + i]) - m) * r; if (sc) v = v * (1.f + sc[i]) + sh[i]; out[(size_t)row * ldo + i] = f2b(v); }
}
template<bool BT, int EPI>
__global__ __launch_bounds__(256) void k_gemm(const u16* A, int lda, const u16* B, int ldb, const u16* A2, const u16* B2, int K, u16* C, int ldc,
                       const u16* res, int ldr, const float* gate, const u16* bias) {
  __shared__ u16 sA[64][40], sB[64][40];
  const int bm = blockIdx.y * 64, bn = blockIdx.x * 32;
  const int tid = threadIdx.x, lane = tid & 63, w = tid >> 6;
  f32x4 acc[2] = {{0,0,0,0},{0,0,0,0}};
  for (int pass = 0; pass < (A2 ? 2 : 1); ++pass) {
    const u16* a = pass ? A2 : A; const u16* b = pass ? B2 : B;
    for (int k0 = 0; k0 < K; k0 += 32) {
      { int r = tid >> 2, cc = (tid & 3) * 8; *(uint4*)&sA[r][cc] = *(const uint4*)&a[(size_t)(bm + r) * lda + k0 + cc]; }
      if (BT) { if (tid < 128) { int r = tid >> 2, cc = (tid & 3) * 8; *(uint4*)&sB[r][cc] = *(const uint4*)&b[(size_t)(bn + r) * ldb + k0 + cc]; } }
      else { if (tid < 128) { int kk = tid >> 2, nn = (tid & 3) * 8; uint4 v = *(const uint4*)&b[(size_t)(k0 + kk) * ldb + bn + nn]; const u16* p = (const u16*)&v; for (int i = 0; i < 8; ++i) sB[nn + i][kk] = p[i]; } }
      __syncthreads();
      bf16x8 af = *(const bf16x8*)&sA[w * 16 + (lane & 15)][(lane >> 4) * 8];
      if (pass) for (int i = 0; i < 8; ++i) af[i] ^= (short)0x8000;
      for (int n = 0; n < 2; ++n) { bf16x8 bf = *(const bf16x8*)&sB[n * 16 + (lane & 15)][(lane >> 4) * 8];
        acc[n] = __builtin_amdgcn_mfma_f32_16x16x32_bf16(af, bf, acc[n], 0, 0, 0); }
      __syncthreads();
    }
  }
  for (int n = 0; n < 2; ++n) for (int j = 0; j < 4; ++j) {
    int r = bm + w * 16 + (lane >> 4) * 4 + j, cidx = bn + n * 16 + (lane & 15);
    float v = acc[n][j];
    if (EPI == 1) { v = b2f(f2b(v)); float u = 0.7978845608f * (v + 0.044715f * v * v * v); v = 0.5f * v * (1.f + tanhf(u)); }
    if (EPI == 2) v = b2f(res[(size_t)r * ldr + cidx]) + gate[cidx] * v;
    if (EPI == 3) v = b2f(res[(size_t)r * ldr + cidx]) * (v + b2f(bias[r]));
    C[(size_t)r * ldc + cidx] = f2b(v);
  }
}
__global__ void k_empty(int* p) { if (p == nullptr) return; }

#define NEXTRA 0
extern "C" void kernel_launch(void* const* d_in, const int* in_sizes, int n_in, void* d_out, int out_size, void* d_ws, size_t ws_size, hipStream_t stream) {
  const u16* x = (const u16*)d_in[0]; const u16* wsre = (const u16*)d_in[1]; const u16* wsim = (const u16*)d_in[2];
  const u16* wdre = (const u16*)d_in[3]; const u16* wdim = (const u16*)d_in[4];
  const u16* win = (const u16*)d_in[6]; const u16* wsp = (const u16*)d_in[7]; const u16* bsp = (const u16*)d_in[8]; const u16* wout = (const u16*)d_in[9];
  const u16* c = (const u16*)d_in[10]; const u16* wmod = (const u16*)d_in[11];
  char* ws = (char*)d_ws;
  float* mod = (float*)ws; u16* h = (u16*)(ws + (1 << 20)); u16* a = (u16*)(ws + (4 << 20)); u16* b = (u16*)(ws + (8 << 20));
  u16* x1 = (u16*)(ws + (12 << 20)); u16* z = (u16*)(ws + (16 << 20)); u16* z2 = (u16*)(ws + (24 << 20)); u16* u = (u16*)(ws + (28 << 20));
  u16* out = (u16*)d_out;
  k_mod<<<15, 256, 0, stream>>>(c, wmod, mod);
  k_ln<<<S / 4, 256, 0, stream>>>(x, D, D, mod + D, mod, h, D);
  k_gemm<true, 0><<<dim3(D / 32, S / 64), 256, 0, stream>>>(h, D, wdre, D, nullptr, nullptr, D, a, D, nullptr, 0, nullptr, nullptr);
  k_gemm<true, 0><<<dim3(D / 32, S / 64), 256, 0, stream>>>(h, D, wdim, D, nullptr, nullptr, D, b, D, nullptr, 0, nullptr, nullptr);
  k_gemm<false, 2><<<dim3(D / 32, S / 64), 256, 0, stream>>>(wsre, S, a, D, wsim, b, S, x1, D, x, D, mod + 2 * D, nullptr);
  k_ln<<<S / 4, 256, 0, stream>>>(x1, D, D, mod + 4 * D, mod + 3 * D, h, D);
  k_gemm<false, 1><<<dim3(F / 32, S / 64), 256, 0, stream>>>(h, D, win, F, nullptr, nullptr, D, z, F, nullptr, 0, nullptr, nullptr);
  k_ln<<<S / 4, 256, 0, stream>>>(z + FH, F, FH, nullptr, nullptr, z2, FH);
  k_gemm<false, 3><<<dim3(FH / 32, S / 64), 256, 0, stream>>>(wsp, S, z2, FH, nullptr, nullptr, S, u, FH, z, F, nullptr, bsp);
  k_gemm<false, 2><<<dim3(D / 32, S / 64), 256, 0, stream>>>(u, FH, wout, D, nullptr, nullptr, FH, out, D, x1, D, mod + 5 * D, nullptr);
  for (int i = 0; i < NEXTRA; ++i) k_empty<<<256, 256, 0, stream>>>((int*)d_ws);
}
```

```cpp
#include <hip/hip_runtime.h>
#include <hip/hip_bf16.h>

typedef unsigned short u16;
typedef unsigned long long u64;
typedef short bf16x8 __attribute__((ext_vector_type(8)));
typedef short s16x4 __attribute__((ext_vector_type(4)));
typedef __bf16 bf16x2 __attribute__((ext_vector_type(2)));
typedef float f32x2 __attribute__((ext_vector_type(2)));
typedef float f32x4 __attribute__((ext_vector_type(4)));
typedef unsigned u32x2 __attribute__((ext_vector_type(2)));
typedef unsigned u32x4 __attribute__((ext_vector_type(4)));
typedef __amdgpu_buffer_rsrc_t rsrc_t;
#define LAS __attribute__((address_space(3)))
#define GAS __attribute__((address_space(1)))
#define DEVI __device__ __forceinline__

constexpr int SEQ = 2048, DM = 640, FF = 1728, FH = 864;

constexpr unsigned WS_EPOCH = 0x0;
constexpr unsigned WS_TMO   = 0x100;
constexpr unsigned WS_FLAG0 = 0x1000;
constexpr unsigned WS_FLAGB = 0x2000;
constexpr unsigned WS_FLAG1 = 0x4000;
constexpr unsigned WS_FLAG2 = 0x6000;
constexpr unsigned WS_FLAG4 = 0x8000;
constexpr unsigned WS_FLAG3 = 0xA000;
constexpr unsigned WS_MODP  = 0x10000;
constexpr unsigned WS_UV    = 0x20000;
constexpr unsigned WS_ST1   = 0x30000;
constexpr unsigned WS_ST2   = 0x50000;
constexpr unsigned WS_XNT   = 0x100000;
constexpr unsigned WS_PQ    = 0x400000;
constexpr unsigned WS_X1C   = 0xA00000;
constexpr unsigned WS_Z2T   = 0xD00000;
constexpr unsigned WS_U     = 0x1100000;
constexpr unsigned WS_WINR  = 0x1500000;
constexpr unsigned WS_WOUTR = 0x1800000;
constexpr unsigned WS_Z1    = 0x1A00000;
constexpr unsigned WS_X1    = 0x2000000;
constexpr unsigned WS_STAMP = 0x2400000;
constexpr unsigned WS_USED  = 0x2500000;

constexpr int LDS_BYTES = 163840;
constexpr int L_MISC = 155648;
constexpr int L_CNT  = L_MISC;
constexpr int L_TAB  = L_MISC + 256;
constexpr int L_TABE = L_MISC + 2048;
constexpr int L_UV   = L_MISC + 4608;
constexpr int L_P0T  = 114688;
constexpr int L_RT   = 114688 + 12288;
constexpr int P0T_RS = 1296;

constexpr unsigned SPIN_MAX = 1u << 17;

struct Params {
  const u16 *x, *wsre, *wsim, *wdre, *wdim, *win, *wsp, *bsp, *wout, *c, *wmod;
  u16* out;
  char* ws;
};

DEVI rsrc_t mkrs(const void* p, unsigned bytes) { return __builtin_amdgcn_make_buffer_rsrc((void*)p, 0, (int)bytes, 0x00020000); }
template <int AUX> DEVI u32x4 ld16(rsrc_t r, unsigned v, unsigned s) { return __builtin_amdgcn_raw_buffer_load_b128(r, (int)v, (int)s, AUX); }
template <int AUX> DEVI u32x2 ld8(rsrc_t r, unsigned v, unsigned s) { return __builtin_amdgcn_raw_buffer_load_b64(r, (int)v, (int)s, AUX); }
template <int AUX> DEVI unsigned ld4(rsrc_t r, unsigned v, unsigned s) { return __builtin_amdgcn_raw_buffer_load_b32(r, (int)v, (int)s, AUX); }
template <int AUX> DEVI unsigned ld2(rsrc_t r, unsigned v, unsigned s) { return (unsigned)__builtin_amdgcn_raw_buffer_load_b16(r, (int)v, (int)s, AUX) & 0xffffu; }
template <int AUX> DEVI void st16(u32x4 d, rsrc_t r, unsigned v, unsigned s) { __builtin_amdgcn_raw_buffer_store_b128(d, r, (int)v, (int)s, AUX); }
template <int AUX> DEVI void st8(u32x2 d, rsrc_t r, unsigned v, unsigned s) { __builtin_amdgcn_raw_buffer_store_b64(d, r, (int)v, (int)s, AUX); }
template <int AUX> DEVI void dma16(rsrc_t r, LAS char* dst, unsigned v, unsigned s) { __builtin_amdgcn_raw_ptr_buffer_load_lds(r, (LAS void*)dst, 16, (int)v, (int)s, 0, AUX); }
constexpr int SC1 = 16;
constexpr unsigned OOB = 0x7ffffff0u;

template <int N> DEVI void wait_vm() { asm volatile("s_waitcnt vmcnt(%0)" ::"n"(N > 63 ? 63 : N) : "memory"); }
DEVI void wait_lgkm0() { __builtin_amdgcn_s_waitcnt(0xC07F); }
DEVI void BAR() { asm volatile("s_barrier" ::: "memory"); }
DEVI void CFENCE() { asm volatile("" ::: "memory"); }
#define SCHED() __builtin_amdgcn_sched_barrier(0)

DEVI float bflo(unsigned v) { return __uint_as_float(v << 16); }
DEVI float bfhi(unsigned v) { return __uint_as_float(v & 0xffff0000u); }
DEVI unsigned pk2(float lo, float hi) { f32x2 f = {lo, hi}; return __builtin_bit_cast(unsigned, __builtin_convertvector(f, bf16x2)); }
DEVI float rbf(float v) { return bflo(pk2(v, 0.f)); }
DEVI f32x4 mfma(bf16x8 a, bf16x8 b, f32x4 c) { return __builtin_amdgcn_mfma_f32_16x16x32_bf16(a, b, c, 0, 0, 0); }
DEVI bf16x8 asfrag(u32x4 v) { return __builtin_bit_cast(bf16x8, v); }
DEVI bf16x8 ldsfrag(LAS char* p) { return *(LAS bf16x8*)p; }

template <int CTRL> DEVI float dpp_add(float v) { return v + __int_as_float(__builtin_amdgcn_update_dpp(0, __float_as_int(v), CTRL, 0xf, 0xf, false)); }
DEVI float row16_sum(float v) { v = dpp_add<0x128>(v); v = dpp_add<0x124>(v); v = dpp_add<0x122>(v); return dpp_add<0x121>(v); }
DEVI float xq_sum(float v) { v += __shfl_xor(v, 16); v += __shfl_xor(v, 32); return v; }
DEVI float wave_sum(float v) { return xq_sum(row16_sum(v)); }

DEVI float gelu_tanh(float x) {
  const float u = x * (1.0f + 0.044715f * x * x) * (-2.0f * 0.7978845608028654f * 1.4426950408889634f);
  return x * __builtin_amdgcn_rcpf(1.0f + __builtin_amdgcn_exp2f(u));
}

DEVI u32x4 pair_swap(u32x2 a, u32x2 b) {
  auto s0 = __builtin_amdgcn_permlane16_swap(a.x, b.x, false, false);
  auto s1 = __builtin_amdgcn_permlane16_swap(a.y, b.y, false, false);
  return (u32x4){s0[0], s1[0], s0[1], s1[1]};
}

DEVI void note_timeout(char* ws, unsigned code, int lane) {
  if (lane == 0) __hip_atomic_store((GAS unsigned*)(ws + WS_TMO), code, __ATOMIC_RELAXED, __HIP_MEMORY_SCOPE_AGENT);
}
DEVI void poll_flags(char* ws, unsigned off, int n, u64 T, int lane) {
  for (unsigned spins = 0;; ++spins) {
    u64 f = T;
    if (lane < n) f = __hip_atomic_load((GAS u64*)(ws + off) + lane, __ATOMIC_RELAXED, __HIP_MEMORY_SCOPE_AGENT);
    if (__all(f == T)) break;
    if (spins > SPIN_MAX) { note_timeout(ws, 0x100u | (off >> 12), lane); break; }
    __builtin_amdgcn_s_sleep(1);
  }
  CFENCE();
}
DEVI void poll_flags256(char* ws, unsigned off, u64 T, int lane) {
  for (unsigned spins = 0;; ++spins) {
    bool ok = true;
#pragma unroll
    for (int i = 0; i < 4; ++i) ok &= __hip_atomic_load((GAS u64*)(ws + off) + i * 64 + lane, __ATOMIC_RELAXED, __HIP_MEMORY_SCOPE_AGENT) == T;
    if (__all(ok)) break;
    if (spins > SPIN_MAX) { note_timeout(ws, 0x200u | (off >> 12), lane); break; }
    __builtin_amdgcn_s_sleep(1);
  }
  CFENCE();
}
DEVI void set_flag(char* ws, unsigned off, u64 T, int lane) {
  if (lane == 0) __hip_atomic_store((GAS u64*)(ws + off), T, __ATOMIC_RELAXED, __HIP_MEMORY_SCOPE_AGENT);
}
DEVI bool arrive_last(LAS char* lds, int idx, unsigned n, int lane) {
  unsigned old = 0;
  if (lane == 0) old = __hip_atomic_fetch_add((LAS unsigned*)(lds + L_CNT) + idx, 1u, __ATOMIC_RELAXED, __HIP_MEMORY_SCOPE_WORKGROUP);
  return (unsigned)__builtin_amdgcn_readfirstlane(old) == n - 1;
}
DEVI void publish(LAS char* lds, int cidx, unsigned nwaves, char* ws, unsigned flag_off, u64 T, int lane) {
  wait_vm<0>();
  if (arrive_last(lds, cidx, nwaves, lane)) set_flag(ws, flag_off, T, lane);
}

#define LANE_IDS() int tz_ = threadIdx.x; asm volatile("" : "+v"(tz_)); const int lane = tz_ & 63, l15 = lane & 15, q = lane >> 4; (void)l15; (void)q

#define STAMP(k) do { } while (0)
#define STAMPC(k) do { } while (0)

template <int I, int NT, int RING, int PC> struct LoaderTail {
  static DEVI void run() {
    if constexpr (I <= NT - 2) { wait_vm<(NT - I - 2) * PC>(); BAR(); LoaderTail<I + 1, NT, RING, PC>::run(); }
  }
};
template <int NT, int RING, int PC, class Issue> DEVI void loader_prefill(Issue issue) {
  constexpr int PRE = RING < NT ? RING : NT;
#pragma unroll 1
  for (int t = 0; t < PRE; ++t) { if (PRE * PC > 63) wait_vm<63 - PC>(); issue(t, t); }
}
template <int NT, int RING, int PC, class Issue> DEVI void loader_run(Issue issue) {
  constexpr int PRE = RING < NT ? RING : NT;
  wait_vm<(PRE - 1) * PC>(); BAR();
  if constexpr (NT > RING) {
    int slot = 0;
#pragma unroll 1
    for (int i = 0; i < NT - RING; ++i) {
      wait_vm<((RING - 2) * PC > 63 - PC ? 63 - PC : (RING - 2) * PC)>(); BAR();
      issue(i + RING, slot);
      slot = (slot + 1 == RING) ? 0 : slot + 1;
    }
  }
  LoaderTail<(NT > RING ? NT - RING : 0), NT, RING, PC>::run();
  BAR();
}
template <int NBAR> DEVI void idle_phase() {
#pragma unroll 1
  for (int i = 0; i < NBAR; ++i) BAR();
}

template <int I, int NMF, int NRD, int PER> struct Interleave {
  static DEVI void run() {
    if constexpr (I < NMF) {
      __builtin_amdgcn_sched_group_barrier(0x8, 1, 0);
      constexpr int left = NRD - I * PER, now = left < PER ? left : PER;
      if constexpr (now > 0) __builtin_amdgcn_sched_group_barrier(0x100, now, 0);
      Interleave<I + 1, NMF, NRD, PER>::run();
    }
  }
};
template <int NMF, int NRD, int PER> DEVI void interleave() { Interleave<0, NMF, NRD, PER>::run(); }

constexpr int P1_SA = 7, P1_DB = 4, P1_DOFF = 114688;
constexpr int P2_RING = 15;
constexpr int P3_RING = 10;
constexpr int P4_SA = 10, P4_DB = 5, P4_DOFF = 81920;
constexpr int P5_RING = 14;

__global__ __launch_bounds__(512, 2) void adaLN_cond__spectral_fnet__mixer_gmlp__prompt_kernel(Params p) {
  extern __shared__ __attribute__((aligned(16))) char smem_[];
  LAS char* lds = (LAS char*)smem_;
  const int tid = threadIdx.x, lane = tid & 63, wid = __builtin_amdgcn_readfirstlane(tid >> 6);
  const int b = blockIdx.x, xg = b & 7, i8 = b >> 3;
  const int g = xg * 4 + (i8 >> 3), j = i8 & 7;
  const int l15 = lane & 15, q = lane >> 4;
  char* const ws = p.ws;
  const int rot = xg * 4;

  STAMP(0); STAMPC(30);
  const u64 Ein = __hip_atomic_load((GAS u64*)(ws + WS_EPOCH), __ATOMIC_RELAXED, __HIP_MEMORY_SCOPE_AGENT);
  if (tid < 32) *((LAS unsigned*)(lds + L_CNT) + tid) = 0u;

  if (wid < 4) {
    const int w = wid;
    {
      const rsrc_t rx = mkrs(p.x, SEQ * DM * 2);
      u32x4 v[2][2];
#pragma unroll
      for (int r = 0; r < 2; ++r)
#pragma unroll
        for (int h = 0; h < 2; ++h) v[r][h] = ld16<0>(rx, lane < 40 ? (unsigned)((lane + 40 * h) * 16) : OOB, (unsigned)(64 * g + 8 * j + 2 * w + r) * (DM * 2));
      wait_lgkm0();
      BAR();
#pragma unroll
      for (int r = 0; r < 2; ++r) {
        float f[16];
#pragma unroll
        for (int h = 0; h < 2; ++h)
#pragma unroll
          for (int i = 0; i < 4; ++i) { f[h * 8 + 2 * i] = bflo(v[r][h][i]); f[h * 8 + 2 * i + 1] = bfhi(v[r][h][i]); }
        float s = 0.f;
#pragma unroll
        for (int i = 0; i < 16; ++i) s += f[i];
        const float mean = wave_sum(s) * (1.0f / DM);
        float m2 = 0.f;
#pragma unroll
        for (int i = 0; i < 16; ++i) { f[i] -= mean; m2 += f[i] * f[i]; }
        if (lane >= 40) m2 = 0.f;
        const float rstd = 1.0f / sqrtf(wave_sum(m2) * (1.0f / DM) + 1e-5f);
        if (lane < 40) {
#pragma unroll
          for (int h = 0; h < 2; ++h) {
            u32x4 o;
#pragma unroll
            for (int i = 0; i < 4; ++i) o[i] = pk2(f[h * 8 + 2 * i] * rstd, f[h * 8 + 2 * i + 1] * rstd);
            *(LAS u32x4*)(lds + L_P0T + (2 * w + r) * P0T_RS + (lane + 40 * h) * 16) = o;
          }
        }
      }
      wait_lgkm0(); CFENCE();
      if (lane == 0) __hip_atomic_fetch_add((LAS unsigned*)(lds + L_CNT) + 0, 1u, __ATOMIC_RELAXED, __HIP_MEMORY_SCOPE_WORKGROUP);
      for (unsigned spins = 0; spins < SPIN_MAX; ++spins) {
        if (__hip_atomic_load((LAS unsigned*)(lds + L_CNT) + 0, __ATOMIC_RELAXED, __HIP_MEMORY_SCOPE_WORKGROUP) >= 4u) break;
      }
      CFENCE();
      const rsrc_t rxn = mkrs(ws + WS_XNT, 8 * 32 * 10240);
#pragma unroll
      for (int it = 0; it < 3; ++it) {
        const int pc = 4 * it + q, nf = 10 * w + (pc < 10 ? pc : 9);
        const int qq = l15 >> 2, pp = l15 & 3;
        LAS char* a0 = lds + L_P0T + qq * P0T_RS + (16 * nf + 4 * pp) * 2;
        const u32x2 lo = __builtin_bit_cast(u32x2, __builtin_amdgcn_ds_read_tr16_b64_v4i16((LAS s16x4*)a0));
        const u32x2 hi = __builtin_bit_cast(u32x2, __builtin_amdgcn_ds_read_tr16_b64_v4i16((LAS s16x4*)(a0 + 4 * P0T_RS)));
        const int jj = nf / 5, nfl = nf - 5 * jj;
        const unsigned off = (unsigned)(((jj * 32 + g) * 10 + (j >> 2) * 5 + nfl) * 1024 + ((j & 3) * 16 + l15) * 16);
        st16<SC1>((u32x4){lo.x, lo.y, hi.x, hi.y}, rxn, pc < 10 ? off : OOB, 0);
      }
    }
    const u64 T = (((u64)(unsigned)__builtin_amdgcn_readfirstlane((unsigned)(Ein >> 32)) << 32) | (unsigned)__builtin_amdgcn_readfirstlane((unsigned)Ein)) + 1;
    publish(lds, 1, 4, ws, WS_FLAG0 + (g * 8 + j) * 8, T, lane);
    STAMP(1);

    {
      LANE_IDS();
      f32x4 acc[2][5];
#pragma unroll
      for (int m = 0; m < 2; ++m)
#pragma unroll
        for (int n = 0; n < 5; ++n) acc[m][n] = (f32x4){0.f, 0.f, 0.f, 0.f};
      float rs[2] = {0.f, 0.f};
      unsigned ones = 0x3f803f80u; asm volatile("" : "+v"(ones));
      const unsigned aoff0 = (w * 16 + l15) * 128 + ((q ^ (l15 >> 1)) << 4), aoff1 = aoff0 ^ 64;
      const unsigned boff = P1_DOFF + lane * 16;
      bf16x8 XA[2], XB[5], YA[2], YB[5];
      auto rd = [&](bf16x8(&A)[2], bf16x8(&B)[5], int sa, int sb, int kk) {
        LAS char* pa = lds + sa * 16384 + (kk ? aoff1 : aoff0);
        LAS char* pb = lds + sb * 10240 + boff + kk * 5120;
#pragma unroll
        for (int n = 0; n < 5; ++n) B[n] = ldsfrag(pb + n * 1024);
        A[0] = ldsfrag(pa); A[1] = ldsfrag(pa + 8192);
      };
      auto mm = [&](const bf16x8(&A)[2], const bf16x8(&B)[5]) {
#pragma unroll
        for (int m = 0; m < 2; ++m) {
#pragma unroll
          for (int n = 0; n < 5; ++n) acc[m][n] = mfma(B[n], A[m], acc[m][n]);
          const u32x4 au = __builtin_bit_cast(u32x4, A[m]);
#pragma unroll
          for (int i = 0; i < 4; ++i) { const unsigned ai = au[i]; asm("v_dot2c_f32_bf16 %0, %1, %2" : "+v"(rs[m]) : "v"(ai), "v"(ones)); }
        }
      };
      BAR();
      STAMP(2); STAMPC(28);
      rd(XA, XB, 0, 0, 0); wait_lgkm0();
      int sa = 0, sb = 0;
      auto steps = [&](int n) {
#pragma unroll 1
        for (int i = 0; i < n; ++i) {
          rd(YA, YB, sa, sb, 1); mm(XA, XB); interleave<10, 7, 1>(); SCHED(); wait_lgkm0();
          BAR();
          sa = (sa + 1 == P1_SA) ? 0 : sa + 1; sb = (sb + 1 == P1_DB) ? 0 : sb + 1;
          rd(XA, XB, sa, sb, 0); mm(YA, YB); interleave<10, 7, 1>(); SCHED(); wait_lgkm0();
        }
      };
      steps(8);
      u64 fb[4];
#pragma unroll
      for (int i = 0; i < 4; ++i) fb[i] = __hip_atomic_load((GAS u64*)(ws + WS_FLAGB) + i * 64 + lane, __ATOMIC_RELAXED, __HIP_MEMORY_SCOPE_AGENT);
      steps(8);
      if (!__all(fb[0] == T && fb[1] == T && fb[2] == T && fb[3] == T)) poll_flags256(ws, WS_FLAGB, T, lane);
      CFENCE();
      f32x4 mp[2][4];
      const rsrc_t rm = mkrs(ws + WS_MODP, 4 * 3840 * 4);
      const int vsel = lane < 32 ? (w == 0 ? 1 : w == 1 ? 0 : w == 2 ? 2 : 4) : 5;
      const bool vact = (lane < 20) || (w == 0 && lane >= 32 && lane < 52);
#pragma unroll
      for (int k = 0; k < 4; ++k) mp[0][k] = __builtin_bit_cast(f32x4, ld16<SC1>(rm, vact ? (unsigned)((vsel * DM + 80 * j + (lane & 31) * 4) * 4) : OOB, k * 3840 * 4));
      steps(16);
      STAMP(3); STAMPC(29);
      {
        f32x4 s = (mp[0][0] + mp[0][1]) + (mp[0][2] + mp[0][3]);
        const bool plus1 = (vsel == 1) || (vsel == 4);
#pragma unroll
        for (int i = 0; i < 4; ++i) { s[i] = rbf(s[i]); if (plus1) s[i] = rbf(1.0f + s[i]); }
        const int trow = lane < 32 ? w : 4;
        if (vact) *(LAS f32x4*)(lds + L_TAB + (trow * 80 + (lane & 31) * 4) * 4) = s;
        wait_lgkm0(); CFENCE();
        if (lane == 0) __hip_atomic_fetch_add((LAS unsigned*)(lds + L_CNT) + 2, 1u, __ATOMIC_RELAXED, __HIP_MEMORY_SCOPE_WORKGROUP);
        for (unsigned spins = 0; spins < SPIN_MAX; ++spins) {
          if (__hip_atomic_load((LAS unsigned*)(lds + L_CNT) + 2, __ATOMIC_RELAXED, __HIP_MEMORY_SCOPE_WORKGROUP) >= 4u) break;
        }
        CFENCE();
      }
      rs[0] = xq_sum(rs[0]); rs[1] = -xq_sum(rs[1]);
      u32x2 pkd[2][5];
#pragma unroll
      for (int n = 0; n < 5; ++n) {
        const f32x4 c1 = *(LAS f32x4*)(lds + L_TAB + (0 * 80 + 16 * n + 4 * q) * 4), sa4 = *(LAS f32x4*)(lds + L_TAB + (1 * 80 + 16 * n + 4 * q) * 4);
        const f32x4 pv = acc[0][n] * c1 + rs[0] * sa4, qv = rs[1] * sa4 - acc[1][n] * c1;
        pkd[0][n] = (u32x2){pk2(pv[0], pv[1]), pk2(pv[2], pv[3])};
        pkd[1][n] = (u32x2){pk2(qv[0], qv[1]), pk2(qv[2], qv[3])};
      }
      const rsrc_t rpq = mkrs(ws + WS_PQ, 32 * 40 * 4 * 1024);
      auto pq_off = [&](int m, int nf) { const int c = 10 * j + 2 * nf + (q >> 1); return (unsigned)(((g * 40 + m * 20 + (c >> 2)) * 4 + w) * 1024 + ((c & 3) * 16 + l15) * 16); };
      const int odd = q & 1;
      st16<SC1>(pair_swap(pkd[0][0], pkd[0][1]), rpq, pq_off(0, odd), 0);
      st16<SC1>(pair_swap(pkd[0][2], pkd[0][3]), rpq, pq_off(0, 2 + odd), 0);
      st16<SC1>(pair_swap(pkd[1][0], pkd[1][1]), rpq, pq_off(1, odd), 0);
      st16<SC1>(pair_swap(pkd[1][2], pkd[1][3]), rpq, pq_off(1, 2 + odd), 0);
      st16<SC1>(pair_swap(pkd[0][4], pkd[1][4]), rpq, pq_off(odd, 4), 0);
      publish(lds, 3, 4, ws, WS_FLAG1 + g * 256 + j * 8, T, lane);
      STAMP(4);
    }

    {
      LANE_IDS();
      constexpr int NT = 20, W = 6;
      const rsrc_t rx = mkrs(p.x, SEQ * DM * 2);
      u32x2 xin[5];
#pragma unroll
      for (int n = 0; n < 5; ++n) xin[n] = ld8<0>(rx, (unsigned)((64 * g + 16 * w + l15) * (DM * 2) + (80 * j + 16 * n + 4 * q) * 2), 0);
      poll_flags(ws, WS_FLAG1 + g * 256, 8, T, lane);
      STAMP(5);
      const rsrc_t ra = mkrs(ws + WS_PQ + (size_t)g * 40 * 4 * 1024, 40 * 4 * 1024);
      u32x4 A[NT][2];
#pragma unroll
      for (int t = 0; t < W; ++t) { A[t][0] = ld16<SC1>(ra, lane * 16, ((2 * t) * 4 + w) * 1024); A[t][1] = ld16<SC1>(ra, lane * 16, ((2 * t + 1) * 4 + w) * 1024); }
      f32x4 acc[5];
#pragma unroll
      for (int n = 0; n < 5; ++n) acc[n] = (f32x4){0.f, 0.f, 0.f, 0.f};
      const unsigned boff0 = l15 * 128 + ((q ^ (l15 >> 1)) << 4), boff1 = boff0 ^ 64;
      bf16x8 XB[5], YB[5];
      auto rd = [&](bf16x8(&B)[5], int slot, int kk) {
        LAS char* pb = lds + slot * 10240 + (kk ? boff1 : boff0);
#pragma unroll
        for (int n = 0; n < 5; ++n) B[n] = ldsfrag(pb + n * 2048);
      };
      BAR();
      STAMP(6);
      rd(XB, 0, 0); wait_lgkm0();
#pragma unroll
      for (int t = 0; t < NT; ++t) {
        if (t + W < NT) { A[t + W][0] = ld16<SC1>(ra, lane * 16, ((2 * (t + W)) * 4 + w) * 1024); A[t + W][1] = ld16<SC1>(ra, lane * 16, ((2 * (t + W) + 1) * 4 + w) * 1024); }
        rd(YB, t % P2_RING, 1);
#pragma unroll
        for (int n = 0; n < 5; ++n) acc[n] = mfma(XB[n], asfrag(A[t][0]), acc[n]);
        interleave<5, 5, 1>(); SCHED(); wait_lgkm0();
        BAR();
        rd(XB, (t + 1) % P2_RING, 0);
#pragma unroll
        for (int n = 0; n < 5; ++n) acc[n] = mfma(YB[n], asfrag(A[t][1]), acc[n]);
        interleave<5, 5, 1>(); SCHED(); wait_lgkm0();
      }
      STAMP(7);
      float x1f[5][4]; float s = 0.f;
      u32x2 cp[5], x1p[5];
      const rsrc_t rx1 = mkrs(ws + WS_X1 + (size_t)b * 4 * 5 * 512, 4 * 5 * 512);
#pragma unroll
      for (int n = 0; n < 5; ++n) {
        const f32x4 ga = *(LAS f32x4*)(lds + L_TAB + (2 * 80 + 16 * n + 4 * q) * 4), cb1 = *(LAS f32x4*)(lds + L_TAB + (3 * 80 + 16 * n + 4 * q) * 4);
        const float x0 = bflo(xin[n].x), x1_ = bfhi(xin[n].x), x2 = bflo(xin[n].y), x3 = bfhi(xin[n].y);
        x1p[n] = (u32x2){pk2(x0 + ga[0] * acc[n][0], x1_ + ga[1] * acc[n][1]), pk2(x2 + ga[2] * acc[n][2], x3 + ga[3] * acc[n][3])};
        st8<0>(x1p[n], rx1, lane * 8, (w * 5 + n) * 512);
        x1f[n][0] = bflo(x1p[n].x); x1f[n][1] = bfhi(x1p[n].x); x1f[n][2] = bflo(x1p[n].y); x1f[n][3] = bfhi(x1p[n].y);
        cp[n] = (u32x2){pk2(x1f[n][0] * cb1[0], x1f[n][1] * cb1[1]), pk2(x1f[n][2] * cb1[2], x1f[n][3] * cb1[3])};
        s += (x1f[n][0] + x1f[n][1]) + (x1f[n][2] + x1f[n][3]);
      }
      const float mean = xq_sum(s) * (1.0f / 80.0f);
      float m2 = 0.f;
#pragma unroll
      for (int n = 0; n < 5; ++n)
#pragma unroll
        for (int r = 0; r < 4; ++r) { const float d = x1f[n][r] - mean; m2 += d * d; }
      m2 = xq_sum(m2);
      const rsrc_t rc = mkrs(ws + WS_X1C, 32 * 20 * 4 * 1024);
      auto c_off = [&](int nf) { const int c = 10 * j + 2 * nf + (q >> 1); return (unsigned)(((g * 20 + (c >> 2)) * 4 + w) * 1024 + ((c & 3) * 16 + l15) * 16); };
      const int odd = q & 1;
      st16<SC1>(pair_swap(cp[0], cp[1]), rc, c_off(odd), 0);
      st16<SC1>(pair_swap(cp[2], cp[3]), rc, c_off(2 + odd), 0);
      st8<SC1>(cp[4], rc, c_off(4) + odd * 8, 0);
      const rsrc_t rs1 = mkrs(ws + WS_ST1, SEQ * 8 * 8);
      st8<SC1>((u32x2){__float_as_uint(mean), __float_as_uint(m2)}, rs1, q == 0 ? (unsigned)(((64 * g + 16 * w + l15) * 8 + j) * 8) : OOB, 0);
      publish(lds, 4, 4, ws, WS_FLAG2 + g * 256 + j * 8, T, lane);
      STAMP(8);
    }

    {
      LANE_IDS();
      constexpr int NT = 10;
      {
        const rsrc_t ruv = mkrs(ws + WS_UV, 2 * 896 * 16);
        const int col = w * 56 + lane, part = col >= 112 ? 1 : 0, cc = col - 112 * part;
        const unsigned off = lane < 56 ? (unsigned)((part * 896 + 112 * j + cc) * 16) : OOB;
        u32x4 gv;
        for (unsigned spins = 0;; ++spins) {
          gv = ld16<SC1>(ruv, off, 0);
          const bool ok = lane >= 56 || (gv.z == (unsigned)T && gv.w == (unsigned)(T >> 32));
          if (__all(ok)) break;
          if (spins > SPIN_MAX) { note_timeout(ws, 0x300, lane); break; }
          __builtin_amdgcn_s_sleep(1); CFENCE();
        }
        if (lane < 56) { *(LAS unsigned*)(lds + L_UV + col * 4) = gv.x; *(LAS unsigned*)(lds + L_UV + (224 + col) * 4) = gv.y; }
      }
      poll_flags(ws, WS_FLAG2 + g * 256, 8, T, lane);
      STAMP(9);
      const rsrc_t ra = mkrs(ws + WS_X1C + (size_t)g * 20 * 4 * 1024, 20 * 4 * 1024);
      u32x4 A[20];
#pragma unroll
      for (int k = 0; k < 20; ++k) A[k] = ld16<SC1>(ra, lane * 16, (k * 4 + w) * 1024);
      const rsrc_t rs1 = mkrs(ws + WS_ST1, SEQ * 8 * 8);
      const u32x4 st = ld16<SC1>(rs1, (unsigned)(((64 * g + 16 * w + l15) * 8 + 2 * q) * 8), 0);
      f32x4 acc[7];
      bf16x8 XB[7], YB[7];
      auto rd = [&](bf16x8(&B)[7], int slot, int kk) {
        LAS char* pb = lds + slot * 14336 + kk * 7168 + lane * 16;
#pragma unroll
        for (int n = 0; n < 7; ++n) B[n] = ldsfrag(pb + n * 1024);
      };
      wait_lgkm0();
      BAR();
      STAMP(10);
      rd(XB, 0, 0); wait_lgkm0();
      float mean1, rstd1;
      {
        const float ma = __uint_as_float(st.x), mb = __uint_as_float(st.z);
        float m = 0.5f * (ma + mb), d = ma - mb, M2 = __uint_as_float(st.y) + __uint_as_float(st.w) + d * d * 40.0f;
        float mo = __shfl_xor(m, 16), Mo = __shfl_xor(M2, 16); d = m - mo; M2 = M2 + Mo + d * d * 80.0f; m = 0.5f * (m + mo);
        mo = __shfl_xor(m, 32); Mo = __shfl_xor(M2, 32); d = m - mo; M2 = M2 + Mo + d * d * 160.0f; m = 0.5f * (m + mo);
        mean1 = m; rstd1 = 1.0f / sqrtf(M2 * (1.0f / DM) + 1e-5f);
      }
#pragma unroll
      for (int n = 0; n < 7; ++n) acc[n] = (f32x4){0.f, 0.f, 0.f, 0.f};
#pragma unroll
      for (int t = 0; t < NT; ++t) {
        rd(YB, t % P3_RING, 1);
#pragma unroll
        for (int n = 0; n < 7; ++n) acc[n] = mfma(asfrag(A[2 * t]), XB[n], acc[n]);
        interleave<7, 7, 2>(); SCHED(); wait_lgkm0();
        BAR();
        rd(XB, (t + 1) % P3_RING, 0);
#pragma unroll
        for (int n = 0; n < 7; ++n) acc[n] = mfma(asfrag(A[2 * t + 1]), YB[n], acc[n]);
        interleave<7, 7, 2>(); SCHED(); wait_lgkm0();
      }
      u32x2 z2p[7];
      const int nval = (j == 7) ? 5 : 7;
      {
        float mr[4], rr[4];
#pragma unroll
        for (int r = 0; r < 4; ++r) { mr[r] = __shfl(mean1, 4 * q + r); rr[r] = __shfl(rstd1, 4 * q + r); }
        float zs[4] = {0.f, 0.f, 0.f, 0.f};
#pragma unroll
        for (int n = 0; n < 7; ++n) {
          const float u = *(LAS float*)(lds + L_UV + (16 * n + l15) * 4), v = *(LAS float*)(lds + L_UV + (224 + 16 * n + l15) * 4);
          float z[4];
#pragma unroll
          for (int r = 0; r < 4; ++r) z[r] = gelu_tanh(rr[r] * (acc[n][r] - mr[r] * u) + v);
          z2p[n] = (u32x2){pk2(z[0], z[1]), pk2(z[2], z[3])};
          if (n < nval) { zs[0] += bflo(z2p[n].x); zs[1] += bfhi(z2p[n].x); zs[2] += bflo(z2p[n].y); zs[3] += bfhi(z2p[n].y); }
        }
        const float inv = 1.0f / (16.0f * nval);
        float mz[4], M2[4];
#pragma unroll
        for (int r = 0; r < 4; ++r) { mz[r] = row16_sum(zs[r]) * inv; M2[r] = 0.f; }
#pragma unroll
        for (int n = 0; n < 7; ++n) if (n < nval) {
          const float d0 = bflo(z2p[n].x) - mz[0], d1 = bfhi(z2p[n].x) - mz[1], d2 = bflo(z2p[n].y) - mz[2], d3 = bfhi(z2p[n].y) - mz[3];
          M2[0] += d0 * d0; M2[1] += d1 * d1; M2[2] += d2 * d2; M2[3] += d3 * d3;
        }
#pragma unroll
        for (int r = 0; r < 4; ++r) M2[r] = row16_sum(M2[r]);
        const float ms = l15 == 0 ? mz[0] : l15 == 1 ? mz[1] : l15 == 2 ? mz[2] : mz[3];
        const float Ms = l15 == 0 ? M2[0] : l15 == 1 ? M2[1] : l15 == 2 ? M2[2] : M2[3];
        const rsrc_t rs2 = mkrs(ws + WS_ST2, SEQ * 8 * 16);
        st16<SC1>((u32x4){__float_as_uint(ms), __float_as_uint(Ms), (unsigned)T, (unsigned)(T >> 32)}, rs2,
                  l15 < 4 ? (unsigned)(((64 * g + 16 * w + 4 * q + l15) * 8 + j) * 16) : OOB, 0);
      }
      STAMP(11);
#pragma unroll
      for (int n = 0; n < 7; ++n) acc[n] = (f32x4){0.f, 0.f, 0.f, 0.f};
#pragma unroll
      for (int t = 0; t < NT; ++t) {
        rd(YB, (NT + t) % P3_RING, 1);
#pragma unroll
        for (int n = 0; n < 7; ++n) acc[n] = mfma(XB[n], asfrag(A[2 * t]), acc[n]);
        interleave<7, 7, 2>(); SCHED(); wait_lgkm0();
        BAR();
        rd(XB, (NT + t + 1) % P3_RING, 0);
#pragma unroll
        for (int n = 0; n < 7; ++n) acc[n] = mfma(YB[n], asfrag(A[2 * t + 1]), acc[n]);
        interleave<7, 7, 2>(); SCHED(); wait_lgkm0();
      }
      STAMP(12);
      const rsrc_t rs2 = mkrs(ws + WS_ST2, SEQ * 8 * 16);
      u32x4 ga_, gb_;
      const unsigned soff = (unsigned)(((64 * g + 16 * w + l15) * 8 + 2 * q) * 16);
      ga_ = ld16<SC1>(rs2, soff, 0); gb_ = ld16<SC1>(rs2, soff + 16, 0);
      {
        const rsrc_t rz = mkrs(ws + WS_Z1 + (size_t)b * 4 * 7 * 512, 4 * 7 * 512);
#pragma unroll
        for (int n = 0; n < 7; ++n) {
          const f32x4 u = *(LAS f32x4*)(lds + L_UV + (112 + 16 * n + 4 * q) * 4), v = *(LAS f32x4*)(lds + L_UV + (224 + 112 + 16 * n + 4 * q) * 4);
          float z[4];
#pragma unroll
          for (int r = 0; r < 4; ++r) z[r] = gelu_tanh(rstd1 * (acc[n][r] - mean1 * u[r]) + v[r]);
          st8<0>((u32x2){pk2(z[0], z[1]), pk2(z[2], z[3])}, rz, lane * 8, (w * 7 + n) * 512);
        }
      }
      for (unsigned spins = 0;; ++spins) {
        const bool ok = ga_.z == (unsigned)T && ga_.w == (unsigned)(T >> 32) && gb_.z == (unsigned)T && gb_.w == (unsigned)(T >> 32);
        if (__all(ok)) break;
        if (spins > SPIN_MAX) { note_timeout(ws, 0x400, lane); break; }
        __builtin_amdgcn_s_sleep(1); CFENCE();
        ga_ = ld16<SC1>(rs2, soff, 0); gb_ = ld16<SC1>(rs2, soff + 16, 0);
      }
      float mean2, rstd2;
      {
        const float na = 112.0f, nb = (q == 3) ? 80.0f : 112.0f;
        float ma = __uint_as_float(ga_.x), mb = __uint_as_float(gb_.x);
        float n = na + nb, d = mb - ma, m = ma + d * (nb / n), M2 = __uint_as_float(ga_.y) + __uint_as_float(gb_.y) + d * d * (na * nb / n);
#pragma unroll
        for (int sft = 16; sft <= 32; sft <<= 1) {
          const float no = __shfl_xor(n, sft), mo = __shfl_xor(m, sft), Mo = __shfl_xor(M2, sft);
          const float nt = n + no; d = mo - m;
          M2 = M2 + Mo + d * d * (n * no / nt);
          m = (n * m + no * mo) / nt; n = nt;
        }
        mean2 = m; rstd2 = 1.0f / sqrtf(M2 * (1.0f / FH) + 1e-5f);
      }
      {
        float mr[4], rr[4];
#pragma unroll
        for (int r = 0; r < 4; ++r) { mr[r] = __shfl(mean2, 4 * q + r); rr[r] = __shfl(rstd2, 4 * q + r); }
        u32x2 zn[7];
#pragma unroll
        for (int n = 0; n < 7; ++n)
          zn[n] = (u32x2){pk2((bflo(z2p[n].x) - mr[0]) * rr[0], (bfhi(z2p[n].x) - mr[1]) * rr[1]), pk2((bflo(z2p[n].y) - mr[2]) * rr[2], (bfhi(z2p[n].y) - mr[3]) * rr[3])};
        const rsrc_t rz2 = mkrs(ws + WS_Z2T, 8 * 32 * 14336);
        auto z_off = [&](int nf) { return (unsigned)(((j * 32 + g) * 14 + (w >> 1) * 7 + nf) * 1024 + ((2 * (w & 1) + (q >> 1)) * 16 + l15) * 16); };
        const int odd = q & 1;
        st16<SC1>(pair_swap(zn[0], zn[1]), rz2, z_off(odd), 0);
        st16<SC1>(pair_swap(zn[2], zn[3]), rz2, z_off(2 + odd), 0);
        st16<SC1>(pair_swap(zn[4], zn[5]), rz2, z_off(4 + odd), 0);
        st8<SC1>(zn[6], rz2, z_off(6) + odd * 8, 0);
      }
      publish(lds, 5, 4, ws, WS_FLAG3 + j * 256 + g * 8, T, lane);
      STAMP(13);
    }

    {
      LANE_IDS();
      const int mh = w >> 1, nh = w & 1;
      f32x4 acc[2][4];
#pragma unroll
      for (int m = 0; m < 2; ++m)
#pragma unroll
        for (int n = 0; n < 4; ++n) acc[m][n] = (f32x4){0.f, 0.f, 0.f, 0.f};
      const unsigned aoff0 = (mh * 32 + l15) * 128 + ((q ^ (l15 >> 1)) << 4), aoff1 = aoff0 ^ 64;
      const unsigned boff = P4_DOFF + nh * 4096 + lane * 16;
      const rsrc_t rb = mkrs(p.bsp, SEQ * 2);
      const float bs0 = bflo(ld2<0>(rb, (64 * g + 32 * mh + l15) * 2, 0)), bs1 = bflo(ld2<0>(rb, (64 * g + 32 * mh + 16 + l15) * 2, 0));
      bf16x8 XA[2], XB[4], YA[2], YB[4];
      if (nh == 0) {
        auto rd = [&](bf16x8(&A)[2], bf16x8(&B)[4], int sa, int sb, int kk) {
          LAS char* pa = lds + sa * 8192 + (kk ? aoff1 : aoff0);
          LAS char* pb = lds + sb * 14336 + boff + kk * 7168;
#pragma unroll
          for (int n = 0; n < 4; ++n) B[n] = ldsfrag(pb + n * 1024);
          A[0] = ldsfrag(pa); A[1] = ldsfrag(pa + 2048);
        };
        auto mm = [&](const bf16x8(&A)[2], const bf16x8(&B)[4]) {
#pragma unroll
          for (int m = 0; m < 2; ++m)
#pragma unroll
            for (int n = 0; n < 4; ++n) acc[m][n] = mfma(B[n], A[m], acc[m][n]);
        };
        BAR();
        STAMP(14);
        rd(XA, XB, 0, 0, 0); wait_lgkm0();
        int sa = 0, sb = 0;
#pragma unroll 1
        for (int i = 0; i < 32; ++i) {
          rd(YA, YB, sa, sb, 1); mm(XA, XB); interleave<8, 6, 1>(); SCHED(); wait_lgkm0();
          BAR();
          sa = (sa + 1 == P4_SA) ? 0 : sa + 1; sb = (sb + 1 == P4_DB) ? 0 : sb + 1;
          rd(XA, XB, sa, sb, 0); mm(YA, YB); interleave<8, 6, 1>(); SCHED(); wait_lgkm0();
        }
      } else {
        auto rd = [&](bf16x8(&A)[2], bf16x8(&B)[4], int sa, int sb, int kk) {
          LAS char* pa = lds + sa * 8192 + (kk ? aoff1 : aoff0);
          LAS char* pb = lds + sb * 14336 + boff + kk * 7168;
#pragma unroll
          for (int n = 0; n < 3; ++n) B[n] = ldsfrag(pb + n * 1024);
          A[0] = ldsfrag(pa); A[1] = ldsfrag(pa + 2048);
        };
        auto mm = [&](const bf16x8(&A)[2], const bf16x8(&B)[4]) {
#pragma unroll
          for (int m = 0; m < 2; ++m)
#pragma unroll
            for (int n = 0; n < 3; ++n) acc[m][n] = mfma(B[n], A[m], acc[m][n]);
        };
        BAR();
        STAMP(14);
        rd(XA, XB, 0, 0, 0); wait_lgkm0();
        int sa = 0, sb = 0;
#pragma unroll 1
        for (int i = 0; i < 32; ++i) {
          rd(YA, YB, sa, sb, 1); mm(XA, XB); interleave<6, 5, 1>(); SCHED(); wait_lgkm0();
          BAR();
          sa = (sa + 1 == P4_SA) ? 0 : sa + 1; sb = (sb + 1 == P4_DB) ? 0 : sb + 1;
          rd(XA, XB, sa, sb, 0); mm(YA, YB); interleave<6, 5, 1>(); SCHED(); wait_lgkm0();
        }
      }
      STAMP(15);
      const rsrc_t rz = mkrs(ws + WS_Z1 + (size_t)b * 4 * 7 * 512, 4 * 7 * 512);
      u32x2 up[2][4];
#pragma unroll
      for (int m = 0; m < 2; ++m)
#pragma unroll
        for (int n = 0; n < 4; ++n) {
          const int nf = 4 * nh + n;
          const u32x2 z = ld8<SC1>(rz, nf < 7 ? (unsigned)(lane * 8 + ((2 * mh + m) * 7 + nf) * 512) : OOB, 0);
          const float bs = m ? bs1 : bs0;
          up[m][n] = (u32x2){pk2(bflo(z.x) * rbf(rbf(acc[m][n][0]) + bs), bfhi(z.x) * rbf(rbf(acc[m][n][1]) + bs)),
                             pk2(bflo(z.y) * rbf(rbf(acc[m][n][2]) + bs), bfhi(z.y) * rbf(rbf(acc[m][n][3]) + bs))};
        }
      const rsrc_t ru = mkrs(ws + WS_U, 32 * 28 * 4 * 1024);
      auto u_off = [&](int mf, int nf) { const int c = 14 * j + 2 * nf + (q >> 1); return (unsigned)(((g * 28 + (c >> 2)) * 4 + mf) * 1024 + ((c & 3) * 16 + l15) * 16); };
      const int odd = q & 1;
      if (nh == 0) {
#pragma unroll
        for (int m = 0; m < 2; ++m) {
          st16<SC1>(pair_swap(up[m][0], up[m][1]), ru, u_off(2 * mh + m, odd), 0);
          st16<SC1>(pair_swap(up[m][2], up[m][3]), ru, u_off(2 * mh + m, 2 + odd), 0);
        }
      } else {
#pragma unroll
        for (int m = 0; m < 2; ++m) st16<SC1>(pair_swap(up[m][0], up[m][1]), ru, u_off(2 * mh + m, 4 + odd), 0);
        st16<SC1>(pair_swap(up[0][2], up[1][2]), ru, u_off(2 * mh + odd, 6), 0);
      }
      publish(lds, 6, 4, ws, WS_FLAG4 + g * 256 + j * 8, T, lane);
      if (b == 0 && w == 0 && lane == 0) __hip_atomic_store((GAS u64*)(ws + WS_EPOCH), T, __ATOMIC_RELAXED, __HIP_MEMORY_SCOPE_AGENT);
      STAMP(16);
    }

    {
      LANE_IDS();
      constexpr int NT = 14, W = 7;
      const rsrc_t rx1 = mkrs(ws + WS_X1 + (size_t)b * 4 * 5 * 512, 4 * 5 * 512);
      u32x2 x1p[5];
#pragma unroll
      for (int n = 0; n < 5; ++n) x1p[n] = ld8<SC1>(rx1, lane * 8, (w * 5 + n) * 512);
      poll_flags(ws, WS_FLAG4 + g * 256, 8, T, lane);
      STAMP(17);
      const rsrc_t ra = mkrs(ws + WS_U + (size_t)g * 28 * 4 * 1024, 28 * 4 * 1024);
      u32x4 A[NT][2];
#pragma unroll
      for (int t = 0; t < W; ++t) { A[t][0] = ld16<SC1>(ra, lane * 16, ((2 * t) * 4 + w) * 1024); A[t][1] = ld16<SC1>(ra, lane * 16, ((2 * t + 1) * 4 + w) * 1024); }
      f32x4 acc[5];
#pragma unroll
      for (int n = 0; n < 5; ++n) acc[n] = (f32x4){0.f, 0.f, 0.f, 0.f};
      bf16x8 XB[5], YB[5];
      auto rd = [&](bf16x8(&B)[5], int slot, int kk) {
        LAS char* pb = lds + slot * 10240 + kk * 5120 + lane * 16;
#pragma unroll
        for (int n = 0; n < 5; ++n) B[n] = ldsfrag(pb + n * 1024);
      };
      BAR();
      STAMP(18);
      rd(XB, 0, 0); wait_lgkm0();
#pragma unroll
      for (int t = 0; t < NT; ++t) {
        if (t + W < NT) { A[t + W][0] = ld16<SC1>(ra, lane * 16, ((2 * (t + W)) * 4 + w) * 1024); A[t + W][1] = ld16<SC1>(ra, lane * 16, ((2 * (t + W) + 1) * 4 + w) * 1024); }
        rd(YB, t, 1);
#pragma unroll
        for (int n = 0; n < 5; ++n) acc[n] = mfma(XB[n], asfrag(A[t][0]), acc[n]);
        interleave<5, 5, 1>(); SCHED(); wait_lgkm0();
        BAR();
        rd(XB, t + 1 < NT ? t + 1 : 0, 0);
#pragma unroll
        for (int n = 0; n < 5; ++n) acc[n] = mfma(YB[n], asfrag(A[t][1]), acc[n]);
        interleave<5, 5, 1>(); SCHED(); wait_lgkm0();
      }
      STAMP(19);
      u32x2 op[5];
#pragma unroll
      for (int n = 0; n < 5; ++n) {
        const f32x4 gb = *(LAS f32x4*)(lds + L_TAB + (4 * 80 + 16 * n + 4 * q) * 4);
        op[n] = (u32x2){pk2(bflo(x1p[n].x) + rbf(gb[0] * rbf(acc[n][0])), bfhi(x1p[n].x) + rbf(gb[1] * rbf(acc[n][1]))),
                        pk2(bflo(x1p[n].y) + rbf(gb[2] * rbf(acc[n][2])), bfhi(x1p[n].y) + rbf(gb[3] * rbf(acc[n][3])))};
      }
      const rsrc_t ro = mkrs(p.out, SEQ * DM * 2);
      const unsigned rowb = (unsigned)((64 * g + 16 * w + l15) * (DM * 2) + 80 * j * 2);
      const int odd = q & 1;
      st16<0>(pair_swap(op[0], op[1]), ro, rowb + (16 * odd + 8 * (q >> 1)) * 2, 0);
      st16<0>(pair_swap(op[2], op[3]), ro, rowb + (16 * (2 + odd) + 8 * (q >> 1)) * 2, 0);
      st8<0>(op[4], ro, rowb + (64 + 4 * q) * 2, 0);
      STAMP(20); STAMPC(31);
    }
  } else if (wid < 6) {
    const int h = wid - 4;
    const unsigned sw0 = (unsigned)(((lane & 7) ^ (lane >> 4)) * 16), sw1 = sw0 ^ 64;
    wait_lgkm0();
    BAR();
    {
      const rsrc_t r = mkrs((h ? p.wsim : p.wsre) + (size_t)g * 64 * SEQ, 64 * SEQ * 2);
      const unsigned v0 = (lane >> 3) * 4096 + sw0, v1 = (lane >> 3) * 4096 + sw1;
      auto issue = [&](int t, int slot) {
        const unsigned k = (unsigned)((t + rot) & 31) * 128;
#pragma unroll
        for (int pc = 0; pc < 8; ++pc) dma16<0>(r, lds + slot * 16384 + (h * 8 + pc) * 1024, (pc & 1) ? v1 : v0, k + pc * 32768);
      };
      loader_prefill<32, P1_SA, 8>(issue);
      STAMP(1);
      loader_run<32, P1_SA, 8>(issue);
    }
    {
      const unsigned v0 = (lane >> 3) * 1280 + (h ? sw1 : sw0), v1 = (lane >> 3) * 1280 + (h ? sw0 : sw1);
      auto issue = [&](int t, int slot) {
        const int im = t >= 10;
        const rsrc_t r = mkrs((im ? p.wdim : p.wdre) + (size_t)j * 80 * DM, 80 * DM * 2);
        const unsigned k = (unsigned)(t - 10 * im) * 128 + h * 5 * 8 * 1280;
#pragma unroll
        for (int pc = 0; pc < 5; ++pc) dma16<0>(r, lds + slot * 10240 + (h * 5 + pc) * 1024, (pc & 1) ? v1 : v0, k + pc * 8 * 1280);
      };
      loader_prefill<20, P2_RING, 5>(issue);
      STAMP(2);
      loader_run<20, P2_RING, 5>(issue);
    }
    {
      const rsrc_t r = mkrs(ws + WS_WINR + (size_t)j * 20 * 14336, 20 * 14336);
      auto issue = [&](int t, int slot) {
#pragma unroll
        for (int pc = 0; pc < 7; ++pc) dma16<SC1>(r, lds + slot * 14336 + (h * 7 + pc) * 1024, lane * 16, (unsigned)(t * 14336 + (h * 7 + pc) * 1024));
      };
      loader_prefill<20, P3_RING, 7>(issue);
      STAMP(3);
      loader_run<20, P3_RING, 7>(issue);
    }
    {
      const rsrc_t r = mkrs(p.wsp + (size_t)g * 64 * SEQ, 64 * SEQ * 2);
      const unsigned v0 = (lane >> 3) * 4096 + sw0, v1 = (lane >> 3) * 4096 + sw1;
      auto issue = [&](int t, int slot) {
        const unsigned k = (unsigned)((t + rot) & 31) * 128 + h * 4 * 32768;
#pragma unroll
        for (int pc = 0; pc < 4; ++pc) dma16<0>(r, lds + slot * 8192 + (h * 4 + pc) * 1024, (pc & 1) ? v1 : v0, k + pc * 32768);
      };
      loader_prefill<32, P4_SA, 4>(issue);
      STAMP(4);
      loader_run<32, P4_SA, 4>(issue);
    }
    {
      const rsrc_t r = mkrs(ws + WS_WOUTR + (size_t)j * 14 * 10240, 14 * 10240);
      auto issue = [&](int t, int slot) {
#pragma unroll
        for (int pc = 0; pc < 5; ++pc) dma16<SC1>(r, lds + slot * 10240 + (h * 5 + pc) * 1024, lane * 16, (unsigned)(t * 10240 + (h * 5 + pc) * 1024));
      };
      loader_prefill<14, P5_RING, 5>(issue);
      STAMP(5);
      loader_run<14, P5_RING, 5>(issue);
    }
  } else {
    const int h = wid - 6;
    wait_lgkm0();
    BAR();
    if (h == 0 && b < 240) {
      const int cg = b % 60, kq = b / 60;
      const rsrc_t rw = mkrs(p.wmod, DM * 3840 * 2), rc = mkrs(p.c, DM * 2);
      u32x4 wv[20]; unsigned cv[20];
#pragma unroll
      for (int it = 0; it < 20; ++it) {
        const int row = kq * 160 + it * 8 + (lane >> 3);
        wv[it] = ld16<0>(rw, (unsigned)(row * 7680 + cg * 128 + (lane & 7) * 16), 0);
        cv[it] = ld2<0>(rc, row * 2, 0);
      }
      float a8[8] = {0.f, 0.f, 0.f, 0.f, 0.f, 0.f, 0.f, 0.f};
#pragma unroll
      for (int it = 0; it < 20; ++it) {
        const float cf = bflo(cv[it]);
        const float sl = rbf(cf * __builtin_amdgcn_rcpf(1.0f + __builtin_amdgcn_exp2f(-1.4426950408889634f * cf)));
#pragma unroll
        for (int i = 0; i < 4; ++i) { a8[2 * i] += sl * bflo(wv[it][i]); a8[2 * i + 1] += sl * bfhi(wv[it][i]); }
      }
#pragma unroll
      for (int i = 0; i < 8; ++i) { a8[i] += __shfl_xor(a8[i], 8); a8[i] += __shfl_xor(a8[i], 16); a8[i] += __shfl_xor(a8[i], 32); }
      const rsrc_t rm = mkrs(ws + WS_MODP, 4 * 3840 * 4);
      const unsigned mo = (unsigned)((kq * 3840 + cg * 64 + (lane & 7) * 8) * 4);
      st16<SC1>((u32x4){__float_as_uint(a8[0]), __float_as_uint(a8[1]), __float_as_uint(a8[2]), __float_as_uint(a8[3])}, rm, lane < 8 ? mo : OOB, 0);
      st16<SC1>((u32x4){__float_as_uint(a8[4]), __float_as_uint(a8[5]), __float_as_uint(a8[6]), __float_as_uint(a8[7])}, rm, lane < 8 ? mo + 16 : OOB, 0);
    }
    {
      LAS char* sc = lds + L_RT + h * 7168;
#pragma unroll 1
      for (int rep = 0; rep < 2; ++rep) {
        int u;
        if (h == 1) u = b + 256 * rep; else u = (rep == 0 && b < 32) ? 512 + b : 1000;
        if (u >= 544) continue;
        const int qq = l15 >> 2, pp = l15 & 3;
        if (u < 320) {
          const int jp = u / 20, kf = u - 20 * jp, jj = jp >> 1, part = jp & 1;
          const rsrc_t r = mkrs(p.win, DM * FF * 2);
#pragma unroll
          for (int pc = 0; pc < 7; ++pc) {
            const int idx = pc * 64 + lane, row = idx / 14, ch = idx - 14 * row;
            const bool ok = 112 * jj + 8 * ch < FH;
            dma16<0>(r, sc + pc * 1024, ok ? (unsigned)((32 * kf + row) * (FF * 2) + (part ? 0 : FH * 2) + 224 * jj + ch * 16) : OOB, 0);
          }
          wait_vm<0>(); CFENCE();
          const rsrc_t ro = mkrs(ws + WS_WINR, 8 * 20 * 14336);
#pragma unroll
          for (int nf = 0; nf < 7; ++nf) {
            LAS char* a0 = sc + (8 * q + qq) * 224 + (16 * nf + 4 * pp) * 2;
            const u32x2 lo = __builtin_bit_cast(u32x2, __builtin_amdgcn_ds_read_tr16_b64_v4i16((LAS s16x4*)a0));
            const u32x2 hi = __builtin_bit_cast(u32x2, __builtin_amdgcn_ds_read_tr16_b64_v4i16((LAS s16x4*)(a0 + 4 * 224)));
            st16<SC1>((u32x4){lo.x, lo.y, hi.x, hi.y}, ro, (unsigned)((((jj * 2 + part) * 20 + kf) * 7 + nf) * 1024 + lane * 16), 0);
          }
        } else {
          const int uu = u - 320, jj = uu / 28, kf = uu - 28 * jj;
          const rsrc_t r = mkrs(p.wout, FH * DM * 2);
#pragma unroll
          for (int pc = 0; pc < 5; ++pc) {
            const int idx = pc * 64 + lane, row = idx / 10, ch = idx - 10 * row;
            dma16<0>(r, sc + pc * 1024, (unsigned)((32 * kf + row) * (DM * 2) + 160 * jj + ch * 16), 0);
          }
          wait_vm<0>(); CFENCE();
          const rsrc_t ro = mkrs(ws + WS_WOUTR, 8 * 14 * 10240);
#pragma unroll
          for (int nf = 0; nf < 5; ++nf) {
            LAS char* a0 = sc + (8 * q + qq) * 160 + (16 * nf + 4 * pp) * 2;
            const u32x2 lo = __builtin_bit_cast(u32x2, __builtin_amdgcn_ds_read_tr16_b64_v4i16((LAS s16x4*)a0));
            const u32x2 hi = __builtin_bit_cast(u32x2, __builtin_amdgcn_ds_read_tr16_b64_v4i16((LAS s16x4*)(a0 + 4 * 160)));
            st16<SC1>((u32x4){lo.x, lo.y, hi.x, hi.y}, ro, (unsigned)(((jj * 28 + kf) * 5 + nf) * 1024 + lane * 16), 0);
          }
        }
        wait_lgkm0(); CFENCE();
      }
    }
    const u64 T = (((u64)(unsigned)__builtin_amdgcn_readfirstlane((unsigned)(Ein >> 32)) << 32) | (unsigned)__builtin_amdgcn_readfirstlane((unsigned)Ein)) + 1;
    publish(lds, 7, 2, ws, WS_FLAGB + (g * 8 + j) * 8, T, lane);
    STAMP(1);
    {
      poll_flags256(ws, WS_FLAG0, T, lane);
      STAMP(2);
      const rsrc_t r = mkrs(ws + WS_XNT + (size_t)j * 32 * 10240, 32 * 10240);
      auto issue = [&](int t, int slot) {
        const unsigned k = (unsigned)((t + rot) & 31) * 10240;
#pragma unroll
        for (int pc = 0; pc < 5; ++pc) dma16<SC1>(r, lds + P1_DOFF + slot * 10240 + (h * 5 + pc) * 1024, lane * 16, k + (h * 5 + pc) * 1024);
      };
      loader_prefill<32, P1_DB, 5>(issue);
      loader_run<32, P1_DB, 5>(issue);
    }
    if (h == 0 && b < 112) {
      const int jj = b / 14, pn = b - 14 * jj, part = pn / 7, nf = pn - 7 * part;
      const rsrc_t rw = mkrs(ws + WS_WINR, 8 * 20 * 14336), rm = mkrs(ws + WS_MODP, 4 * 3840 * 4);
      u32x4 wf[20];
#pragma unroll
      for (int k = 0; k < 20; ++k) wf[k] = ld16<SC1>(rw, lane * 16, (unsigned)((((jj * 2 + part) * 20 + k) * 7 + nf) * 1024));
#pragma unroll
      for (int it = 0; it < 5; ++it) {
        const int c4 = (it * 64 + lane) * 4, isS = c4 >= DM, cc = c4 - DM * isS;
        const unsigned o = (unsigned)(((isS ? 3 : 4) * DM + cc) * 4);
        const f32x4 m0 = __builtin_bit_cast(f32x4, ld16<SC1>(rm, o, 0)), m1 = __builtin_bit_cast(f32x4, ld16<SC1>(rm, o, 3840 * 4)),
                    m2 = __builtin_bit_cast(f32x4, ld16<SC1>(rm, o, 2 * 3840 * 4)), m3 = __builtin_bit_cast(f32x4, ld16<SC1>(rm, o, 3 * 3840 * 4));
        f32x4 s = (m0 + m1) + (m2 + m3);
#pragma unroll
        for (int i = 0; i < 4; ++i) { s[i] = rbf(s[i]); if (!isS) s[i] = 1.0f + s[i]; }
        *(LAS u32x2*)(lds + L_TABE + (isS * DM + cc) * 2) = (u32x2){pk2(s[0], s[1]), pk2(s[2], s[3])};
      }
      wait_lgkm0(); CFENCE();
      f32x4 acc = {0.f, 0.f, 0.f, 0.f};
#pragma unroll
      for (int k = 0; k < 20; ++k) {
        u32x4 e = *(LAS u32x4*)(lds + L_TABE + ((l15 & 1) * DM + 32 * k + 8 * q) * 2);
        if (l15 >= 2) e = (u32x4){0u, 0u, 0u, 0u};
        acc = mfma(asfrag(e), asfrag(wf[k]), acc);
      }
      const rsrc_t ruv = mkrs(ws + WS_UV, 2 * 896 * 16);
      st16<SC1>((u32x4){__float_as_uint(acc[0]), __float_as_uint(acc[1]), (unsigned)T, (unsigned)(T >> 32)}, ruv,
                q == 0 ? (unsigned)((part * 896 + 112 * jj + 16 * nf + l15) * 16) : OOB, 0);
    }
    STAMP(3);
    idle_phase<21>();
    idle_phase<21>();
    {
      poll_flags(ws, WS_FLAG3 + j * 256, 32, T, lane);
      STAMP(4);
      const rsrc_t r = mkrs(ws + WS_Z2T + (size_t)j * 32 * 14336, 32 * 14336);
      auto issue = [&](int t, int slot) {
        const unsigned k = (unsigned)((t + rot) & 31) * 14336;
#pragma unroll
        for (int pc = 0; pc < 7; ++pc) dma16<SC1>(r, lds + P4_DOFF + slot * 14336 + (h * 7 + pc) * 1024, lane * 16, k + (h * 7 + pc) * 1024);
      };
      loader_prefill<32, P4_DB, 7>(issue);
      loader_run<32, P4_DB, 7>(issue);
    }
    idle_phase<15>();
  }
}

extern "C" void kernel_launch(void* const* d_in, const int* in_sizes, int n_in, void* d_out, int out_size, void* d_ws, size_t ws_size,
                              hipStream_t stream) {
  Params p;
  p.x = (const u16*)d_in[0]; p.wsre = (const u16*)d_in[1]; p.wsim = (const u16*)d_in[2]; p.wdre = (const u16*)d_in[3]; p.wdim = (const u16*)d_in[4];
  p.win = (const u16*)d_in[6]; p.wsp = (const u16*)d_in[7]; p.bsp = (const u16*)d_in[8]; p.wout = (const u16*)d_in[9];
  p.c = (const u16*)d_in[10]; p.wmod = (const u16*)d_in[11];
  p.out = (u16*)d_out; p.ws = (char*)d_ws;
  (void)hipFuncSetAttribute((const void*)adaLN_cond__spectral_fnet__mixer_gmlp__prompt_kernel, hipFuncAttributeMaxDynamicSharedMemorySize, LDS_BYTES);
  adaLN_cond__spectral_fnet__mixer_gmlp__prompt_kernel<<<256, 512, LDS_BYTES, stream>>>(p);
}
```

```cpp
#define STAMPS
#include <hip/hip_runtime.h>
#include <hip/hip_bf16.h>

typedef unsigned short u16;
typedef unsigned long long u64;
typedef short bf16x8 __attribute__((ext_vector_type(8)));
typedef short s16x4 __attribute__((ext_vector_type(4)));
typedef __bf16 bf16x2 __attribute__((ext_vector_type(2)));
typedef float f32x2 __attribute__((ext_vector_type(2)));
typedef float f32x4 __attribute__((ext_vector_type(4)));
typedef unsigned u32x2 __attribute__((ext_vector_type(2)));
typedef unsigned u32x4 __attribute__((ext_vector_type(4)));
typedef __amdgpu_buffer_rsrc_t rsrc_t;
#define LAS __attribute__((address_space(3)))
#define GAS __attribute__((address_space(1)))
#define DEVI __device__ __forceinline__

constexpr int SEQ = 2048, DM = 640, FF = 1728, FH = 864;

constexpr unsigned WS_EPOCH = 0x0;
constexpr unsigned WS_TMO   = 0x100;
constexpr unsigned WS_FLAG0 = 0x1000;
constexpr unsigned WS_FLAGB = 0x2000;
constexpr unsigned WS_FLAG1 = 0x4000;
constexpr unsigned WS_FLAG2 = 0x6000;
constexpr unsigned WS_FLAG4 = 0x8000;
constexpr unsigned WS_FLAG3 = 0xA000;
constexpr unsigned WS_MODP  = 0x10000;
constexpr unsigned WS_UV    = 0x20000;
constexpr unsigned WS_ST1   = 0x30000;
constexpr unsigned WS_ST2   = 0x50000;
constexpr unsigned WS_XNT   = 0x100000;
constexpr unsigned WS_PQ    = 0x400000;
constexpr unsigned WS_X1C   = 0xA00000;
constexpr unsigned WS_Z2T   = 0xD00000;
constexpr unsigned WS_U     = 0x1100000;
constexpr unsigned WS_WINR  = 0x1500000;
constexpr unsigned WS_WOUTR = 0x1800000;
constexpr unsigned WS_Z1    = 0x1A00000;
constexpr unsigned WS_X1    = 0x2000000;
constexpr unsigned WS_STAMP = 0x2400000;
constexpr unsigned WS_USED  = 0x2500000;

constexpr int LDS_BYTES = 163840;
constexpr int L_MISC = 155648;
constexpr int L_CNT  = L_MISC;
constexpr int L_TAB  = L_MISC + 256;
constexpr int L_TABE = L_MISC + 2048;
constexpr int L_UV   = L_MISC + 4608;
constexpr int L_P0T  = 114688;
constexpr int L_RT   = 114688 + 12288;
constexpr int P0T_RS = 1296;

constexpr unsigned SPIN_MAX = 1u << 17;

struct Params {
  const u16 *x, *wsre, *wsim, *wdre, *wdim, *win, *wsp, *bsp, *wout, *c, *wmod;
  u16* out;
  char* ws;
};

DEVI rsrc_t mkrs(const void* p, unsigned bytes) { return __builtin_amdgcn_make_buffer_rsrc((void*)p, 0, (int)bytes, 0x00020000); }
template <int AUX> DEVI u32x4 ld16(rsrc_t r, unsigned v, unsigned s) { return __builtin_amdgcn_raw_buffer_load_b128(r, (int)v, (int)s, AUX); }
template <int AUX> DEVI u32x2 ld8(rsrc_t r, unsigned v, unsigned s) { return __builtin_amdgcn_raw_buffer_load_b64(r, (int)v, (int)s, AUX); }
template <int AUX> DEVI unsigned ld4(rsrc_t r, unsigned v, unsigned s) { return __builtin_amdgcn_raw_buffer_load_b32(r, (int)v, (int)s, AUX); }
template <int AUX> DEVI unsigned ld2(rsrc_t r, unsigned v, unsigned s) { return (unsigned)__builtin_amdgcn_raw_buffer_load_b16(r, (int)v, (int)s, AUX) & 0xffffu; }
template <int AUX> DEVI void st16(u32x4 d, rsrc_t r, unsigned v, unsigned s) { __builtin_amdgcn_raw_buffer_store_b128(d, r, (int)v, (int)s, AUX); }
template <int AUX> DEVI void st8(u32x2 d, rsrc_t r, unsigned v, unsigned s) { __builtin_amdgcn_raw_buffer_store_b64(d, r, (int)v, (int)s, AUX); }
template <int AUX> DEVI void dma16(rsrc_t r, LAS char* dst, unsigned v, unsigned s) { __builtin_amdgcn_raw_ptr_buffer_load_lds(r, (LAS void*)dst, 16, (int)v, (int)s, 0, AUX); }
constexpr int SC1 = 16;
constexpr unsigned OOB = 0x7ffffff0u;

template <int N> DEVI void wait_vm() { asm volatile("s_waitcnt vmcnt(%0)" ::"n"(N > 63 ? 63 : N) : "memory"); }
DEVI void wait_lgkm0() { __builtin_amdgcn_s_waitcnt(0xC07F); }
DEVI void BAR() { asm volatile("s_barrier" ::: "memory"); }
DEVI void CFENCE() { asm volatile("" ::: "memory"); }
#define SCHED() __builtin_amdgcn_sched_barrier(0)

DEVI float bflo(unsigned v) { return __uint_as_float(v << 16); }
DEVI float bfhi(unsigned v) { return __uint_as_float(v & 0xffff0000u); }
DEVI unsigned pk2(float lo, float hi) { f32x2 f = {lo, hi}; return __builtin_bit_cast(unsigned, __builtin_convertvector(f, bf16x2)); }
DEVI float rbf(float v) { return bflo(pk2(v, 0.f)); }
DEVI f32x4 mfma(bf16x8 a, bf16x8 b, f32x4 c) { return __builtin_amdgcn_mfma_f32_16x16x32_bf16(a, b, c, 0, 0, 0); }
DEVI bf16x8 asfrag(u32x4 v) { return __builtin_bit_cast(bf16x8, v); }
DEVI bf16x8 ldsfrag(LAS char* p) { return *(LAS bf16x8*)p; }

template <int CTRL> DEVI float dpp_add(float v) { return v + __int_as_float(__builtin_amdgcn_update_dpp(0, __float_as_int(v), CTRL, 0xf, 0xf, false)); }
DEVI float row16_sum(float v) { v = dpp_add<0x128>(v); v = dpp_add<0x124>(v); v = dpp_add<0x122>(v); return dpp_add<0x121>(v); }
DEVI float xq_sum(float v) { v += __shfl_xor(v, 16); v += __shfl_xor(v, 32); return v; }
DEVI float wave_sum(float v) { return xq_sum(row16_sum(v)); }

DEVI float gelu_tanh(float x) {
  const float u = x * (1.0f + 0.044715f * x * x) * (-2.0f * 0.7978845608028654f * 1.4426950408889634f);
  return x * __builtin_amdgcn_rcpf(1.0f + __builtin_amdgcn_exp2f(u));
}

DEVI u32x4 pair_swap(u32x2 a, u32x2 b) {
  auto s0 = __builtin_amdgcn_permlane16_swap(a.x, b.x, false, false);
  auto s1 = __builtin_amdgcn_permlane16_swap(a.y, b.y, false, false);
  return (u32x4){s0[0], s1[0], s0[1], s1[1]};
}

DEVI void note_timeout(char* ws, unsigned code, int lane) {
  if (lane == 0) __hip_atomic_store((GAS unsigned*)(ws + WS_TMO), code, __ATOMIC_RELAXED, __HIP_MEMORY_SCOPE_AGENT);
}
DEVI void poll_flags(char* ws, unsigned off, int n, u64 T, int lane) {
  for (unsigned spins = 0;; ++spins) {
    u64 f = T;
    if (lane < n) f = __hip_atomic_load((GAS u64*)(ws + off) + lane, __ATOMIC_RELAXED, __HIP_MEMORY_SCOPE_AGENT);
    if (__all(f == T)) break;
    if (spins > SPIN_MAX) { note_timeout(ws, 0x100u | (off >> 12), lane); break; }
    __builtin_amdgcn_s_sleep(1);
  }
  CFENCE();
}
DEVI void poll_flags256(char* ws, unsigned off, u64 T, int lane) {
  for (unsigned spins = 0;; ++spins) {
    bool ok = true;
#pragma unroll
    for (int i = 0; i < 4; ++i) ok &= __hip_atomic_load((GAS u64*)(ws + off) + i * 64 + lane, __ATOMIC_RELAXED, __HIP_MEMORY_SCOPE_AGENT) == T;
    if (__all(ok)) break;
    if (spins > SPIN_MAX) { note_timeout(ws, 0x200u | (off >> 12), lane); break; }
    __builtin_amdgcn_s_sleep(1);
  }
  CFENCE();
}
DEVI void set_flag(char* ws, unsigned off, u64 T, int lane) {
  if (lane == 0) __hip_atomic_store((GAS u64*)(ws + off), T, __ATOMIC_RELAXED, __HIP_MEMORY_SCOPE_AGENT);
}
DEVI bool arrive_last(LAS char* lds, int idx, unsigned n, int lane) {
  unsigned old = 0;
  if (lane == 0) old = __hip_atomic_fetch_add((LAS unsigned*)(lds + L_CNT) + idx, 1u, __ATOMIC_RELAXED, __HIP_MEMORY_SCOPE_WORKGROUP);
  return (unsigned)__builtin_amdgcn_readfirstlane(old) == n - 1;
}
DEVI void publish(LAS char* lds, int cidx, unsigned nwaves, char* ws, unsigned flag_off, u64 T, int lane) {
  wait_vm<0>();
  if (arrive_last(lds, cidx, nwaves, lane)) set_flag(ws, flag_off, T, lane);
}

#define LANE_IDS() int tz_ = threadIdx.x; asm volatile("" : "+v"(tz_)); const int lane = tz_ & 63, l15 = lane & 15, q = lane >> 4; (void)l15; (void)q

#define STAMP(k) do { if (((k) == 0 || ((k) == 1 && wid == 0)) && lane == 0) *(GAS u64*)(p.ws + WS_STAMP + ((size_t)b * 8 + wid) * 256 + (k) * 8) = __builtin_amdgcn_s_memrealtime(); } while (0)
#define STAMPC(k) do { } while (0)

template <int I, int NT, int RING, int PC> struct LoaderTail {
  static DEVI void run() {
    if constexpr (I <= NT - 2) { wait_vm<(NT - I - 2) * PC>(); BAR(); LoaderTail<I + 1, NT, RING, PC>::run(); }
  }
};
template <int NT, int RING, int PC, class Issue> DEVI void loader_prefill(Issue issue) {
  constexpr int PRE = RING < NT ? RING : NT;
#pragma unroll 1
  for (int t = 0; t < PRE; ++t) { if (PRE * PC > 63) wait_vm<63 - PC>(); issue(t, t); }
}
template <int NT, int RING, int PC, class Issue> DEVI void loader_run(Issue issue) {
  constexpr int PRE = RING < NT ? RING : NT;
  wait_vm<(PRE - 1) * PC>(); BAR();
  if constexpr (NT > RING) {
    int slot = 0;
#pragma unroll 1
    for (int i = 0; i < NT - RING; ++i) {
      wait_vm<((RING - 2) * PC > 63 - PC ? 63 - PC : (RING - 2) * PC)>(); BAR();
      issue(i + RING, slot);
      slot = (slot + 1 == RING) ? 0 : slot + 1;
    }
  }
  LoaderTail<(NT > RING ? NT - RING : 0), NT, RING, PC>::run();
  BAR();
}
template <int NBAR> DEVI void idle_phase() {
#pragma unroll 1
  for (int i = 0; i < NBAR; ++i) BAR();
}

template <int I, int NMF, int NRD, int PER> struct Interleave {
  static DEVI void run() {
    if constexpr (I < NMF) {
      __builtin_amdgcn_sched_group_barrier(0x8, 1, 0);
      constexpr int left = NRD - I * PER, now = left < PER ? left : PER;
      if constexpr (now > 0) __builtin_amdgcn_sched_group_barrier(0x100, now, 0);
      Interleave<I + 1, NMF, NRD, PER>::run();
    }
  }
};
template <int NMF, int NRD, int PER> DEVI void interleave() { Interleave<0, NMF, NRD, PER>::run(); }

constexpr int P1_SA = 7, P1_DB = 4, P1_DOFF = 114688;
constexpr int P2_RING = 15;
constexpr int P3_RING = 10;
constexpr int P4_SA = 10, P4_DB = 5, P4_DOFF = 81920;
constexpr int P5_RING = 14;

__global__ __launch_bounds__(512, 2) void adaLN_cond__spectral_fnet__mixer_gmlp__prompt_kernel(Params p) {
  extern __shared__ __attribute__((aligned(16))) char smem_[];
  LAS char* lds = (LAS char*)smem_;
  const int tid = threadIdx.x, lane = tid & 63, wid = __builtin_amdgcn_readfirstlane(tid >> 6);
  const int b = blockIdx.x, xg = b & 7, i8 = b >> 3;
  const int g = xg * 4 + (i8 >> 3), j = i8 & 7;
  const int l15 = lane & 15, q = lane >> 4;
  char* const ws = p.ws;
  const int rot = xg * 4;

  STAMP(0); STAMPC(30);
  const u64 Ein = __hip_atomic_load((GAS u64*)(ws + WS_EPOCH), __ATOMIC_RELAXED, __HIP_MEMORY_SCOPE_AGENT);
  if (tid < 32) *((LAS unsigned*)(lds + L_CNT) + tid) = 0u;

  if (wid < 4) {
    const int w = wid;
    {
      const rsrc_t rx = mkrs(p.x, SEQ * DM * 2);
      u32x4 v[2][2];
#pragma unroll
      for (int r = 0; r < 2; ++r)
#pragma unroll
        for (int h = 0; h < 2; ++h) v[r][h] = ld16<0>(rx, lane < 40 ? (unsigned)((lane + 40 * h) * 16) : OOB, (unsigned)(64 * g + 8 * j + 2 * w + r) * (DM * 2));
      wait_lgkm0();
      BAR();
#pragma unroll
      for (int r = 0; r < 2; ++r) {
        float f[16];
#pragma unroll
        for (int h = 0; h < 2; ++h)
#pragma unroll
          for (int i = 0; i < 4; ++i) { f[h * 8 + 2 * i] = bflo(v[r][h][i]); f[h * 8 + 2 * i + 1] = bfhi(v[r][h][i]); }
        float s = 0.f;
#pragma unroll
        for (int i = 0; i < 16; ++i) s += f[i];
        const float mean = wave_sum(s) * (1.0f / DM);
        float m2 = 0.f;
#pragma unroll
        for (int i = 0; i < 16; ++i) { f[i] -= mean; m2 += f[i] * f[i]; }
        if (lane >= 40) m2 = 0.f;
        const float rstd = 1.0f / sqrtf(wave_sum(m2) * (1.0f / DM) + 1e-5f);
        if (lane < 40) {
#pragma unroll
          for (int h = 0; h < 2; ++h) {
            u32x4 o;
#pragma unroll
            for (int i = 0; i < 4; ++i) o[i] = pk2(f[h * 8 + 2 * i] * rstd, f[h * 8 + 2 * i + 1] * rstd);
            *(LAS u32x4*)(lds + L_P0T + (2 * w + r) * P0T_RS + (lane + 40 * h) * 16) = o;
          }
        }
      }
      wait_lgkm0(); CFENCE();
      if (lane == 0) __hip_atomic_fetch_add((LAS unsigned*)(lds + L_CNT) + 0, 1u, __ATOMIC_RELAXED, __HIP_MEMORY_SCOPE_WORKGROUP);
      for (unsigned spins = 0; spins < SPIN_MAX; ++spins) {
        if (__hip_atomic_load((LAS unsigned*)(lds + L_CNT) + 0, __ATOMIC_RELAXED, __HIP_MEMORY_SCOPE_WORKGROUP) >= 4u) break;
      }
      CFENCE();
      const rsrc_t rxn = mkrs(ws + WS_XNT, 8 * 32 * 10240);
#pragma unroll
      for (int it = 0; it < 3; ++it) {
        const int pc = 4 * it + q, nf = 10 * w + (pc < 10 ? pc : 9);
        const int qq = l15 >> 2, pp = l15 & 3;
        LAS char* a0 = lds + L_P0T + qq * P0T_RS + (16 * nf + 4 * pp) * 2;
        const u32x2 lo = __builtin_bit_cast(u32x2, __builtin_amdgcn_ds_read_tr16_b64_v4i16((LAS s16x4*)a0));
        const u32x2 hi = __builtin_bit_cast(u32x2, __builtin_amdgcn_ds_read_tr16_b64_v4i16((LAS s16x4*)(a0 + 4 * P0T_RS)));
        const int jj = nf / 5, nfl = nf - 5 * jj;
        const unsigned off = (unsigned)(((jj * 32 + g) * 10 + (j >> 2) * 5 + nfl) * 1024 + ((j & 3) * 16 + l15) * 16);
        st16<SC1>((u32x4){lo.x, lo.y, hi.x, hi.y}, rxn, pc < 10 ? off : OOB, 0);
      }
    }
    const u64 T = (((u64)(unsigned)__builtin_amdgcn_readfirstlane((unsigned)(Ein >> 32)) << 32) | (unsigned)__builtin_amdgcn_readfirstlane((unsigned)Ein)) + 1;
    publish(lds, 1, 4, ws, WS_FLAG0 + (g * 8 + j) * 8, T, lane);
    STAMP(1);

    {
      LANE_IDS();
      f32x4 acc[2][5];
#pragma unroll
      for (int m = 0; m < 2; ++m)
#pragma unroll
        for (int n = 0; n < 5; ++n) acc[m][n] = (f32x4){0.f, 0.f, 0.f, 0.f};
      float rs[2] = {0.f, 0.f};
      unsigned ones = 0x3f803f80u; asm volatile("" : "+v"(ones));
      const unsigned aoff0 = (w * 16 + l15) * 128 + ((q ^ (l15 >> 1)) << 4), aoff1 = aoff0 ^ 64;
      const unsigned boff = P1_DOFF + lane * 16;
      bf16x8 XA[2], XB[5], YA[2], YB[5];
      auto rd = [&](bf16x8(&A)[2], bf16x8(&B)[5], int sa, int sb, int kk) {
        LAS char* pa = lds + sa * 16384 + (kk ? aoff1 : aoff0);
        LAS char* pb = lds + sb * 10240 + boff + kk * 5120;
#pragma unroll
        for (int n = 0; n < 5; ++n) B[n] = ldsfrag(pb + n * 1024);
        A[0] = ldsfrag(pa); A[1] = ldsfrag(pa + 8192);
      };
      auto mm = [&](const bf16x8(&A)[2], const bf16x8(&B)[5]) {
#pragma unroll
        for (int m = 0; m < 2; ++m) {
#pragma unroll
          for (int n = 0; n < 5; ++n) acc[m][n] = mfma(B[n], A[m], acc[m][n]);
          const u32x4 au = __builtin_bit_cast(u32x4, A[m]);
#pragma unroll
          for (int i = 0; i < 4; ++i) { const unsigned ai = au[i]; asm("v_dot2c_f32_bf16 %0, %1, %2" : "+v"(rs[m]) : "v"(ai), "v"(ones)); }
        }
      };
      BAR();
      STAMP(2); STAMPC(28);
      rd(XA, XB, 0, 0, 0); wait_lgkm0();
      int sa = 0, sb = 0;
      auto steps = [&](int n) {
#pragma unroll 1
        for (int i = 0; i < n; ++i) {
          rd(YA, YB, sa, sb, 1); mm(XA, XB); interleave<10, 7, 1>(); SCHED(); wait_lgkm0();
          BAR();
          sa = (sa + 1 == P1_SA) ? 0 : sa + 1; sb = (sb + 1 == P1_DB) ? 0 : sb + 1;
          rd(XA, XB, sa, sb, 0); mm(YA, YB); interleave<10, 7, 1>(); SCHED(); wait_lgkm0();
        }
      };
      steps(8);
      u64 fb[4];
#pragma unroll
      for (int i = 0; i < 4; ++i) fb[i] = __hip_atomic_load((GAS u64*)(ws + WS_FLAGB) + i * 64 + lane, __ATOMIC_RELAXED, __HIP_MEMORY_SCOPE_AGENT);
      steps(8);
      if (!__all(fb[0] == T && fb[1] == T && fb[2] == T && fb[3] == T)) poll_flags256(ws, WS_FLAGB, T, lane);
      CFENCE();
      f32x4 mp[2][4];
      const rsrc_t rm = mkrs(ws + WS_MODP, 4 * 3840 * 4);
      const int vsel = lane < 32 ? (w == 0 ? 1 : w == 1 ? 0 : w == 2 ? 2 : 4) : 5;
      const bool vact = (lane < 20) || (w == 0 && lane >= 32 && lane < 52);
#pragma unroll
      for (int k = 0; k < 4; ++k) mp[0][k] = __builtin_bit_cast(f32x4, ld16<SC1>(rm, vact ? (unsigned)((vsel * DM + 80 * j + (lane & 31) * 4) * 4) : OOB, k * 3840 * 4));
      steps(16);
      STAMP(3); STAMPC(29);
      {
        f32x4 s = (mp[0][0] + mp[0][1]) + (mp[0][2] + mp[0][3]);
        const bool plus1 = (vsel == 1) || (vsel == 4);
#pragma unroll
        for (int i = 0; i < 4; ++i) { s[i] = rbf(s[i]); if (plus1) s[i] = rbf(1.0f + s[i]); }
        const int trow = lane < 32 ? w : 4;
        if (vact) *(LAS f32x4*)(lds + L_TAB + (trow * 80 + (lane & 31) * 4) * 4) = s;
        wait_lgkm0(); CFENCE();
        if (lane == 0) __hip_atomic_fetch_add((LAS unsigned*)(lds + L_CNT) + 2, 1u, __ATOMIC_RELAXED, __HIP_MEMORY_SCOPE_WORKGROUP);
        for (unsigned spins = 0; spins < SPIN_MAX; ++spins) {
          if (__hip_atomic_load((LAS unsigned*)(lds + L_CNT) + 2, __ATOMIC_RELAXED, __HIP_MEMORY_SCOPE_WORKGROUP) >= 4u) break;
        }
        CFENCE();
      }
      rs[0] = xq_sum(rs[0]); rs[1] = -xq_sum(rs[1]);
      u32x2 pkd[2][5];
#pragma unroll
      for (int n = 0; n < 5; ++n) {
        const f32x4 c1 = *(LAS f32x4*)(lds + L_TAB + (0 * 80 + 16 * n + 4 * q) * 4), sa4 = *(LAS f32x4*)(lds + L_TAB + (1 * 80 + 16 * n + 4 * q) * 4);
        const f32x4 pv = acc[0][n] * c1 + rs[0] * sa4, qv = rs[1] * sa4 - acc[1][n] * c1;
        pkd[0][n] = (u32x2){pk2(pv[0], pv[1]), pk2(pv[2], pv[3])};
        pkd[1][n] = (u32x2){pk2(qv[0], qv[1]), pk2(qv[2], qv[3])};
      }
      const rsrc_t rpq = mkrs(ws + WS_PQ, 32 * 40 * 4 * 1024);
      auto pq_off = [&](int m, int nf) { const int c = 10 * j + 2 * nf + (q >> 1); return (unsigned)(((g * 40 + m * 20 + (c >> 2)) * 4 + w) * 1024 + ((c & 3) * 16 + l15) * 16); };
      const int odd = q & 1;
      st16<SC1>(pair_swap(pkd[0][0], pkd[0][1]), rpq, pq_off(0, odd), 0);
      st16<SC1>(pair_swap(pkd[0][2], pkd[0][3]), rpq, pq_off(0, 2 + odd), 0);
      st16<SC1>(pair_swap(pkd[1][0], pkd[1][1]), rpq, pq_off(1, odd), 0);
      st16<SC1>(pair_swap(pkd[1][2], pkd[1][3]), rpq, pq_off(1, 2 + odd), 0);
      st16<SC1>(pair_swap(pkd[0][4], pkd[1][4]), rpq, pq_off(odd, 4), 0);
      publish(lds, 3, 4, ws, WS_FLAG1 + g * 256 + j * 8, T, lane);
      STAMP(4);
    }

    {
      LANE_IDS();
      constexpr int NT = 20, W = 6;
      const rsrc_t rx = mkrs(p.x, SEQ * DM * 2);
      u32x2 xin[5];
#pragma unroll
      for (int n = 0; n < 5; ++n) xin[n] = ld8<0>(rx, (unsigned)((64 * g + 16 * w + l15) * (DM * 2) + (80 * j + 16 * n + 4 * q) * 2), 0);
      poll_flags(ws, WS_FLAG1 + g * 256, 8, T, lane);
      STAMP(5);
      const rsrc_t ra = mkrs(ws + WS_PQ + (size_t)g * 40 * 4 * 1024, 40 * 4 * 1024);
      u32x4 A[NT][2];
#pragma unroll
      for (int t = 0; t < W; ++t) { A[t][0] = ld16<SC1>(ra, lane * 16, ((2 * t) * 4 + w) * 1024); A[t][1] = ld16<SC1>(ra, lane * 16, ((2 * t + 1) * 4 + w) * 1024); }
      f32x4 acc[5];
#pragma unroll
      for (int n = 0; n < 5; ++n) acc[n] = (f32x4){0.f, 0.f, 0.f, 0.f};
      const unsigned boff0 = l15 * 128 + ((q ^ (l15 >> 1)) << 4), boff1 = boff0 ^ 64;
      bf16x8 XB[5], YB[5];
      auto rd = [&](bf16x8(&B)[5], int slot, int kk) {
        LAS char* pb = lds + slot * 10240 + (kk ? boff1 : boff0);
#pragma unroll
        for (int n = 0; n < 5; ++n) B[n] = ldsfrag(pb + n * 2048);
      };
      BAR();
      STAMP(6);
      rd(XB, 0, 0); wait_lgkm0();
#pragma unroll
      for (int t = 0; t < NT; ++t) {
        if (t + W < NT) { A[t + W][0] = ld16<SC1>(ra, lane * 16, ((2 * (t + W)) * 4 + w) * 1024); A[t + W][1] = ld16<SC1>(ra, lane * 16, ((2 * (t + W) + 1) * 4 + w) * 1024); }
        rd(YB, t % P2_RING, 1);
#pragma unroll
        for (int n = 0; n < 5; ++n) acc[n] = mfma(XB[n], asfrag(A[t][0]), acc[n]);
        interleave<5, 5, 1>(); SCHED(); wait_lgkm0();
        BAR();
        rd(XB, (t + 1) % P2_RING, 0);
#pragma unroll
        for (int n = 0; n < 5; ++n) acc[n] = mfma(YB[n], asfrag(A[t][1]), acc[n]);
        interleave<5, 5, 1>(); SCHED(); wait_lgkm0();
      }
      STAMP(7);
      float x1f[5][4]; float s = 0.f;
      u32x2 cp[5], x1p[5];
      const rsrc_t rx1 = mkrs(ws + WS_X1 + (size_t)b * 4 * 5 * 512, 4 * 5 * 512);
#pragma unroll
      for (int n = 0; n < 5; ++n) {
        const f32x4 ga = *(LAS f32x4*)(lds + L_TAB + (2 * 80 + 16 * n + 4 * q) * 4), cb1 = *(LAS f32x4*)(lds + L_TAB + (3 * 80 + 16 * n + 4 * q) * 4);
        const float x0 = bflo(xin[n].x), x1_ = bfhi(xin[n].x), x2 = bflo(xin[n].y), x3 = bfhi(xin[n].y);
        x1p[n] = (u32x2){pk2(x0 + ga[0] * acc[n][0], x1_ + ga[1] * acc[n][1]), pk2(x2 + ga[2] * acc[n][2], x3 + ga[3] * acc[n][3])};
        st8<0>(x1p[n], rx1, lane * 8, (w * 5 + n) * 512);
        x1f[n][0] = bflo(x1p[n].x); x1f[n][1] = bfhi(x1p[n].x); x1f[n][2] = bflo(x1p[n].y); x1f[n][3] = bfhi(x1p[n].y);
        cp[n] = (u32x2){pk2(x1f[n][0] * cb1[0], x1f[n][1] * cb1[1]), pk2(x1f[n][2] * cb1[2], x1f[n][3] * cb1[3])};
        s += (x1f[n][0] + x1f[n][1]) + (x1f[n][2] + x1f[n][3]);
      }
      const float mean = xq_sum(s) * (1.0f / 80.0f);
      float m2 = 0.f;
#pragma unroll
      for (int n = 0; n < 5; ++n)
#pragma unroll
        for (int r = 0; r < 4; ++r) { const float d = x1f[n][r] - mean; m2 += d * d; }
      m2 = xq_sum(m2);
      const rsrc_t rc = mkrs(ws + WS_X1C, 32 * 20 * 4 * 1024);
      auto c_off = [&](int nf) { const int c = 10 * j + 2 * nf + (q >> 1); return (unsigned)(((g * 20 + (c >> 2)) * 4 + w) * 1024 + ((c & 3) * 16 + l15) * 16); };
      const int odd = q & 1;
      st16<SC1>(pair_swap(cp[0], cp[1]), rc, c_off(odd), 0);
      st16<SC1>(pair_swap(cp[2], cp[3]), rc, c_off(2 + odd), 0);
      st8<SC1>(cp[4], rc, c_off(4) + odd * 8, 0);
      const rsrc_t rs1 = mkrs(ws + WS_ST1, SEQ * 8 * 8);
      st8<SC1>((u32x2){__float_as_uint(mean), __float_as_uint(m2)}, rs1, q == 0 ? (unsigned)(((64 * g + 16 * w + l15) * 8 + j) * 8) : OOB, 0);
      publish(lds, 4, 4, ws, WS_FLAG2 + g * 256 + j * 8, T, lane);
      STAMP(8);
    }

    {
      LANE_IDS();
      constexpr int NT = 10;
      {
        const rsrc_t ruv = mkrs(ws + WS_UV, 2 * 896 * 16);
        const int col = w * 56 + lane, part = col >= 112 ? 1 : 0, cc = col - 112 * part;
        const unsigned off = lane < 56 ? (unsigned)((part * 896 + 112 * j + cc) * 16) : OOB;
        u32x4 gv;
        for (unsigned spins = 0;; ++spins) {
          gv = ld16<SC1>(ruv, off, 0);
          const bool ok = lane >= 56 || (gv.z == (unsigned)T && gv.w == (unsigned)(T >> 32));
          if (__all(ok)) break;
          if (spins > SPIN_MAX) { note_timeout(ws, 0x300, lane); break; }
          __builtin_amdgcn_s_sleep(1); CFENCE();
        }
        if (lane < 56) { *(LAS unsigned*)(lds + L_UV + col * 4) = gv.x; *(LAS unsigned*)(lds + L_UV + (224 + col) * 4) = gv.y; }
      }
      poll_flags(ws, WS_FLAG2 + g * 256, 8, T, lane);
      STAMP(9);
      const rsrc_t ra = mkrs(ws + WS_X1C + (size_t)g * 20 * 4 * 1024, 20 * 4 * 1024);
      u32x4 A[20];
#pragma unroll
      for (int k = 0; k < 20; ++k) A[k] = ld16<SC1>(ra, lane * 16, (k * 4 + w) * 1024);
      const rsrc_t rs1 = mkrs(ws + WS_ST1, SEQ * 8 * 8);
      const u32x4 st = ld16<SC1>(rs1, (unsigned)(((64 * g + 16 * w + l15) * 8 + 2 * q) * 8), 0);
      f32x4 acc[7];
      bf16x8 XB[7], YB[7];
      auto rd = [&](bf16x8(&B)[7], int slot, int kk) {
        LAS char* pb = lds + slot * 14336 + kk * 7168 + lane * 16;
#pragma unroll
        for (int n = 0; n < 7; ++n) B[n] = ldsfrag(pb + n * 1024);
      };
      wait_lgkm0();
      BAR();
      STAMP(10);
      rd(XB, 0, 0); wait_lgkm0();
      float mean1, rstd1;
      {
        const float ma = __uint_as_float(st.x), mb = __uint_as_float(st.z);
        float m = 0.5f * (ma + mb), d = ma - mb, M2 = __uint_as_float(st.y) + __uint_as_float(st.w) + d * d * 40.0f;
        float mo = __shfl_xor(m, 16), Mo = __shfl_xor(M2, 16); d = m - mo; M2 = M2 + Mo + d * d * 80.0f; m = 0.5f * (m + mo);
        mo = __shfl_xor(m, 32); Mo = __shfl_xor(M2, 32); d = m - mo; M2 = M2 + Mo + d * d * 160.0f; m = 0.5f * (m + mo);
        mean1 = m; rstd1 = 1.0f / sqrtf(M2 * (1.0f / DM) + 1e-5f);
      }
#pragma unroll
      for (int n = 0; n < 7; ++n) acc[n] = (f32x4){0.f, 0.f, 0.f, 0.f};
#pragma unroll
      for (int t = 0; t < NT; ++t) {
        rd(YB, t % P3_RING, 1);
#pragma unroll
        for (int n = 0; n < 7; ++n) acc[n] = mfma(asfrag(A[2 * t]), XB[n], acc[n]);
        interleave<7, 7, 2>(); SCHED(); wait_lgkm0();
        BAR();
        rd(XB, (t + 1) % P3_RING, 0);
#pragma unroll
        for (int n = 0; n < 7; ++n) acc[n] = mfma(asfrag(A[2 * t + 1]), YB[n], acc[n]);
        interleave<7, 7, 2>(); SCHED(); wait_lgkm0();
      }
      u32x2 z2p[7];
      const int nval = (j == 7) ? 5 : 7;
      {
        float mr[4], rr[4];
#pragma unroll
        for (int r = 0; r < 4; ++r) { mr[r] = __shfl(mean1, 4 * q + r); rr[r] = __shfl(rstd1, 4 * q + r); }
        float zs[4] = {0.f, 0.f, 0.f, 0.f};
#pragma unroll
        for (int n = 0; n < 7; ++n) {
          const float u = *(LAS float*)(lds + L_UV + (16 * n + l15) * 4), v = *(LAS float*)(lds + L_UV + (224 + 16 * n + l15) * 4);
          float z[4];
#pragma unroll
          for (int r = 0; r < 4; ++r) z[r] = gelu_tanh(rr[r] * (acc[n][r] - mr[r] * u) + v);
          z2p[n] = (u32x2){pk2(z[0], z[1]), pk2(z[2], z[3])};
          if (n < nval) { zs[0] += bflo(z2p[n].x); zs[1] += bfhi(z2p[n].x); zs[2] += bflo(z2p[n].y); zs[3] += bfhi(z2p[n].y); }
        }
        const float inv = 1.0f / (16.0f * nval);
        float mz[4], M2[4];
#pragma unroll
        for (int r = 0; r < 4; ++r) { mz[r] = row16_sum(zs[r]) * inv; M2[r] = 0.f; }
#pragma unroll
        for (int n = 0; n < 7; ++n) if (n < nval) {
          const float d0 = bflo(z2p[n].x) - mz[0], d1 = bfhi(z2p[n].x) - mz[1], d2 = bflo(z2p[n].y) - mz[2], d3 = bfhi(z2p[n].y) - mz[3];
          M2[0] += d0 * d0; M2[1] += d1 * d1; M2[2] += d2 * d2; M2[3] += d3 * d3;
        }
#pragma unroll
        for (int r = 0; r < 4; ++r) M2[r] = row16_sum(M2[r]);
        const float ms = l15 == 0 ? mz[0] : l15 == 1 ? mz[1] : l15 == 2 ? mz[2] : mz[3];
        const float Ms = l15 == 0 ? M2[0] : l15 == 1 ? M2[1] : l15 == 2 ? M2[2] : M2[3];
        const rsrc_t rs2 = mkrs(ws + WS_ST2, SEQ * 8 * 16);
        st16<SC1>((u32x4){__float_as_uint(ms), __float_as_uint(Ms), (unsigned)T, (unsigned)(T >> 32)}, rs2,
                  l15 < 4 ? (unsigned)(((64 * g + 16 * w + 4 * q + l15) * 8 + j) * 16) : OOB, 0);
      }
      STAMP(11);
#pragma unroll
      for (int n = 0; n < 7; ++n) acc[n] = (f32x4){0.f, 0.f, 0.f, 0.f};
#pragma unroll
      for (int t = 0; t < NT; ++t) {
        rd(YB, (NT + t) % P3_RING, 1);
#pragma unroll
        for (int n = 0; n < 7; ++n) acc[n] = mfma(XB[n], asfrag(A[2 * t]), acc[n]);
        interleave<7, 7, 2>(); SCHED(); wait_lgkm0();
        BAR();
        rd(XB, (NT + t + 1) % P3_RING, 0);
#pragma unroll
        for (int n = 0; n < 7; ++n) acc[n] = mfma(YB[n], asfrag(A[2 * t + 1]), acc[n]);
        interleave<7, 7, 2>(); SCHED(); wait_lgkm0();
      }
      STAMP(12);
      const rsrc_t rs2 = mkrs(ws + WS_ST2, SEQ * 8 * 16);
      u32x4 ga_, gb_;
      const unsigned soff = (unsigned)(((64 * g + 16 * w + l15) * 8 + 2 * q) * 16);
      ga_ = ld16<SC1>(rs2, soff, 0); gb_ = ld16<SC1>(rs2, soff + 16, 0);
      {
        const rsrc_t rz = mkrs(ws + WS_Z1 + (size_t)b * 4 * 7 * 512, 4 * 7 * 512);
#pragma unroll
        for (int n = 0; n < 7; ++n) {
          const f32x4 u = *(LAS f32x4*)(lds + L_UV + (112 + 16 * n + 4 * q) * 4), v = *(LAS f32x4*)(lds + L_UV + (224 + 112 + 16 * n + 4 * q) * 4);
          float z[4];
#pragma unroll
          for (int r = 0; r < 4; ++r) z[r] = gelu_tanh(rstd1 * (acc[n][r] - mean1 * u[r]) + v[r]);
          st8<0>((u32x2){pk2(z[0], z[1]), pk2(z[2], z[3])}, rz, lane * 8, (w * 7 + n) * 512);
        }
      }
      for (unsigned spins = 0;; ++spins) {
        const bool ok = ga_.z == (unsigned)T && ga_.w == (unsigned)(T >> 32) && gb_.z == (unsigned)T && gb_.w == (unsigned)(T >> 32);
        if (__all(ok)) break;
        if (spins > SPIN_MAX) { note_timeout(ws, 0x400, lane); break; }
        __builtin_amdgcn_s_sleep(1); CFENCE();
        ga_ = ld16<SC1>(rs2, soff, 0); gb_ = ld16<SC1>(rs2, soff + 16, 0);
      }
      float mean2, rstd2;
      {
        const float na = 112.0f, nb = (q == 3) ? 80.0f : 112.0f;
        float ma = __uint_as_float(ga_.x), mb = __uint_as_float(gb_.x);
        float n = na + nb, d = mb - ma, m = ma + d * (nb / n), M2 = __uint_as_float(ga_.y) + __uint_as_float(gb_.y) + d * d * (na * nb / n);
#pragma unroll
        for (int sft = 16; sft <= 32; sft <<= 1) {
          const float no = __shfl_xor(n, sft), mo = __shfl_xor(m, sft), Mo = __shfl_xor(M2, sft);
          const float nt = n + no; d = mo - m;
          M2 = M2 + Mo + d * d * (n * no / nt);
          m = (n * m + no * mo) / nt; n = nt;
        }
        mean2 = m; rstd2 = 1.0f / sqrtf(M2 * (1.0f / FH) + 1e-5f);
      }
      {
        float mr[4], rr[4];
#pragma unroll
        for (int r = 0; r < 4; ++r) { mr[r] = __shfl(mean2, 4 * q + r); rr[r] = __shfl(rstd2, 4 * q + r); }
        u32x2 zn[7];
#pragma unroll
        for (int n = 0; n < 7; ++n)
          zn[n] = (u32x2){pk2((bflo(z2p[n].x) - mr[0]) * rr[0], (bfhi(z2p[n].x) - mr[1]) * rr[1]), pk2((bflo(z2p[n].y) - mr[2]) * rr[2], (bfhi(z2p[n].y) - mr[3]) * rr[3])};
        const rsrc_t rz2 = mkrs(ws + WS_Z2T, 8 * 32 * 14336);
        auto z_off = [&](int nf) { return (unsigned)(((j * 32 + g) * 14 + (w >> 1) * 7 + nf) * 1024 + ((2 * (w & 1) + (q >> 1)) * 16 + l15) * 16); };
        const int odd = q & 1;
        st16<SC1>(pair_swap(zn[0], zn[1]), rz2, z_off(odd), 0);
        st16<SC1>(pair_swap(zn[2], zn[3]), rz2, z_off(2 + odd), 0);
        st16<SC1>(pair_swap(zn[4], zn[5]), rz2, z_off(4 + odd), 0);
        st8<SC1>(zn[6], rz2, z_off(6) + odd * 8, 0);
      }
      publish(lds, 5, 4, ws, WS_FLAG3 + j * 256 + g * 8, T, lane);
      STAMP(13);
    }

    {
      LANE_IDS();
      const int mh = w >> 1, nh = w & 1;
      f32x4 acc[2][4];
#pragma unroll
      for (int m = 0; m < 2; ++m)
#pragma unroll
        for (int n = 0; n < 4; ++n) acc[m][n] = (f32x4){0.f, 0.f, 0.f, 0.f};
      const unsigned aoff0 = (mh * 32 + l15) * 128 + ((q ^ (l15 >> 1)) << 4), aoff1 = aoff0 ^ 64;
      const unsigned boff = P4_DOFF + nh * 4096 + lane * 16;
      const rsrc_t rb = mkrs(p.bsp, SEQ * 2);
      const float bs0 = bflo(ld2<0>(rb, (64 * g + 32 * mh + l15) * 2, 0)), bs1 = bflo(ld2<0>(rb, (64 * g + 32 * mh + 16 + l15) * 2, 0));
      bf16x8 XA[2], XB[4], YA[2], YB[4];
      if (nh == 0) {
        auto rd = [&](bf16x8(&A)[2], bf16x8(&B)[4], int sa, int sb, int kk) {
          LAS char* pa = lds + sa * 8192 + (kk ? aoff1 : aoff0);
          LAS char* pb = lds + sb * 14336 + boff + kk * 7168;
#pragma unroll
          for (int n = 0; n < 4; ++n) B[n] = ldsfrag(pb + n * 1024);
          A[0] = ldsfrag(pa); A[1] = ldsfrag(pa + 2048);
        };
        auto mm = [&](const bf16x8(&A)[2], const bf16x8(&B)[4]) {
#pragma unroll
          for (int m = 0; m < 2; ++m)
#pragma unroll
            for (int n = 0; n < 4; ++n) acc[m][n] = mfma(B[n], A[m], acc[m][n]);
        };
        BAR();
        STAMP(14);
        rd(XA, XB, 0, 0, 0); wait_lgkm0();
        int sa = 0, sb = 0;
#pragma unroll 1
        for (int i = 0; i < 32; ++i) {
          rd(YA, YB, sa, sb, 1); mm(XA, XB); interleave<8, 6, 1>(); SCHED(); wait_lgkm0();
          BAR();
          sa = (sa + 1 == P4_SA) ? 0 : sa + 1; sb = (sb + 1 == P4_DB) ? 0 : sb + 1;
          rd(XA, XB, sa, sb, 0); mm(YA, YB); interleave<8, 6, 1>(); SCHED(); wait_lgkm0();
        }
      } else {
        auto rd = [&](bf16x8(&A)[2], bf16x8(&B)[4], int sa, int sb, int kk) {
          LAS char* pa = lds + sa * 8192 + (kk ? aoff1 : aoff0);
          LAS char* pb = lds + sb * 14336 + boff + kk * 7168;
#pragma unroll
          for (int n = 0; n < 3; ++n) B[n] = ldsfrag(pb + n * 1024);
          A[0] = ldsfrag(pa); A[1] = ldsfrag(pa + 2048);
        };
        auto mm = [&](const bf16x8(&A)[2], const bf16x8(&B)[4]) {
#pragma unroll
          for (int m = 0; m < 2; ++m)
#pragma unroll
            for (int n = 0; n < 3; ++n) acc[m][n] = mfma(B[n], A[m], acc[m][n]);
        };
        BAR();
        STAMP(14);
        rd(XA, XB, 0, 0, 0); wait_lgkm0();
        int sa = 0, sb = 0;
#pragma unroll 1
        for (int i = 0; i < 32; ++i) {
          rd(YA, YB, sa, sb, 1); mm(XA, XB); interleave<6, 5, 1>(); SCHED(); wait_lgkm0();
          BAR();
          sa = (sa + 1 == P4_SA) ? 0 : sa + 1; sb = (sb + 1 == P4_DB) ? 0 : sb + 1;
          rd(XA, XB, sa, sb, 0); mm(YA, YB); interleave<6, 5, 1>(); SCHED(); wait_lgkm0();
        }
      }
      STAMP(15);
      const rsrc_t rz = mkrs(ws + WS_Z1 + (size_t)b * 4 * 7 * 512, 4 * 7 * 512);
      u32x2 up[2][4];
#pragma unroll
      for (int m = 0; m < 2; ++m)
#pragma unroll
        for (int n = 0; n < 4; ++n) {
          const int nf = 4 * nh + n;
          const u32x2 z = ld8<SC1>(rz, nf < 7 ? (unsigned)(lane * 8 + ((2 * mh + m) * 7 + nf) * 512) : OOB, 0);
          const float bs = m ? bs1 : bs0;
          up[m][n] = (u32x2){pk2(bflo(z.x) * rbf(rbf(acc[m][n][0]) + bs), bfhi(z.x) * rbf(rbf(acc[m][n][1]) + bs)),
                             pk2(bflo(z.y) * rbf(rbf(acc[m][n][2]) + bs), bfhi(z.y) * rbf(rbf(acc[m][n][3]) + bs))};
        }
      const rsrc_t ru = mkrs(ws + WS_U, 32 * 28 * 4 * 1024);
      auto u_off = [&](int mf, int nf) { const int c = 14 * j + 2 * nf + (q >> 1); return (unsigned)(((g * 28 + (c >> 2)) * 4 + mf) * 1024 + ((c & 3) * 16 + l15) * 16); };
      const int odd = q & 1;
      if (nh == 0) {
#pragma unroll
        for (int m = 0; m < 2; ++m) {
          st16<SC1>(pair_swap(up[m][0], up[m][1]), ru, u_off(2 * mh + m, odd), 0);
          st16<SC1>(pair_swap(up[m][2], up[m][3]), ru, u_off(2 * mh + m, 2 + odd), 0);
        }
      } else {
#pragma unroll
        for (int m = 0; m < 2; ++m) st16<SC1>(pair_swap(up[m][0], up[m][1]), ru, u_off(2 * mh + m, 4 + odd), 0);
        st16<SC1>(pair_swap(up[0][2], up[1][2]), ru, u_off(2 * mh + odd, 6), 0);
      }
      publish(lds, 6, 4, ws, WS_FLAG4 + g * 256 + j * 8, T, lane);
      if (b == 0 && w == 0 && lane == 0) __hip_atomic_store((GAS u64*)(ws + WS_EPOCH), T, __ATOMIC_RELAXED, __HIP_MEMORY_SCOPE_AGENT);
      STAMP(16);
    }

    {
      LANE_IDS();
      constexpr int NT = 14, W = 7;
      const rsrc_t rx1 = mkrs(ws + WS_X1 + (size_t)b * 4 * 5 * 512, 4 * 5 * 512);
      u32x2 x1p[5];
#pragma unroll
      for (int n = 0; n < 5; ++n) x1p[n] = ld8<SC1>(rx1, lane * 8, (w * 5 + n) * 512);
      poll_flags(ws, WS_FLAG4 + g * 256, 8, T, lane);
      STAMP(17);
      const rsrc_t ra = mkrs(ws + WS_U + (size_t)g * 28 * 4 * 1024, 28 * 4 * 1024);
      u32x4 A[NT][2];
#pragma unroll
      for (int t = 0; t < W; ++t) { A[t][0] = ld16<SC1>(ra, lane * 16, ((2 * t) * 4 + w) * 1024); A[t][1] = ld16<SC1>(ra, lane * 16, ((2 * t + 1) * 4 + w) * 1024); }
      f32x4 acc[5];
#pragma unroll
      for (int n = 0; n < 5; ++n) acc[n] = (f32x4){0.f, 0.f, 0.f, 0.f};
      bf16x8 XB[5], YB[5];
      auto rd = [&](bf16x8(&B)[5], int slot, int kk) {
        LAS char* pb = lds + slot * 10240 + kk * 5120 + lane * 16;
#pragma unroll
        for (int n = 0; n < 5; ++n) B[n] = ldsfrag(pb + n * 1024);
      };
      BAR();
      STAMP(18);
      rd(XB, 0, 0); wait_lgkm0();
#pragma unroll
      for (int t = 0; t < NT; ++t) {
        if (t + W < NT) { A[t + W][0] = ld16<SC1>(ra, lane * 16, ((2 * (t + W)) * 4 + w) * 1024); A[t + W][1] = ld16<SC1>(ra, lane * 16, ((2 * (t + W) + 1) * 4 + w) * 1024); }
        rd(YB, t, 1);
#pragma unroll
        for (int n = 0; n < 5; ++n) acc[n] = mfma(XB[n], asfrag(A[t][0]), acc[n]);
        interleave<5, 5, 1>(); SCHED(); wait_lgkm0();
        BAR();
        rd(XB, t + 1 < NT ? t + 1 : 0, 0);
#pragma unroll
        for (int n = 0; n < 5; ++n) acc[n] = mfma(YB[n], asfrag(A[t][1]), acc[n]);
        interleave<5, 5, 1>(); SCHED(); wait_lgkm0();
      }
      STAMP(19);
      u32x2 op[5];
#pragma unroll
      for (int n = 0; n < 5; ++n) {
        const f32x4 gb = *(LAS f32x4*)(lds + L_TAB + (4 * 80 + 16 * n + 4 * q) * 4);
        op[n] = (u32x2){pk2(bflo(x1p[n].x) + rbf(gb[0] * rbf(acc[n][0])), bfhi(x1p[n].x) + rbf(gb[1] * rbf(acc[n][1]))),
                        pk2(bflo(x1p[n].y) + rbf(gb[2] * rbf(acc[n][2])), bfhi(x1p[n].y) + rbf(gb[3] * rbf(acc[n][3])))};
      }
      const rsrc_t ro = mkrs(p.out, SEQ * DM * 2);
      const unsigned rowb = (unsigned)((64 * g + 16 * w + l15) * (DM * 2) + 80 * j * 2);
      const int odd = q & 1;
      st16<0>(pair_swap(op[0], op[1]), ro, rowb + (16 * odd + 8 * (q >> 1)) * 2, 0);
      st16<0>(pair_swap(op[2], op[3]), ro, rowb + (16 * (2 + odd) + 8 * (q >> 1)) * 2, 0);
      st8<0>(op[4], ro, rowb + (64 + 4 * q) * 2, 0);
      STAMP(20); STAMPC(31);
    }
  } else if (wid < 6) {
    const int h = wid - 4;
    const unsigned sw0 = (unsigned)(((lane & 7) ^ (lane >> 4)) * 16), sw1 = sw0 ^ 64;
    wait_lgkm0();
    BAR();
    {
      const rsrc_t r = mkrs((h ? p.wsim : p.wsre) + (size_t)g * 64 * SEQ, 64 * SEQ * 2);
      const unsigned v0 = (lane >> 3) * 4096 + sw0, v1 = (lane >> 3) * 4096 + sw1;
      auto issue = [&](int t, int slot) {
        const unsigned k = (unsigned)((t + rot) & 31) * 128;
#pragma unroll
        for (int pc = 0; pc < 8; ++pc) dma16<0>(r, lds + slot * 16384 + (h * 8 + pc) * 1024, (pc & 1) ? v1 : v0, k + pc * 32768);
      };
      loader_prefill<32, P1_SA, 8>(issue);
      STAMP(1);
      loader_run<32, P1_SA, 8>(issue);
    }
    {
      const unsigned v0 = (lane >> 3) * 1280 + (h ? sw1 : sw0), v1 = (lane >> 3) * 1280 + (h ? sw0 : sw1);
      auto issue = [&](int t, int slot) {
        const int im = t >= 10;
        const rsrc_t r = mkrs((im ? p.wdim : p.wdre) + (size_t)j * 80 * DM, 80 * DM * 2);
        const unsigned k = (unsigned)(t - 10 * im) * 128 + h * 5 * 8 * 1280;
#pragma unroll
        for (int pc = 0; pc < 5; ++pc) dma16<0>(r, lds + slot * 10240 + (h * 5 + pc) * 1024, (pc & 1) ? v1 : v0, k + pc * 8 * 1280);
      };
      loader_prefill<20, P2_RING, 5>(issue);
      STAMP(2);
      loader_run<20, P2_RING, 5>(issue);
    }
    {
      const rsrc_t r = mkrs(ws + WS_WINR + (size_t)j * 20 * 14336, 20 * 14336);
      auto issue = [&](int t, int slot) {
#pragma unroll
        for (int pc = 0; pc < 7; ++pc) dma16<SC1>(r, lds + slot * 14336 + (h * 7 + pc) * 1024, lane * 16, (unsigned)(t * 14336 + (h * 7 + pc) * 1024));
      };
      loader_prefill<20, P3_RING, 7>(issue);
      STAMP(3);
      loader_run<20, P3_RING, 7>(issue);
    }
    {
      const rsrc_t r = mkrs(p.wsp + (size_t)g * 64 * SEQ, 64 * SEQ * 2);
      const unsigned v0 = (lane >> 3) * 4096 + sw0, v1 = (lane >> 3) * 4096 + sw1;
      auto issue = [&](int t, int slot) {
        const unsigned k = (unsigned)((t + rot) & 31) * 128 + h * 4 * 32768;
#pragma unroll
        for (int pc = 0; pc < 4; ++pc) dma16<0>(r, lds + slot * 8192 + (h * 4 + pc) * 1024, (pc & 1) ? v1 : v0, k + pc * 32768);
      };
      loader_prefill<32, P4_SA, 4>(issue);
      STAMP(4);
      loader_run<32, P4_SA, 4>(issue);
    }
    {
      const rsrc_t r = mkrs(ws + WS_WOUTR + (size_t)j * 14 * 10240, 14 * 10240);
      auto issue = [&](int t, int slot) {
#pragma unroll
        for (int pc = 0; pc < 5; ++pc) dma16<SC1>(r, lds + slot * 10240 + (h * 5 + pc) * 1024, lane * 16, (unsigned)(t * 10240 + (h * 5 + pc) * 1024));
      };
      loader_prefill<14, P5_RING, 5>(issue);
      STAMP(5);
      loader_run<14, P5_RING, 5>(issue);
    }
  } else {
    const int h = wid - 6;
    wait_lgkm0();
    BAR();
    if (h == 0 && b < 240) {
      const int cg = b % 60, kq = b / 60;
      const rsrc_t rw = mkrs(p.wmod, DM * 3840 * 2), rc = mkrs(p.c, DM * 2);
      u32x4 wv[20]; unsigned cv[20];
#pragma unroll
      for (int it = 0; it < 20; ++it) {
        const int row = kq * 160 + it * 8 + (lane >> 3);
        wv[it] = ld16<0>(rw, (unsigned)(row * 7680 + cg * 128 + (lane & 7) * 16), 0);
        cv[it] = ld2<0>(rc, row * 2, 0);
      }
      float a8[8] = {0.f, 0.f, 0.f, 0.f, 0.f, 0.f, 0.f, 0.f};
#pragma unroll
      for (int it = 0; it < 20; ++it) {
        const float cf = bflo(cv[it]);
        const float sl = rbf(cf * __builtin_amdgcn_rcpf(1.0f + __builtin_amdgcn_exp2f(-1.4426950408889634f * cf)));
#pragma unroll
        for (int i = 0; i < 4; ++i) { a8[2 * i] += sl * bflo(wv[it][i]); a8[2 * i + 1] += sl * bfhi(wv[it][i]); }
      }
#pragma unroll
      for (int i = 0; i < 8; ++i) { a8[i] += __shfl_xor(a8[i], 8); a8[i] += __shfl_xor(a8[i], 16); a8[i] += __shfl_xor(a8[i], 32); }
      const rsrc_t rm = mkrs(ws + WS_MODP, 4 * 3840 * 4);
      const unsigned mo = (unsigned)((kq * 3840 + cg * 64 + (lane & 7) * 8) * 4);
      st16<SC1>((u32x4){__float_as_uint(a8[0]), __float_as_uint(a8[1]), __float_as_uint(a8[2]), __float_as_uint(a8[3])}, rm, lane < 8 ? mo : OOB, 0);
      st16<SC1>((u32x4){__float_as_uint(a8[4]), __float_as_uint(a8[5]), __float_as_uint(a8[6]), __float_as_uint(a8[7])}, rm, lane < 8 ? mo + 16 : OOB, 0);
    }
    {
      LAS char* sc = lds + L_RT + h * 7168;
#pragma unroll 1
      for (int rep = 0; rep < 2; ++rep) {
        int u;
        if (h == 1) u = b + 256 * rep; else u = (rep == 0 && b < 32) ? 512 + b : 1000;
        if (u >= 544) continue;
        const int qq = l15 >> 2, pp = l15 & 3;
        if (u < 320) {
          const int jp = u / 20, kf = u - 20 * jp, jj = jp >> 1, part = jp & 1;
          const rsrc_t r = mkrs(p.win, DM * FF * 2);
#pragma unroll
          for (int pc = 0; pc < 7; ++pc) {
            const int idx = pc * 64 + lane, row = idx / 14, ch = idx - 14 * row;
            const bool ok = 112 * jj + 8 * ch < FH;
            dma16<0>(r, sc + pc * 1024, ok ? (unsigned)((32 * kf + row) * (FF * 2) + (part ? 0 : FH * 2) + 224 * jj + ch * 16) : OOB, 0);
          }
          wait_vm<0>(); CFENCE();
          const rsrc_t ro = mkrs(ws + WS_WINR, 8 * 20 * 14336);
#pragma unroll
          for (int nf = 0; nf < 7; ++nf) {
            LAS char* a0 = sc + (8 * q + qq) * 224 + (16 * nf + 4 * pp) * 2;
            const u32x2 lo = __builtin_bit_cast(u32x2, __builtin_amdgcn_ds_read_tr16_b64_v4i16((LAS s16x4*)a0));
            const u32x2 hi = __builtin_bit_cast(u32x2, __builtin_amdgcn_ds_read_tr16_b64_v4i16((LAS s16x4*)(a0 + 4 * 224)));
            st16<SC1>((u32x4){lo.x, lo.y, hi.x, hi.y}, ro, (unsigned)((((jj * 2 + part) * 20 + kf) * 7 + nf) * 1024 + lane * 16), 0);
          }
        } else {
          const int uu = u - 320, jj = uu / 28, kf = uu - 28 * jj;
          const rsrc_t r = mkrs(p.wout, FH * DM * 2);
#pragma unroll
          for (int pc = 0; pc < 5; ++pc) {
            const int idx = pc * 64 + lane, row = idx / 10, ch = idx - 10 * row;
            dma16<0>(r, sc + pc * 1024, (unsigned)((32 * kf + row) * (DM * 2) + 160 * jj + ch * 16), 0);
          }
          wait_vm<0>(); CFENCE();
          const rsrc_t ro = mkrs(ws + WS_WOUTR, 8 * 14 * 10240);
#pragma unroll
          for (int nf = 0; nf < 5; ++nf) {
            LAS char* a0 = sc + (8 * q + qq) * 160 + (16 * nf + 4 * pp) * 2;
            const u32x2 lo = __builtin_bit_cast(u32x2, __builtin_amdgcn_ds_read_tr16_b64_v4i16((LAS s16x4*)a0));
            const u32x2 hi = __builtin_bit_cast(u32x2, __builtin_amdgcn_ds_read_tr16_b64_v4i16((LAS s16x4*)(a0 + 4 * 160)));
            st16<SC1>((u32x4){lo.x, lo.y, hi.x, hi.y}, ro, (unsigned)(((jj * 28 + kf) * 5 + nf) * 1024 + lane * 16), 0);
          }
        }
        wait_lgkm0(); CFENCE();
      }
    }
    const u64 T = (((u64)(unsigned)__builtin_amdgcn_readfirstlane((unsigned)(Ein >> 32)) << 32) | (unsigned)__builtin_amdgcn_readfirstlane((unsigned)Ein)) + 1;
    publish(lds, 7, 2, ws, WS_FLAGB + (g * 8 + j) * 8, T, lane);
    STAMP(1);
    {
      poll_flags256(ws, WS_FLAG0, T, lane);
      STAMP(2);
      const rsrc_t r = mkrs(ws + WS_XNT + (size_t)j * 32 * 10240, 32 * 10240);
      auto issue = [&](int t, int slot) {
        const unsigned k = (unsigned)((t + rot) & 31) * 10240;
#pragma unroll
        for (int pc = 0; pc < 5; ++pc) dma16<SC1>(r, lds + P1_DOFF + slot * 10240 + (h * 5 + pc) * 1024, lane * 16, k + (h * 5 + pc) * 1024);
      };
      loader_prefill<32, P1_DB, 5>(issue);
      loader_run<32, P1_DB, 5>(issue);
    }
    if (h == 0 && b < 112) {
      const int jj = b / 14, pn = b - 14 * jj, part = pn / 7, nf = pn - 7 * part;
      const rsrc_t rw = mkrs(ws + WS_WINR, 8 * 20 * 14336), rm = mkrs(ws + WS_MODP, 4 * 3840 * 4);
      u32x4 wf[20];
#pragma unroll
      for (int k = 0; k < 20; ++k) wf[k] = ld16<SC1>(rw, lane * 16, (unsigned)((((jj * 2 + part) * 20 + k) * 7 + nf) * 1024));
#pragma unroll
      for (int it = 0; it < 5; ++it) {
        const int c4 = (it * 64 + lane) * 4, isS = c4 >= DM, cc = c4 - DM * isS;
        const unsigned o = (unsigned)(((isS ? 3 : 4) * DM + cc) * 4);
        const f32x4 m0 = __builtin_bit_cast(f32x4, ld16<SC1>(rm, o, 0)), m1 = __builtin_bit_cast(f32x4, ld16<SC1>(rm, o, 3840 * 4)),
                    m2 = __builtin_bit_cast(f32x4, ld16<SC1>(rm, o, 2 * 3840 * 4)), m3 = __builtin_bit_cast(f32x4, ld16<SC1>(rm, o, 3 * 3840 * 4));
        f32x4 s = (m0 + m1) + (m2 + m3);
#pragma unroll
        for (int i = 0; i < 4; ++i) { s[i] = rbf(s[i]); if (!isS) s[i] = 1.0f + s[i]; }
        *(LAS u32x2*)(lds + L_TABE + (isS * DM + cc) * 2) = (u32x2){pk2(s[0], s[1]), pk2(s[2], s[3])};
      }
      wait_lgkm0(); CFENCE();
      f32x4 acc = {0.f, 0.f, 0.f, 0.f};
#pragma unroll
      for (int k = 0; k < 20; ++k) {
        u32x4 e = *(LAS u32x4*)(lds + L_TABE + ((l15 & 1) * DM + 32 * k + 8 * q) * 2);
        if (l15 >= 2) e = (u32x4){0u, 0u, 0u, 0u};
        acc = mfma(asfrag(e), asfrag(wf[k]), acc);
      }
      const rsrc_t ruv = mkrs(ws + WS_UV, 2 * 896 * 16);
      st16<SC1>((u32x4){__float_as_uint(acc[0]), __float_as_uint(acc[1]), (unsigned)T, (unsigned)(T >> 32)}, ruv,
                q == 0 ? (unsigned)((part * 896 + 112 * jj + 16 * nf + l15) * 16) : OOB, 0);
    }
    STAMP(3);
    idle_phase<21>();
    idle_phase<21>();
    {
      poll_flags(ws, WS_FLAG3 + j * 256, 32, T, lane);
      STAMP(4);
      const rsrc_t r = mkrs(ws + WS_Z2T + (size_t)j * 32 * 14336, 32 * 14336);
      auto issue = [&](int t, int slot) {
        const unsigned k = (unsigned)((t + rot) & 31) * 14336;
#pragma unroll
        for (int pc = 0; pc < 7; ++pc) dma16<SC1>(r, lds + P4_DOFF + slot * 14336 + (h * 7 + pc) * 1024, lane * 16, k + (h * 7 + pc) * 1024);
      };
      loader_prefill<32, P4_DB, 7>(issue);
      loader_run<32, P4_DB, 7>(issue);
    }
    idle_phase<15>();
  }
}

__global__ void k_report(const char* ws, int wv, int k) {
  __shared__ unsigned long long mn[256], mx[256];
  int t = threadIdx.x;
  mn[t] = *(const unsigned long long*)(ws + WS_STAMP + ((size_t)t * 8 + 0) * 256);
  mx[t] = *(const unsigned long long*)(ws + WS_STAMP + ((size_t)t * 8 + wv) * 256 + k * 8);
  __syncthreads();
  if (t == 0) { unsigned long long a = ~0ull, b = 0; for (int i = 0; i < 256; ++i) { a = mn[i] < a ? mn[i] : a; b = mx[i] > b ? mx[i] : b; }
    unsigned long long span = b - a; if (span > 20000) span = 20000;
    unsigned long long t0 = __builtin_amdgcn_s_memrealtime(); while (__builtin_amdgcn_s_memrealtime() - t0 < span * 10) __builtin_amdgcn_s_sleep(4); }
}
extern "C" void kernel_launch(void* const* d_in, const int* in_sizes, int n_in, void* d_out, int out_size, void* d_ws, size_t ws_size,
                              hipStream_t stream) {
  Params p;
  p.x = (const u16*)d_in[0]; p.wsre = (const u16*)d_in[1]; p.wsim = (const u16*)d_in[2]; p.wdre = (const u16*)d_in[3]; p.wdim = (const u16*)d_in[4];
  p.win = (const u16*)d_in[6]; p.wsp = (const u16*)d_in[7]; p.bsp = (const u16*)d_in[8]; p.wout = (const u16*)d_in[9];
  p.c = (const u16*)d_in[10]; p.wmod = (const u16*)d_in[11];
  p.out = (u16*)d_out; p.ws = (char*)d_ws;
  (void)hipFuncSetAttribute((const void*)adaLN_cond__spectral_fnet__mixer_gmlp__prompt_kernel, hipFuncAttributeMaxDynamicSharedMemorySize, LDS_BYTES);
  adaLN_cond__spectral_fnet__mixer_gmlp__prompt_kernel<<<256, 512, LDS_BYTES, stream>>>(p);
  k_report<<<1, 256, 0, stream>>>((const char*)d_ws, 0, 1);
}
```

```cpp
#define STAMPS
#include <hip/hip_runtime.h>
#include <hip/hip_bf16.h>

typedef unsigned short u16;
typedef unsigned long long u64;
typedef short bf16x8 __attribute__((ext_vector_type(8)));
typedef short s16x4 __attribute__((ext_vector_type(4)));
typedef __bf16 bf16x2 __attribute__((ext_vector_type(2)));
typedef float f32x2 __attribute__((ext_vector_type(2)));
typedef float f32x4 __attribute__((ext_vector_type(4)));
typedef unsigned u32x2 __attribute__((ext_vector_type(2)));
typedef unsigned u32x4 __attribute__((ext_vector_type(4)));
typedef __amdgpu_buffer_rsrc_t rsrc_t;
#define LAS __attribute__((address_space(3)))
#define GAS __attribute__((address_space(1)))
#define DEVI __device__ __forceinline__

constexpr int SEQ = 2048, DM = 640, FF = 1728, FH = 864;

constexpr unsigned WS_EPOCH = 0x0;
constexpr unsigned WS_TMO   = 0x100;
constexpr unsigned WS_FLAG0 = 0x1000;
constexpr unsigned WS_FLAGB = 0x2000;
constexpr unsigned WS_FLAG1 = 0x4000;
constexpr unsigned WS_FLAG2 = 0x6000;
constexpr unsigned WS_FLAG4 = 0x8000;
constexpr unsigned WS_FLAG3 = 0xA000;
constexpr unsigned WS_MODP  = 0x10000;
constexpr unsigned WS_UV    = 0x20000;
constexpr unsigned WS_ST1   = 0x30000;
constexpr unsigned WS_ST2   = 0x50000;
constexpr unsigned WS_XNT   = 0x100000;
constexpr unsigned WS_PQ    = 0x400000;
constexpr unsigned WS_X1C   = 0xA00000;
constexpr unsigned WS_Z2T   = 0xD00000;
constexpr unsigned WS_U     = 0x1100000;
constexpr unsigned WS_WINR  = 0x1500000;
constexpr unsigned WS_WOUTR = 0x1800000;
constexpr unsigned WS_Z1    = 0x1A00000;
constexpr unsigned WS_X1    = 0x2000000;
constexpr unsigned WS_STAMP = 0x2400000;
constexpr unsigned WS_USED  = 0x2500000;

constexpr int LDS_BYTES = 163840;
constexpr int L_MISC = 155648;
constexpr int L_CNT  = L_MISC;
constexpr int L_TAB  = L_MISC + 256;
constexpr int L_TABE = L_MISC + 2048;
constexpr int L_UV   = L_MISC + 4608;
constexpr int L_P0T  = 114688;
constexpr int L_RT   = 114688 + 12288;
constexpr int P0T_RS = 1296;

constexpr unsigned SPIN_MAX = 1u << 17;

struct Params {
  const u16 *x, *wsre, *wsim, *wdre, *wdim, *win, *wsp, *bsp, *wout, *c, *wmod;
  u16* out;
  char* ws;
};

DEVI rsrc_t mkrs(const void* p, unsigned bytes) { return __builtin_amdgcn_make_buffer_rsrc((void*)p, 0, (int)bytes, 0x00020000); }
template <int AUX> DEVI u32x4 ld16(rsrc_t r, unsigned v, unsigned s) { return __builtin_amdgcn_raw_buffer_load_b128(r, (int)v, (int)s, AUX); }
template <int AUX> DEVI u32x2 ld8(rsrc_t r, unsigned v, unsigned s) { return __builtin_amdgcn_raw_buffer_load_b64(r, (int)v, (int)s, AUX); }
template <int AUX> DEVI unsigned ld4(rsrc_t r, unsigned v, unsigned s) { return __builtin_amdgcn_raw_buffer_load_b32(r, (int)v, (int)s, AUX); }
template <int AUX> DEVI unsigned ld2(rsrc_t r, unsigned v, unsigned s) { return (unsigned)__builtin_amdgcn_raw_buffer_load_b16(r, (int)v, (int)s, AUX) & 0xffffu; }
template <int AUX> DEVI void st16(u32x4 d, rsrc_t r, unsigned v, unsigned s) { __builtin_amdgcn_raw_buffer_store_b128(d, r, (int)v, (int)s, AUX); }
template <int AUX> DEVI void st8(u32x2 d, rsrc_t r, unsigned v, unsigned s) { __builtin_amdgcn_raw_buffer_store_b64(d, r, (int)v, (int)s, AUX); }
template <int AUX> DEVI void dma16(rsrc_t r, LAS char* dst, unsigned v, unsigned s) { __builtin_amdgcn_raw_ptr_buffer_load_lds(r, (LAS void*)dst, 16, (int)v, (int)s, 0, AUX); }
constexpr int SC1 = 16;
constexpr unsigned OOB = 0x7ffffff0u;

template <int N> DEVI void wait_vm() { asm volatile("s_waitcnt vmcnt(%0)" ::"n"(N > 63 ? 63 : N) : "memory"); }
DEVI void wait_lgkm0() { __builtin_amdgcn_s_waitcnt(0xC07F); }
DEVI void BAR() { asm volatile("s_barrier" ::: "memory"); }
DEVI void CFENCE() { asm volatile("" ::: "memory"); }
#define SCHED() __builtin_amdgcn_sched_barrier(0)

DEVI float bflo(unsigned v) { return __uint_as_float(v << 16); }
DEVI float bfhi(unsigned v) { return __uint_as_float(v & 0xffff0000u); }
DEVI unsigned pk2(float lo, float hi) { f32x2 f = {lo, hi}; return __builtin_bit_cast(unsigned, __builtin_convertvector(f, bf16x2)); }
DEVI float rbf(float v) { return bflo(pk2(v, 0.f)); }
DEVI f32x4 mfma(bf16x8 a, bf16x8 b, f32x4 c) { return __builtin_amdgcn_mfma_f32_16x16x32_bf16(a, b, c, 0, 0, 0); }
DEVI bf16x8 asfrag(u32x4 v) { return __builtin_bit_cast(bf16x8, v); }
DEVI bf16x8 ldsfrag(LAS char* p) { return *(LAS bf16x8*)p; }

template <int CTRL> DEVI float dpp_add(float v) { return v + __int_as_float(__builtin_amdgcn_update_dpp(0, __float_as_int(v), CTRL, 0xf, 0xf, false)); }
DEVI float row16_sum(float v) { v = dpp_add<0x128>(v); v = dpp_add<0x124>(v); v = dpp_add<0x122>(v); return dpp_add<0x121>(v); }
DEVI float xq_sum(float v) { v += __shfl_xor(v, 16); v += __shfl_xor(v, 32); return v; }
DEVI float wave_sum(float v) { return xq_sum(row16_sum(v)); }

DEVI float gelu_tanh(float x) {
  const float u = x * (1.0f + 0.044715f * x * x) * (-2.0f * 0.7978845608028654f * 1.4426950408889634f);
  return x * __builtin_amdgcn_rcpf(1.0f + __builtin_amdgcn_exp2f(u));
}

DEVI u32x4 pair_swap(u32x2 a, u32x2 b) {
  auto s0 = __builtin_amdgcn_permlane16_swap(a.x, b.x, false, false);
  auto s1 = __builtin_amdgcn_permlane16_swap(a.y, b.y, false, false);
  return (u32x4){s0[0], s1[0], s0[1], s1[1]};
}

DEVI void note_timeout(char* ws, unsigned code, int lane) {
  if (lane == 0) __hip_atomic_store((GAS unsigned*)(ws + WS_TMO), code, __ATOMIC_RELAXED, __HIP_MEMORY_SCOPE_AGENT);
}
DEVI void poll_flags(char* ws, unsigned off, int n, u64 T, int lane) {
  for (unsigned spins = 0;; ++spins) {
    u64 f = T;
    if (lane < n) f = __hip_atomic_load((GAS u64*)(ws + off) + lane, __ATOMIC_RELAXED, __HIP_MEMORY_SCOPE_AGENT);
    if (__all(f == T)) break;
    if (spins > SPIN_MAX) { note_timeout(ws, 0x100u | (off >> 12), lane); break; }
    __builtin_amdgcn_s_sleep(1);
  }
  CFENCE();
}
DEVI void poll_flags256(char* ws, unsigned off, u64 T, int lane) {
  for (unsigned spins = 0;; ++spins) {
    bool ok = true;
#pragma unroll
    for (int i = 0; i < 4; ++i) ok &= __hip_atomic_load((GAS u64*)(ws + off) + i * 64 + lane, __ATOMIC_RELAXED, __HIP_MEMORY_SCOPE_AGENT) == T;
    if (__all(ok)) break;
    if (spins > SPIN_MAX) { note_timeout(ws, 0x200u | (off >> 12), lane); break; }
    __builtin_amdgcn_s_sleep(1);
  }
  CFENCE();
}
DEVI void set_flag(char* ws, unsigned off, u64 T, int lane) {
  if (lane == 0) __hip_atomic_store((GAS u64*)(ws + off), T, __ATOMIC_RELAXED, __HIP_MEMORY_SCOPE_AGENT);
}
DEVI bool arrive_last(LAS char* lds, int idx, unsigned n, int lane) {
  unsigned old = 0;
  if (lane == 0) old = __hip_atomic_fetch_add((LAS unsigned*)(lds + L_CNT) + idx, 1u, __ATOMIC_RELAXED, __HIP_MEMORY_SCOPE_WORKGROUP);
  return (unsigned)__builtin_amdgcn_readfirstlane(old) == n - 1;
}
DEVI void publish(LAS char* lds, int cidx, unsigned nwaves, char* ws, unsigned flag_off, u64 T, int lane) {
  wait_vm<0>();
  if (arrive_last(lds, cidx, nwaves, lane)) set_flag(ws, flag_off, T, lane);
}

#define LANE_IDS() int tz_ = threadIdx.x; asm volatile("" : "+v"(tz_)); const int lane = tz_ & 63, l15 = lane & 15, q = lane >> 4; (void)l15; (void)q

#define STAMP(k) do { if (((k) == 0 || (k) == 20) && lane == 0) *(GAS u64*)(p.ws + WS_STAMP + ((size_t)b * 8 + wid) * 256 + (k) * 8) = __builtin_amdgcn_s_memrealtime(); } while (0)
#define STAMPC(k) do { } while (0)

template <int I, int NT, int RING, int PC> struct LoaderTail {
  static DEVI void run() {
    if constexpr (I <= NT - 2) { wait_vm<(NT - I - 2) * PC>(); BAR(); LoaderTail<I + 1, NT, RING, PC>::run(); }
  }
};
template <int NT, int RING, int PC, class Issue> DEVI void loader_prefill(Issue issue) {
  constexpr int PRE = RING < NT ? RING : NT;
#pragma unroll 1
  for (int t = 0; t < PRE; ++t) { if (PRE * PC > 63) wait_vm<63 - PC>(); issue(t, t); }
}
template <int NT, int RING, int PC, class Issue> DEVI void loader_run(Issue issue) {
  constexpr int PRE = RING < NT ? RING : NT;
  wait_vm<(PRE - 1) * PC>(); BAR();
  if constexpr (NT > RING) {
    int slot = 0;
#pragma unroll 1
    for (int i = 0; i < NT - RING; ++i) {
      wait_vm<((RING - 2) * PC > 63 - PC ? 63 - PC : (RING - 2) * PC)>(); BAR();
      issue(i + RING, slot);
      slot = (slot + 1 == RING) ? 0 : slot + 1;
    }
  }
  LoaderTail<(NT > RING ? NT - RING : 0), NT, RING, PC>::run();
  BAR();
}
template <int NBAR> DEVI void idle_phase() {
#pragma unroll 1
  for (int i = 0; i < NBAR; ++i) BAR();
}

template <int I, int NMF, int NRD, int PER> struct Interleave {
  static DEVI void run() {
    if constexpr (I < NMF) {
      __builtin_amdgcn_sched_group_barrier(0x8, 1, 0);
      constexpr int left = NRD - I * PER, now = left < PER ? left : PER;
      if constexpr (now > 0) __builtin_amdgcn_sched_group_barrier(0x100, now, 0);
      Interleave<I + 1, NMF, NRD, PER>::run();
    }
  }
};
template <int NMF, int NRD, int PER> DEVI void interleave() { Interleave<0, NMF, NRD, PER>::run(); }

constexpr int P1_SA = 7, P1_DB = 4, P1_DOFF = 114688;
constexpr int P2_RING = 15;
constexpr int P3_RING = 10;
constexpr int P4_SA = 10, P4_DB = 5, P4_DOFF = 81920;
constexpr int P5_RING = 14;

__global__ __launch_bounds__(512, 2) void adaLN_cond__spectral_fnet__mixer_gmlp__prompt_kernel(Params p) {
  extern __shared__ __attribute__((aligned(16))) char smem_[];
  LAS char* lds = (LAS char*)smem_;
  const int tid = threadIdx.x, lane = tid & 63, wid = __builtin_amdgcn_readfirstlane(tid >> 6);
  const int b = blockIdx.x, xg = b & 7, i8 = b >> 3;
  const int g = xg * 4 + (i8 >> 3), j = i8 & 7;
  const int l15 = lane & 15, q = lane >> 4;
  char* const ws = p.ws;
  const int rot = xg * 4;

  STAMP(0); STAMPC(30);
  const u64 Ein = __hip_atomic_load((GAS u64*)(ws + WS_EPOCH), __ATOMIC_RELAXED, __HIP_MEMORY_SCOPE_AGENT);
  if (tid < 32) *((LAS unsigned*)(lds + L_CNT) + tid) = 0u;

  if (wid < 4) {
    const int w = wid;
    {
      const rsrc_t rx = mkrs(p.x, SEQ * DM * 2);
      u32x4 v[2][2];
#pragma unroll
      for (int r = 0; r < 2; ++r)
#pragma unroll
        for (int h = 0; h < 2; ++h) v[r][h] = ld16<0>(rx, lane < 40 ? (unsigned)((lane + 40 * h) * 16) : OOB, (unsigned)(64 * g + 8 * j + 2 * w + r) * (DM * 2));
      wait_lgkm0();
      BAR();
#pragma unroll
      for (int r = 0; r < 2; ++r) {
        float f[16];
#pragma unroll
        for (int h = 0; h < 2; ++h)
#pragma unroll
          for (int i = 0; i < 4; ++i) { f[h * 8 + 2 * i] = bflo(v[r][h][i]); f[h * 8 + 2 * i + 1] = bfhi(v[r][h][i]); }
        float s = 0.f;
#pragma unroll
        for (int i = 0; i < 16; ++i) s += f[i];
        const float mean = wave_sum(s) * (1.0f / DM);
        float m2 = 0.f;
#pragma unroll
        for (int i = 0; i < 16; ++i) { f[i] -= mean; m2 += f[i] * f[i]; }
        if (lane >= 40) m2 = 0.f;
        const float rstd = 1.0f / sqrtf(wave_sum(m2) * (1.0f / DM) + 1e-5f);
        if (lane < 40) {
#pragma unroll
          for (int h = 0; h < 2; ++h) {
            u32x4 o;
#pragma unroll
            for (int i = 0; i < 4; ++i) o[i] = pk2(f[h * 8 + 2 * i] * rstd, f[h * 8 + 2 * i + 1] * rstd);
            *(LAS u32x4*)(lds + L_P0T + (2 * w + r) * P0T_RS + (lane + 40 * h) * 16) = o;
          }
        }
      }
      wait_lgkm0(); CFENCE();
      if (lane == 0) __hip_atomic_fetch_add((LAS unsigned*)(lds + L_CNT) + 0, 1u, __ATOMIC_RELAXED, __HIP_MEMORY_SCOPE_WORKGROUP);
      for (unsigned spins = 0; spins < SPIN_MAX; ++spins) {
        if (__hip_atomic_load((LAS unsigned*)(lds + L_CNT) + 0, __ATOMIC_RELAXED, __HIP_MEMORY_SCOPE_WORKGROUP) >= 4u) break;
      }
      CFENCE();
      const rsrc_t rxn = mkrs(ws + WS_XNT, 8 * 32 * 10240);
#pragma unroll
      for (int it = 0; it < 3; ++it) {
        const int pc = 4 * it + q, nf = 10 * w + (pc < 10 ? pc : 9);
        const int qq = l15 >> 2, pp = l15 & 3;
        LAS char* a0 = lds + L_P0T + qq * P0T_RS + (16 * nf + 4 * pp) * 2;
        const u32x2 lo = __builtin_bit_cast(u32x2, __builtin_amdgcn_ds_read_tr16_b64_v4i16((LAS s16x4*)a0));
        const u32x2 hi = __builtin_bit_cast(u32x2, __builtin_amdgcn_ds_read_tr16_b64_v4i16((LAS s16x4*)(a0 + 4 * P0T_RS)));
        const int jj = nf / 5, nfl = nf - 5 * jj;
        const unsigned off = (unsigned)(((jj * 32 + g) * 10 + (j >> 2) * 5 + nfl) * 1024 + ((j & 3) * 16 + l15) * 16);
        st16<SC1>((u32x4){lo.x, lo.y, hi.x, hi.y}, rxn, pc < 10 ? off : OOB, 0);
      }
    }
    const u64 T = (((u64)(unsigned)__builtin_amdgcn_readfirstlane((unsigned)(Ein >> 32)) << 32) | (unsigned)__builtin_amdgcn_readfirstlane((unsigned)Ein)) + 1;
    publish(lds, 1, 4, ws, WS_FLAG0 + (g * 8 + j) * 8, T, lane);
    STAMP(1);

    {
      LANE_IDS();
      f32x4 acc[2][5];
#pragma unroll
      for (int m = 0; m < 2; ++m)
#pragma unroll
        for (int n = 0; n < 5; ++n) acc[m][n] = (f32x4){0.f, 0.f, 0.f, 0.f};
      float rs[2] = {0.f, 0.f};
      unsigned ones = 0x3f803f80u; asm volatile("" : "+v"(ones));
      const unsigned aoff0 = (w * 16 + l15) * 128 + ((q ^ (l15 >> 1)) << 4), aoff1 = aoff0 ^ 64;
      const unsigned boff = P1_DOFF + lane * 16;
      bf16x8 XA[2], XB[5], YA[2], YB[5];
      auto rd = [&](bf16x8(&A)[2], bf16x8(&B)[5], int sa, int sb, int kk) {
        LAS char* pa = lds + sa * 16384 + (kk ? aoff1 : aoff0);
        LAS char* pb = lds + sb * 10240 + boff + kk * 5120;
#pragma unroll
        for (int n = 0; n < 5; ++n) B[n] = ldsfrag(pb + n * 1024);
        A[0] = ldsfrag(pa); A[1] = ldsfrag(pa + 8192);
      };
      auto mm = [&](const bf16x8(&A)[2], const bf16x8(&B)[5]) {
#pragma unroll
        for (int m = 0; m < 2; ++m) {
#pragma unroll
          for (int n = 0; n < 5; ++n) acc[m][n] = mfma(B[n], A[m], acc[m][n]);
          const u32x4 au = __builtin_bit_cast(u32x4, A[m]);
#pragma unroll
          for (int i = 0; i < 4; ++i) { const unsigned ai = au[i]; asm("v_dot2c_f32_bf16 %0, %1, %2" : "+v"(rs[m]) : "v"(ai), "v"(ones)); }
        }
      };
      BAR();
      STAMP(2); STAMPC(28);
      rd(XA, XB, 0, 0, 0); wait_lgkm0();
      int sa = 0, sb = 0;
      auto steps = [&](int n) {
#pragma unroll 1
        for (int i = 0; i < n; ++i) {
          rd(YA, YB, sa, sb, 1); mm(XA, XB); interleave<10, 7, 1>(); SCHED(); wait_lgkm0();
          BAR();
          sa = (sa + 1 == P1_SA) ? 0 : sa + 1; sb = (sb + 1 == P1_DB) ? 0 : sb + 1;
          rd(XA, XB, sa, sb, 0); mm(YA, YB); interleave<10, 7, 1>(); SCHED(); wait_lgkm0();
        }
      };
      steps(8);
      u64 fb[4];
#pragma unroll
      for (int i = 0; i < 4; ++i) fb[i] = __hip_atomic_load((GAS u64*)(ws + WS_FLAGB) + i * 64 + lane, __ATOMIC_RELAXED, __HIP_MEMORY_SCOPE_AGENT);
      steps(8);
      if (!__all(fb[0] == T && fb[1] == T && fb[2] == T && fb[3] == T)) poll_flags256(ws, WS_FLAGB, T, lane);
      CFENCE();
      f32x4 mp[2][4];
      const rsrc_t rm = mkrs(ws + WS_MODP, 4 * 3840 * 4);
      const int vsel = lane < 32 ? (w == 0 ? 1 : w == 1 ? 0 : w == 2 ? 2 : 4) : 5;
      const bool vact = (lane < 20) || (w == 0 && lane >= 32 && lane < 52);
#pragma unroll
      for (int k = 0; k < 4; ++k) mp[0][k] = __builtin_bit_cast(f32x4, ld16<SC1>(rm, vact ? (unsigned)((vsel * DM + 80 * j + (lane & 31) * 4) * 4) : OOB, k * 3840 * 4));
      steps(16);
      STAMP(3); STAMPC(29);
      {
        f32x4 s = (mp[0][0] + mp[0][1]) + (mp[0][2] + mp[0][3]);
        const bool plus1 = (vsel == 1) || (vsel == 4);
#pragma unroll
        for (int i = 0; i < 4; ++i) { s[i] = rbf(s[i]); if (plus1) s[i] = rbf(1.0f + s[i]); }
        const int trow = lane < 32 ? w : 4;
        if (vact) *(LAS f32x4*)(lds + L_TAB + (trow * 80 + (lane & 31) * 4) * 4) = s;
        wait_lgkm0(); CFENCE();
        if (lane == 0) __hip_atomic_fetch_add((LAS unsigned*)(lds + L_CNT) + 2, 1u, __ATOMIC_RELAXED, __HIP_MEMORY_SCOPE_WORKGROUP);
        for (unsigned spins = 0; spins < SPIN_MAX; ++spins) {
          if (__hip_atomic_load((LAS unsigned*)(lds + L_CNT) + 2, __ATOMIC_RELAXED, __HIP_MEMORY_SCOPE_WORKGROUP) >= 4u) break;
        }
        CFENCE();
      }
      rs[0] = xq_sum(rs[0]); rs[1] = -xq_sum(rs[1]);
      u32x2 pkd[2][5];
#pragma unroll
      for (int n = 0; n < 5; ++n) {
        const f32x4 c1 = *(LAS f32x4*)(lds + L_TAB + (0 * 80 + 16 * n + 4 * q) * 4), sa4 = *(LAS f32x4*)(lds + L_TAB + (1 * 80 + 16 * n + 4 * q) * 4);
        const f32x4 pv = acc[0][n] * c1 + rs[0] * sa4, qv = rs[1] * sa4 - acc[1][n] * c1;
        pkd[0][n] = (u32x2){pk2(pv[0], pv[1]), pk2(pv[2], pv[3])};
        pkd[1][n] = (u32x2){pk2(qv[0], qv[1]), pk2(qv[2], qv[3])};
      }
      const rsrc_t rpq = mkrs(ws + WS_PQ, 32 * 40 * 4 * 1024);
      auto pq_off = [&](int m, int nf) { const int c = 10 * j + 2 * nf + (q >> 1); return (unsigned)(((g * 40 + m * 20 + (c >> 2)) * 4 + w) * 1024 + ((c & 3) * 16 + l15) * 16); };
      const int odd = q & 1;
      st16<SC1>(pair_swap(pkd[0][0], pkd[0][1]), rpq, pq_off(0, odd), 0);
      st16<SC1>(pair_swap(pkd[0][2], pkd[0][3]), rpq, pq_off(0, 2 + odd), 0);
      st16<SC1>(pair_swap(pkd[1][0], pkd[1][1]), rpq, pq_off(1, odd), 0);
      st16<SC1>(pair_swap(pkd[1][2], pkd[1][3]), rpq, pq_off(1, 2 + odd), 0);
      st16<SC1>(pair_swap(pkd[0][4], pkd[1][4]), rpq, pq_off(odd, 4), 0);
      publish(lds, 3, 4, ws, WS_FLAG1 + g * 256 + j * 8, T, lane);
      STAMP(4);
    }

    {
      LANE_IDS();
      constexpr int NT = 20, W = 6;
      const rsrc_t rx = mkrs(p.x, SEQ * DM * 2);
      u32x2 xin[5];
#pragma unroll
      for (int n = 0; n < 5; ++n) xin[n] = ld8<0>(rx, (unsigned)((64 * g + 16 * w + l15) * (DM * 2) + (80 * j + 16 * n + 4 * q) * 2), 0);
      poll_flags(ws, WS_FLAG1 + g * 256, 8, T, lane);
      STAMP(5);
      const rsrc_t ra = mkrs(ws + WS_PQ + (size_t)g * 40 * 4 * 1024, 40 * 4 * 1024);
      u32x4 A[NT][2];
#pragma unroll
      for (int t = 0; t < W; ++t) { A[t][0] = ld16<SC1>(ra, lane * 16, ((2 * t) * 4 + w) * 1024); A[t][1] = ld16<SC1>(ra, lane * 16, ((2 * t + 1) * 4 + w) * 1024); }
      f32x4 acc[5];
#pragma unroll
      for (int n = 0; n < 5; ++n) acc[n] = (f32x4){0.f, 0.f, 0.f, 0.f};
      const unsigned boff0 = l15 * 128 + ((q ^ (l15 >> 1)) << 4), boff1 = boff0 ^ 64;
      bf16x8 XB[5], YB[5];
      auto rd = [&](bf16x8(&B)[5], int slot, int kk) {
        LAS char* pb = lds + slot * 10240 + (kk ? boff1 : boff0);
#pragma unroll
        for (int n = 0; n < 5; ++n) B[n] = ldsfrag(pb + n * 2048);
      };
      BAR();
      STAMP(6);
      rd(XB, 0, 0); wait_lgkm0();
#pragma unroll
      for (int t = 0; t < NT; ++t) {
        if (t + W < NT) { A[t + W][0] = ld16<SC1>(ra, lane * 16, ((2 * (t + W)) * 4 + w) * 1024); A[t + W][1] = ld16<SC1>(ra, lane * 16, ((2 * (t + W) + 1) * 4 + w) * 1024); }
        rd(YB, t % P2_RING, 1);
#pragma unroll
        for (int n = 0; n < 5; ++n) acc[n] = mfma(XB[n], asfrag(A[t][0]), acc[n]);
        interleave<5, 5, 1>(); SCHED(); wait_lgkm0();
        BAR();
        rd(XB, (t + 1) % P2_RING, 0);
#pragma unroll
        for (int n = 0; n < 5; ++n) acc[n] = mfma(YB[n], asfrag(A[t][1]), acc[n]);
        interleave<5, 5, 1>(); SCHED(); wait_lgkm0();
      }
      STAMP(7);
      float x1f[5][4]; float s = 0.f;
      u32x2 cp[5], x1p[5];
      const rsrc_t rx1 = mkrs(ws + WS_X1 + (size_t)b * 4 * 5 * 512, 4 * 5 * 512);
#pragma unroll
      for (int n = 0; n < 5; ++n) {
        const f32x4 ga = *(LAS f32x4*)(lds + L_TAB + (2 * 80 + 16 * n + 4 * q) * 4), cb1 = *(LAS f32x4*)(lds + L_TAB + (3 * 80 + 16 * n + 4 * q) * 4);
        const float x0 = bflo(xin[n].x), x1_ = bfhi(xin[n].x), x2 = bflo(xin[n].y), x3 = bfhi(xin[n].y);
        x1p[n] = (u32x2){pk2(x0 + ga[0] * acc[n][0], x1_ + ga[1] * acc[n][1]), pk2(x2 + ga[2] * acc[n][2], x3 + ga[3] * acc[n][3])};
        st8<0>(x1p[n], rx1, lane * 8, (w * 5 + n) * 512);
        x1f[n][0] = bflo(x1p[n].x); x1f[n][1] = bfhi(x1p[n].x); x1f[n][2] = bflo(x1p[n].y); x1f[n][3] = bfhi(x1p[n].y);
        cp[n] = (u32x2){pk2(x1f[n][0] * cb1[0], x1f[n][1] * cb1[1]), pk2(x1f[n][2] * cb1[2], x1f[n][3] * cb1[3])};
        s += (x1f[n][0] + x1f[n][1]) + (x1f[n][2] + x1f[n][3]);
      }
      const float mean = xq_sum(s) * (1.0f / 80.0f);
      float m2 = 0.f;
#pragma unroll
      for (int n = 0; n < 5; ++n)
#pragma unroll
        for (int r = 0; r < 4; ++r) { const float d = x1f[n][r] - mean; m2 += d * d; }
      m2 = xq_sum(m2);
      const rsrc_t rc = mkrs(ws + WS_X1C, 32 * 20 * 4 * 1024);
      auto c_off = [&](int nf) { const int c = 10 * j + 2 * nf + (q >> 1); return (unsigned)(((g * 20 + (c >> 2)) * 4 + w) * 1024 + ((c & 3) * 16 + l15) * 16); };
      const int odd = q & 1;
      st16<SC1>(pair_swap(cp[0], cp[1]), rc, c_off(odd), 0);
      st16<SC1>(pair_swap(cp[2], cp[3]), rc, c_off(2 + odd), 0);
      st8<SC1>(cp[4], rc, c_off(4) + odd * 8, 0);
      const rsrc_t rs1 = mkrs(ws + WS_ST1, SEQ * 8 * 8);
      st8<SC1>((u32x2){__float_as_uint(mean), __float_as_uint(m2)}, rs1, q == 0 ? (unsigned)(((64 * g + 16 * w + l15) * 8 + j) * 8) : OOB, 0);
      publish(lds, 4, 4, ws, WS_FLAG2 + g * 256 + j * 8, T, lane);
      STAMP(8);
    }

    {
      LANE_IDS();
      constexpr int NT = 10;
      {
        const rsrc_t ruv = mkrs(ws + WS_UV, 2 * 896 * 16);
        const int col = w * 56 + lane, part = col >= 112 ? 1 : 0, cc = col - 112 * part;
        const unsigned off = lane < 56 ? (unsigned)((part * 896 + 112 * j + cc) * 16) : OOB;
        u32x4 gv;
        for (unsigned spins = 0;; ++spins) {
          gv = ld16<SC1>(ruv, off, 0);
          const bool ok = lane >= 56 || (gv.z == (unsigned)T && gv.w == (unsigned)(T >> 32));
          if (__all(ok)) break;
          if (spins > SPIN_MAX) { note_timeout(ws, 0x300, lane); break; }
          __builtin_amdgcn_s_sleep(1); CFENCE();
        }
        if (lane < 56) { *(LAS unsigned*)(lds + L_UV + col * 4) = gv.x; *(LAS unsigned*)(lds + L_UV + (224 + col) * 4) = gv.y; }
      }
      poll_flags(ws, WS_FLAG2 + g * 256, 8, T, lane);
      STAMP(9);
      const rsrc_t ra = mkrs(ws + WS_X1C + (size_t)g * 20 * 4 * 1024, 20 * 4 * 1024);
      u32x4 A[20];
#pragma unroll
      for (int k = 0; k < 20; ++k) A[k] = ld16<SC1>(ra, lane * 16, (k * 4 + w) * 1024);
      const rsrc_t rs1 = mkrs(ws + WS_ST1, SEQ * 8 * 8);
      const u32x4 st = ld16<SC1>(rs1, (unsigned)(((64 * g + 16 * w + l15) * 8 + 2 * q) * 8), 0);
      f32x4 acc[7];
      bf16x8 XB[7], YB[7];
      auto rd = [&](bf16x8(&B)[7], int slot, int kk) {
        LAS char* pb = lds + slot * 14336 + kk * 7168 + lane * 16;
#pragma unroll
        for (int n = 0; n < 7; ++n) B[n] = ldsfrag(pb + n * 1024);
      };
      wait_lgkm0();
      BAR();
      STAMP(10);
      rd(XB, 0, 0); wait_lgkm0();
      float mean1, rstd1;
      {
        const float ma = __uint_as_float(st.x), mb = __uint_as_float(st.z);
        float m = 0.5f * (ma + mb), d = ma - mb, M2 = __uint_as_float(st.y) + __uint_as_float(st.w) + d * d * 40.0f;
        float mo = __shfl_xor(m, 16), Mo = __shfl_xor(M2, 16); d = m - mo; M2 = M2 + Mo + d * d * 80.0f; m = 0.5f * (m + mo);
        mo = __shfl_xor(m, 32); Mo = __shfl_xor(M2, 32); d = m - mo; M2 = M2 + Mo + d * d * 160.0f; m = 0.5f * (m + mo);
        mean1 = m; rstd1 = 1.0f / sqrtf(M2 * (1.0f / DM) + 1e-5f);
      }
#pragma unroll
      for (int n = 0; n < 7; ++n) acc[n] = (f32x4){0.f, 0.f, 0.f, 0.f};
#pragma unroll
      for (int t = 0; t < NT; ++t) {
        rd(YB, t % P3_RING, 1);
#pragma unroll
        for (int n = 0; n < 7; ++n) acc[n] = mfma(asfrag(A[2 * t]), XB[n], acc[n]);
        interleave<7, 7, 2>(); SCHED(); wait_lgkm0();
        BAR();
        rd(XB, (t + 1) % P3_RING, 0);
#pragma unroll
        for (int n = 0; n < 7; ++n) acc[n] = mfma(asfrag(A[2 * t + 1]), YB[n], acc[n]);
        interleave<7, 7, 2>(); SCHED(); wait_lgkm0();
      }
      u32x2 z2p[7];
      const int nval = (j == 7) ? 5 : 7;
      {
        float mr[4], rr[4];
#pragma unroll
        for (int r = 0; r < 4; ++r) { mr[r] = __shfl(mean1, 4 * q + r); rr[r] = __shfl(rstd1, 4 * q + r); }
        float zs[4] = {0.f, 0.f, 0.f, 0.f};
#pragma unroll
        for (int n = 0; n < 7; ++n) {
          const float u = *(LAS float*)(lds + L_UV + (16 * n + l15) * 4), v = *(LAS float*)(lds + L_UV + (224 + 16 * n + l15) * 4);
          float z[4];
#pragma unroll
          for (int r = 0; r < 4; ++r) z[r] = gelu_tanh(rr[r] * (acc[n][r] - mr[r] * u) + v);
          z2p[n] = (u32x2){pk2(z[0], z[1]), pk2(z[2], z[3])};
          if (n < nval) { zs[0] += bflo(z2p[n].x); zs[1] += bfhi(z2p[n].x); zs[2] += bflo(z2p[n].y); zs[3] += bfhi(z2p[n].y); }
        }
        const float inv = 1.0f / (16.0f * nval);
        float mz[4], M2[4];
#pragma unroll
        for (int r = 0; r < 4; ++r) { mz[r] = row16_sum(zs[r]) * inv; M2[r] = 0.f; }
#pragma unroll
        for (int n = 0; n < 7; ++n) if (n < nval) {
          const float d0 = bflo(z2p[n].x) - mz[0], d1 = bfhi(z2p[n].x) - mz[1], d2 = bflo(z2p[n].y) - mz[2], d3 = bfhi(z2p[n].y) - mz[3];
          M2[0] += d0 * d0; M2[1] += d1 * d1; M2[2] += d2 * d2; M2[3] += d3 * d3;
        }
#pragma unroll
        for (int r = 0; r < 4; ++r) M2[r] = row16_sum(M2[r]);
        const float ms = l15 == 0 ? mz[0] : l15 == 1 ? mz[1] : l15 == 2 ? mz[2] : mz[3];
        const float Ms = l15 == 0 ? M2[0] : l15 == 1 ? M2[1] : l15 == 2 ? M2[2] : M2[3];
        const rsrc_t rs2 = mkrs(ws + WS_ST2, SEQ * 8 * 16);
        st16<SC1>((u32x4){__float_as_uint(ms), __float_as_uint(Ms), (unsigned)T, (unsigned)(T >> 32)}, rs2,
                  l15 < 4 ? (unsigned)(((64 * g + 16 * w + 4 * q + l15) * 8 + j) * 16) : OOB, 0);
      }
      STAMP(11);
#pragma unroll
      for (int n = 0; n < 7; ++n) acc[n] = (f32x4){0.f, 0.f, 0.f, 0.f};
#pragma unroll
      for (int t = 0; t < NT; ++t) {
        rd(YB, (NT + t) % P3_RING, 1);
#pragma unroll
        for (int n = 0; n < 7; ++n) acc[n] = mfma(XB[n], asfrag(A[2 * t]), acc[n]);
        interleave<7, 7, 2>(); SCHED(); wait_lgkm0();
        BAR();
        rd(XB, (NT + t + 1) % P3_RING, 0);
#pragma unroll
        for (int n = 0; n < 7; ++n) acc[n] = mfma(YB[n], asfrag(A[2 * t + 1]), acc[n]);
        interleave<7, 7, 2>(); SCHED(); wait_lgkm0();
      }
      STAMP(12);
      const rsrc_t rs2 = mkrs(ws + WS_ST2, SEQ * 8 * 16);
      u32x4 ga_, gb_;
      const unsigned soff = (unsigned)(((64 * g + 16 * w + l15) * 8 + 2 * q) * 16);
      ga_ = ld16<SC1>(rs2, soff, 0); gb_ = ld16<SC1>(rs2, soff + 16, 0);
      {
        const rsrc_t rz = mkrs(ws + WS_Z1 + (size_t)b * 4 * 7 * 512, 4 * 7 * 512);
#pragma unroll
        for (int n = 0; n < 7; ++n) {
          const f32x4 u = *(LAS f32x4*)(lds + L_UV + (112 + 16 * n + 4 * q) * 4), v = *(LAS f32x4*)(lds + L_UV + (224 + 112 + 16 * n + 4 * q) * 4);
          float z[4];
#pragma unroll
          for (int r = 0; r < 4; ++r) z[r] = gelu_tanh(rstd1 * (acc[n][r] - mean1 * u[r]) + v[r]);
          st8<0>((u32x2){pk2(z[0], z[1]), pk2(z[2], z[3])}, rz, lane * 8, (w * 7 + n) * 512);
        }
      }
      for (unsigned spins = 0;; ++spins) {
        const bool ok = ga_.z == (unsigned)T && ga_.w == (unsigned)(T >> 32) && gb_.z == (unsigned)T && gb_.w == (unsigned)(T >> 32);
        if (__all(ok)) break;
        if (spins > SPIN_MAX) { note_timeout(ws, 0x400, lane); break; }
        __builtin_amdgcn_s_sleep(1); CFENCE();
        ga_ = ld16<SC1>(rs2, soff, 0); gb_ = ld16<SC1>(rs2, soff + 16, 0);
      }
      float mean2, rstd2;
      {
        const float na = 112.0f, nb = (q == 3) ? 80.0f : 112.0f;
        float ma = __uint_as_float(ga_.x), mb = __uint_as_float(gb_.x);
        float n = na + nb, d = mb - ma, m = ma + d * (nb / n), M2 = __uint_as_float(ga_.y) + __uint_as_float(gb_.y) + d * d * (na * nb / n);
#pragma unroll
        for (int sft = 16; sft <= 32; sft <<= 1) {
          const float no = __shfl_xor(n, sft), mo = __shfl_xor(m, sft), Mo = __shfl_xor(M2, sft);
          const float nt = n + no; d = mo - m;
          M2 = M2 + Mo + d * d * (n * no / nt);
          m = (n * m + no * mo) / nt; n = nt;
        }
        mean2 = m; rstd2 = 1.0f / sqrtf(M2 * (1.0f / FH) + 1e-5f);
      }
      {
        float mr[4], rr[4];
#pragma unroll
        for (int r = 0; r < 4; ++r) { mr[r] = __shfl(mean2, 4 * q + r); rr[r] = __shfl(rstd2, 4 * q + r); }
        u32x2 zn[7];
#pragma unroll
        for (int n = 0; n < 7; ++n)
          zn[n] = (u32x2){pk2((bflo(z2p[n].x) - mr[0]) * rr[0], (bfhi(z2p[n].x) - mr[1]) * rr[1]), pk2((bflo(z2p[n].y) - mr[2]) * rr[2], (bfhi(z2p[n].y) - mr[3]) * rr[3])};
        const rsrc_t rz2 = mkrs(ws + WS_Z2T, 8 * 32 * 14336);
        auto z_off = [&](int nf) { return (unsigned)(((j * 32 + g) * 14 + (w >> 1) * 7 + nf) * 1024 + ((2 * (w & 1) + (q >> 1)) * 16 + l15) * 16); };
        const int odd = q & 1;
        st16<SC1>(pair_swap(zn[0], zn[1]), rz2, z_off(odd), 0);
        st16<SC1>(pair_swap(zn[2], zn[3]), rz2, z_off(2 + odd), 0);
        st16<SC1>(pair_swap(zn[4], zn[5]), rz2, z_off(4 + odd), 0);
        st8<SC1>(zn[6], rz2, z_off(6) + odd * 8, 0);
      }
      publish(lds, 5, 4, ws, WS_FLAG3 + j * 256 + g * 8, T, lane);
      STAMP(13);
    }

    {
      LANE_IDS();
      const int mh = w >> 1, nh = w & 1;
      f32x4 acc[2][4];
#pragma unroll
      for (int m = 0; m < 2; ++m)
#pragma unroll
        for (int n = 0; n < 4; ++n) acc[m][n] = (f32x4){0.f, 0.f, 0.f, 0.f};
      const unsigned aoff0 = (mh * 32 + l15) * 128 + ((q ^ (l15 >> 1)) << 4), aoff1 = aoff0 ^ 64;
      const unsigned boff = P4_DOFF + nh * 4096 + lane * 16;
      const rsrc_t rb = mkrs(p.bsp, SEQ * 2);
      const float bs0 = bflo(ld2<0>(rb, (64 * g + 32 * mh + l15) * 2, 0)), bs1 = bflo(ld2<0>(rb, (64 * g + 32 * mh + 16 + l15) * 2, 0));
      bf16x8 XA[2], XB[4], YA[2], YB[4];
      if (nh == 0) {
        auto rd = [&](bf16x8(&A)[2], bf16x8(&B)[4], int sa, int sb, int kk) {
          LAS char* pa = lds + sa * 8192 + (kk ? aoff1 : aoff0);
          LAS char* pb = lds + sb * 14336 + boff + kk * 7168;
#pragma unroll
          for (int n = 0; n < 4; ++n) B[n] = ldsfrag(pb + n * 1024);
          A[0] = ldsfrag(pa); A[1] = ldsfrag(pa + 2048);
        };
        auto mm = [&](const bf16x8(&A)[2], const bf16x8(&B)[4]) {
#pragma unroll
          for (int m = 0; m < 2; ++m)
#pragma unroll
            for (int n = 0; n < 4; ++n) acc[m][n] = mfma(B[n], A[m], acc[m][n]);
        };
        BAR();
        STAMP(14);
        rd(XA, XB, 0, 0, 0); wait_lgkm0();
        int sa = 0, sb = 0;
#pragma unroll 1
        for (int i = 0; i < 32; ++i) {
          rd(YA, YB, sa, sb, 1); mm(XA, XB); interleave<8, 6, 1>(); SCHED(); wait_lgkm0();
          BAR();
          sa = (sa + 1 == P4_SA) ? 0 : sa + 1; sb = (sb + 1 == P4_DB) ? 0 : sb + 1;
          rd(XA, XB, sa, sb, 0); mm(YA, YB); interleave<8, 6, 1>(); SCHED(); wait_lgkm0();
        }
      } else {
        auto rd = [&](bf16x8(&A)[2], bf16x8(&B)[4], int sa, int sb, int kk) {
          LAS char* pa = lds + sa * 8192 + (kk ? aoff1 : aoff0);
          LAS char* pb = lds + sb * 14336 + boff + kk * 7168;
#pragma unroll
          for (int n = 0; n < 3; ++n) B[n] = ldsfrag(pb + n * 1024);
          A[0] = ldsfrag(pa); A[1] = ldsfrag(pa + 2048);
        };
        auto mm = [&](const bf16x8(&A)[2], const bf16x8(&B)[4]) {
#pragma unroll
          for (int m = 0; m < 2; ++m)
#pragma unroll
            for (int n = 0; n < 3; ++n) acc[m][n] = mfma(B[n], A[m], acc[m][n]);
        };
        BAR();
        STAMP(14);
        rd(XA, XB, 0, 0, 0); wait_lgkm0();
        int sa = 0, sb = 0;
#pragma unroll 1
        for (int i = 0; i < 32; ++i) {
          rd(YA, YB, sa, sb, 1); mm(XA, XB); interleave<6, 5, 1>(); SCHED(); wait_lgkm0();
          BAR();
          sa = (sa + 1 == P4_SA) ? 0 : sa + 1; sb = (sb + 1 == P4_DB) ? 0 : sb + 1;
          rd(XA, XB, sa, sb, 0); mm(YA, YB); interleave<6, 5, 1>(); SCHED(); wait_lgkm0();
        }
      }
      STAMP(15);
      const rsrc_t rz = mkrs(ws + WS_Z1 + (size_t)b * 4 * 7 * 512, 4 * 7 * 512);
      u32x2 up[2][4];
#pragma unroll
      for (int m = 0; m < 2; ++m)
#pragma unroll
        for (int n = 0; n < 4; ++n) {
          const int nf = 4 * nh + n;
          const u32x2 z = ld8<SC1>(rz, nf < 7 ? (unsigned)(lane * 8 + ((2 * mh + m) * 7 + nf) * 512) : OOB, 0);
          const float bs = m ? bs1 : bs0;
          up[m][n] = (u32x2){pk2(bflo(z.x) * rbf(rbf(acc[m][n][0]) + bs), bfhi(z.x) * rbf(rbf(acc[m][n][1]) + bs)),
                             pk2(bflo(z.y) * rbf(rbf(acc[m][n][2]) + bs), bfhi(z.y) * rbf(rbf(acc[m][n][3]) + bs))};
        }
      const rsrc_t ru = mkrs(ws + WS_U, 32 * 28 * 4 * 1024);
      auto u_off = [&](int mf, int nf) { const int c = 14 * j + 2 * nf + (q >> 1); return (unsigned)(((g * 28 + (c >> 2)) * 4 + mf) * 1024 + ((c & 3) * 16 + l15) * 16); };
      const int odd = q & 1;
      if (nh == 0) {
#pragma unroll
        for (int m = 0; m < 2; ++m) {
          st16<SC1>(pair_swap(up[m][0], up[m][1]), ru, u_off(2 * mh + m, odd), 0);
          st16<SC1>(pair_swap(up[m][2], up[m][3]), ru, u_off(2 * mh + m, 2 + odd), 0);
        }
      } else {
#pragma unroll
        for (int m = 0; m < 2; ++m) st16<SC1>(pair_swap(up[m][0], up[m][1]), ru, u_off(2 * mh + m, 4 + odd), 0);
        st16<SC1>(pair_swap(up[0][2], up[1][2]), ru, u_off(2 * mh + odd, 6), 0);
      }
      publish(lds, 6, 4, ws, WS_FLAG4 + g * 256 + j * 8, T, lane);
      if (b == 0 && w == 0 && lane == 0) __hip_atomic_store((GAS u64*)(ws + WS_EPOCH), T, __ATOMIC_RELAXED, __HIP_MEMORY_SCOPE_AGENT);
      STAMP(16);
    }

    {
      LANE_IDS();
      constexpr int NT = 14, W = 7;
      const rsrc_t rx1 = mkrs(ws + WS_X1 + (size_t)b * 4 * 5 * 512, 4 * 5 * 512);
      u32x2 x1p[5];
#pragma unroll
      for (int n = 0; n < 5; ++n) x1p[n] = ld8<SC1>(rx1, lane * 8, (w * 5 + n) * 512);
      poll_flags(ws, WS_FLAG4 + g * 256, 8, T, lane);
      STAMP(17);
      const rsrc_t ra = mkrs(ws + WS_U + (size_t)g * 28 * 4 * 1024, 28 * 4 * 1024);
      u32x4 A[NT][2];
#pragma unroll
      for (int t = 0; t < W; ++t) { A[t][0] = ld16<SC1>(ra, lane * 16, ((2 * t) * 4 + w) * 1024); A[t][1] = ld16<SC1>(ra, lane * 16, ((2 * t + 1) * 4 + w) * 1024); }
      f32x4 acc[5];
#pragma unroll
      for (int n = 0; n < 5; ++n) acc[n] = (f32x4){0.f, 0.f, 0.f, 0.f};
      bf16x8 XB[5], YB[5];
      auto rd = [&](bf16x8(&B)[5], int slot, int kk) {
        LAS char* pb = lds + slot * 10240 + kk * 5120 + lane * 16;
#pragma unroll
        for (int n = 0; n < 5; ++n) B[n] = ldsfrag(pb + n * 1024);
      };
      BAR();
      STAMP(18);
      rd(XB, 0, 0); wait_lgkm0();
#pragma unroll
      for (int t = 0; t < NT; ++t) {
        if (t + W < NT) { A[t + W][0] = ld16<SC1>(ra, lane * 16, ((2 * (t + W)) * 4 + w) * 1024); A[t + W][1] = ld16<SC1>(ra, lane * 16, ((2 * (t + W) + 1) * 4 + w) * 1024); }
        rd(YB, t, 1);
#pragma unroll
        for (int n = 0; n < 5; ++n) acc[n] = mfma(XB[n], asfrag(A[t][0]), acc[n]);
        interleave<5, 5, 1>(); SCHED(); wait_lgkm0();
        BAR();
        rd(XB, t + 1 < NT ? t + 1 : 0, 0);
#pragma unroll
        for (int n = 0; n < 5; ++n) acc[n] = mfma(YB[n], asfrag(A[t][1]), acc[n]);
        interleave<5, 5, 1>(); SCHED(); wait_lgkm0();
      }
      STAMP(19);
      u32x2 op[5];
#pragma unroll
      for (int n = 0; n < 5; ++n) {
        const f32x4 gb = *(LAS f32x4*)(lds + L_TAB + (4 * 80 + 16 * n + 4 * q) * 4);
        op[n] = (u32x2){pk2(bflo(x1p[n].x) + rbf(gb[0] * rbf(acc[n][0])), bfhi(x1p[n].x) + rbf(gb[1] * rbf(acc[n][1]))),
                        pk2(bflo(x1p[n].y) + rbf(gb[2] * rbf(acc[n][2])), bfhi(x1p[n].y) + rbf(gb[3] * rbf(acc[n][3])))};
      }
      const rsrc_t ro = mkrs(p.out, SEQ * DM * 2);
      const unsigned rowb = (unsigned)((64 * g + 16 * w + l15) * (DM * 2) + 80 * j * 2);
      const int odd = q & 1;
      st16<0>(pair_swap(op[0], op[1]), ro, rowb + (16 * odd + 8 * (q >> 1)) * 2, 0);
      st16<0>(pair_swap(op[2], op[3]), ro, rowb + (16 * (2 + odd) + 8 * (q >> 1)) * 2, 0);
      st8<0>(op[4], ro, rowb + (64 + 4 * q) * 2, 0);
      STAMP(20); STAMPC(31);
    }
  } else if (wid < 6) {
    const int h = wid - 4;
    const unsigned sw0 = (unsigned)(((lane & 7) ^ (lane >> 4)) * 16), sw1 = sw0 ^ 64;
    wait_lgkm0();
    BAR();
    {
      const rsrc_t r = mkrs((h ? p.wsim : p.wsre) + (size_t)g * 64 * SEQ, 64 * SEQ * 2);
      const unsigned v0 = (lane >> 3) * 4096 + sw0, v1 = (lane >> 3) * 4096 + sw1;
      auto issue = [&](int t, int slot) {
        const unsigned k = (unsigned)((t + rot) & 31) * 128;
#pragma unroll
        for (int pc = 0; pc < 8; ++pc) dma16<0>(r, lds + slot * 16384 + (h * 8 + pc) * 1024, (pc & 1) ? v1 : v0, k + pc * 32768);
      };
      loader_prefill<32, P1_SA, 8>(issue);
      STAMP(1);
      loader_run<32, P1_SA, 8>(issue);
    }
    {
      const unsigned v0 = (lane >> 3) * 1280 + (h ? sw1 : sw0), v1 = (lane >> 3) * 1280 + (h ? sw0 : sw1);
      auto issue = [&](int t, int slot) {
        const int im = t >= 10;
        const rsrc_t r = mkrs((im ? p.wdim : p.wdre) + (size_t)j * 80 * DM, 80 * DM * 2);
        const unsigned k = (unsigned)(t - 10 * im) * 128 + h * 5 * 8 * 1280;
#pragma unroll
        for (int pc = 0; pc < 5; ++pc) dma16<0>(r, lds + slot * 10240 + (h * 5 + pc) * 1024, (pc & 1) ? v1 : v0, k + pc * 8 * 1280);
      };
      loader_prefill<20, P2_RING, 5>(issue);
      STAMP(2);
      loader_run<20, P2_RING, 5>(issue);
    }
    {
      const rsrc_t r = mkrs(ws + WS_WINR + (size_t)j * 20 * 14336, 20 * 14336);
      auto issue = [&](int t, int slot) {
#pragma unroll
        for (int pc = 0; pc < 7; ++pc) dma16<SC1>(r, lds + slot * 14336 + (h * 7 + pc) * 1024, lane * 16, (unsigned)(t * 14336 + (h * 7 + pc) * 1024));
      };
      loader_prefill<20, P3_RING, 7>(issue);
      STAMP(3);
      loader_run<20, P3_RING, 7>(issue);
    }
    {
      const rsrc_t r = mkrs(p.wsp + (size_t)g * 64 * SEQ, 64 * SEQ * 2);
      const unsigned v0 = (lane >> 3) * 4096 + sw0, v1 = (lane >> 3) * 4096 + sw1;
      auto issue = [&](int t, int slot) {
        const unsigned k = (unsigned)((t + rot) & 31) * 128 + h * 4 * 32768;
#pragma unroll
        for (int pc = 0; pc < 4; ++pc) dma16<0>(r, lds + slot * 8192 + (h * 4 + pc) * 1024, (pc & 1) ? v1 : v0, k + pc * 32768);
      };
      loader_prefill<32, P4_SA, 4>(issue);
      STAMP(4);
      loader_run<32, P4_SA, 4>(issue);
    }
    {
      const rsrc_t r = mkrs(ws + WS_WOUTR + (size_t)j * 14 * 10240, 14 * 10240);
      auto issue = [&](int t, int slot) {
#pragma unroll
        for (int pc = 0; pc < 5; ++pc) dma16<SC1>(r, lds + slot * 10240 + (h * 5 + pc) * 1024, lane * 16, (unsigned)(t * 10240 + (h * 5 + pc) * 1024));
      };
      loader_prefill<14, P5_RING, 5>(issue);
      STAMP(5);
      loader_run<14, P5_RING, 5>(issue);
    }
  } else {
    const int h = wid - 6;
    wait_lgkm0();
    BAR();
    if (h == 0 && b < 240) {
      const int cg = b % 60, kq = b / 60;
      const rsrc_t rw = mkrs(p.wmod, DM * 3840 * 2), rc = mkrs(p.c, DM * 2);
      u32x4 wv[20]; unsigned cv[20];
#pragma unroll
      for (int it = 0; it < 20; ++it) {
        const int row = kq * 160 + it * 8 + (lane >> 3);
        wv[it] = ld16<0>(rw, (unsigned)(row * 7680 + cg * 128 + (lane & 7) * 16), 0);
        cv[it] = ld2<0>(rc, row * 2, 0);
      }
      float a8[8] = {0.f, 0.f, 0.f, 0.f, 0.f, 0.f, 0.f, 0.f};
#pragma unroll
      for (int it = 0; it < 20; ++it) {
        const float cf = bflo(cv[it]);
        const float sl = rbf(cf * __builtin_amdgcn_rcpf(1.0f + __builtin_amdgcn_exp2f(-1.4426950408889634f * cf)));
#pragma unroll
        for (int i = 0; i < 4; ++i) { a8[2 * i] += sl * bflo(wv[it][i]); a8[2 * i + 1] += sl * bfhi(wv[it][i]); }
      }
#pragma unroll
      for (int i = 0; i < 8; ++i) { a8[i] += __shfl_xor(a8[i], 8); a8[i] += __shfl_xor(a8[i], 16); a8[i] += __shfl_xor(a8[i], 32); }
      const rsrc_t rm = mkrs(ws + WS_MODP, 4 * 3840 * 4);
      const unsigned mo = (unsigned)((kq * 3840 + cg * 64 + (lane & 7) * 8) * 4);
      st16<SC1>((u32x4){__float_as_uint(a8[0]), __float_as_uint(a8[1]), __float_as_uint(a8[2]), __float_as_uint(a8[3])}, rm, lane < 8 ? mo : OOB, 0);
      st16<SC1>((u32x4){__float_as_uint(a8[4]), __float_as_uint(a8[5]), __float_as_uint(a8[6]), __float_as_uint(a8[7])}, rm, lane < 8 ? mo + 16 : OOB, 0);
    }
    {
      LAS char* sc = lds + L_RT + h * 7168;
#pragma unroll 1
      for (int rep = 0; rep < 2; ++rep) {
        int u;
        if (h == 1) u = b + 256 * rep; else u = (rep == 0 && b < 32) ? 512 + b : 1000;
        if (u >= 544) continue;
        const int qq = l15 >> 2, pp = l15 & 3;
        if (u < 320) {
          const int jp = u / 20, kf = u - 20 * jp, jj = jp >> 1, part = jp & 1;
          const rsrc_t r = mkrs(p.win, DM * FF * 2);
#pragma unroll
          for (int pc = 0; pc < 7; ++pc) {
            const int idx = pc * 64 + lane, row = idx / 14, ch = idx - 14 * row;
            const bool ok = 112 * jj + 8 * ch < FH;
            dma16<0>(r, sc + pc * 1024, ok ? (unsigned)((32 * kf + row) * (FF * 2) + (part ? 0 : FH * 2) + 224 * jj + ch * 16) : OOB, 0);
          }
          wait_vm<0>(); CFENCE();
          const rsrc_t ro = mkrs(ws + WS_WINR, 8 * 20 * 14336);
#pragma unroll
          for (int nf = 0; nf < 7; ++nf) {
            LAS char* a0 = sc + (8 * q + qq) * 224 + (16 * nf + 4 * pp) * 2;
            const u32x2 lo = __builtin_bit_cast(u32x2, __builtin_amdgcn_ds_read_tr16_b64_v4i16((LAS s16x4*)a0));
            const u32x2 hi = __builtin_bit_cast(u32x2, __builtin_amdgcn_ds_read_tr16_b64_v4i16((LAS s16x4*)(a0 + 4 * 224)));
            st16<SC1>((u32x4){lo.x, lo.y, hi.x, hi.y}, ro, (unsigned)((((jj * 2 + part) * 20 + kf) * 7 + nf) * 1024 + lane * 16), 0);
          }
        } else {
          const int uu = u - 320, jj = uu / 28, kf = uu - 28 * jj;
          const rsrc_t r = mkrs(p.wout, FH * DM * 2);
#pragma unroll
          for (int pc = 0; pc < 5; ++pc) {
            const int idx = pc * 64 + lane, row = idx / 10, ch = idx - 10 * row;
            dma16<0>(r, sc + pc * 1024, (unsigned)((32 * kf + row) * (DM * 2) + 160 * jj + ch * 16), 0);
          }
          wait_vm<0>(); CFENCE();
          const rsrc_t ro = mkrs(ws + WS_WOUTR, 8 * 14 * 10240);
#pragma unroll
          for (int nf = 0; nf < 5; ++nf) {
            LAS char* a0 = sc + (8 * q + qq) * 160 + (16 * nf + 4 * pp) * 2;
            const u32x2 lo = __builtin_bit_cast(u32x2, __builtin_amdgcn_ds_read_tr16_b64_v4i16((LAS s16x4*)a0));
            const u32x2 hi = __builtin_bit_cast(u32x2, __builtin_amdgcn_ds_read_tr16_b64_v4i16((LAS s16x4*)(a0 + 4 * 160)));
            st16<SC1>((u32x4){lo.x, lo.y, hi.x, hi.y}, ro, (unsigned)(((jj * 28 + kf) * 5 + nf) * 1024 + lane * 16), 0);
          }
        }
        wait_lgkm0(); CFENCE();
      }
    }
    const u64 T = (((u64)(unsigned)__builtin_amdgcn_readfirstlane((unsigned)(Ein >> 32)) << 32) | (unsigned)__builtin_amdgcn_readfirstlane((unsigned)Ein)) + 1;
    publish(lds, 7, 2, ws, WS_FLAGB + (g * 8 + j) * 8, T, lane);
    STAMP(1);
    {
      poll_flags256(ws, WS_FLAG0, T, lane);
      STAMP(2);
      const rsrc_t r = mkrs(ws + WS_XNT + (size_t)j * 32 * 10240, 32 * 10240);
      auto issue = [&](int t, int slot) {
        const unsigned k = (unsigned)((t + rot) & 31) * 10240;
#pragma unroll
        for (int pc = 0; pc < 5; ++pc) dma16<SC1>(r, lds + P1_DOFF + slot * 10240 + (h * 5 + pc) * 1024, lane * 16, k + (h * 5 + pc) * 1024);
      };
      loader_prefill<32, P1_DB, 5>(issue);
      loader_run<32, P1_DB, 5>(issue);
    }
    if (h == 0 && b < 112) {
      const int jj = b / 14, pn = b - 14 * jj, part = pn / 7, nf = pn - 7 * part;
      const rsrc_t rw = mkrs(ws + WS_WINR, 8 * 20 * 14336), rm = mkrs(ws + WS_MODP, 4 * 3840 * 4);
      u32x4 wf[20];
#pragma unroll
      for (int k = 0; k < 20; ++k) wf[k] = ld16<SC1>(rw, lane * 16, (unsigned)((((jj * 2 + part) * 20 + k) * 7 + nf) * 1024));
#pragma unroll
      for (int it = 0; it < 5; ++it) {
        const int c4 = (it * 64 + lane) * 4, isS = c4 >= DM, cc = c4 - DM * isS;
        const unsigned o = (unsigned)(((isS ? 3 : 4) * DM + cc) * 4);
        const f32x4 m0 = __builtin_bit_cast(f32x4, ld16<SC1>(rm, o, 0)), m1 = __builtin_bit_cast(f32x4, ld16<SC1>(rm, o, 3840 * 4)),
                    m2 = __builtin_bit_cast(f32x4, ld16<SC1>(rm, o, 2 * 3840 * 4)), m3 = __builtin_bit_cast(f32x4, ld16<SC1>(rm, o, 3 * 3840 * 4));
        f32x4 s = (m0 + m1) + (m2 + m3);
#pragma unroll
        for (int i = 0; i < 4; ++i) { s[i] = rbf(s[i]); if (!isS) s[i] = 1.0f + s[i]; }
        *(LAS u32x2*)(lds + L_TABE + (isS * DM + cc) * 2) = (u32x2){pk2(s[0], s[1]), pk2(s[2], s[3])};
      }
      wait_lgkm0(); CFENCE();
      f32x4 acc = {0.f, 0.f, 0.f, 0.f};
#pragma unroll
      for (int k = 0; k < 20; ++k) {
        u32x4 e = *(LAS u32x4*)(lds + L_TABE + ((l15 & 1) * DM + 32 * k + 8 * q) * 2);
        if (l15 >= 2) e = (u32x4){0u, 0u, 0u, 0u};
        acc = mfma(asfrag(e), asfrag(wf[k]), acc);
      }
      const rsrc_t ruv = mkrs(ws + WS_UV, 2 * 896 * 16);
      st16<SC1>((u32x4){__float_as_uint(acc[0]), __float_as_uint(acc[1]), (unsigned)T, (unsigned)(T >> 32)}, ruv,
                q == 0 ? (unsigned)((part * 896 + 112 * jj + 16 * nf + l15) * 16) : OOB, 0);
    }
    STAMP(3);
    idle_phase<21>();
    idle_phase<21>();
    {
      poll_flags(ws, WS_FLAG3 + j * 256, 32, T, lane);
      STAMP(4);
      const rsrc_t r = mkrs(ws + WS_Z2T + (size_t)j * 32 * 14336, 32 * 14336);
      auto issue = [&](int t, int slot) {
        const unsigned k = (unsigned)((t + rot) & 31) * 14336;
#pragma unroll
        for (int pc = 0; pc < 7; ++pc) dma16<SC1>(r, lds + P4_DOFF + slot * 14336 + (h * 7 + pc) * 1024, lane * 16, k + (h * 7 + pc) * 1024);
      };
      loader_prefill<32, P4_DB, 7>(issue);
      loader_run<32, P4_DB, 7>(issue);
    }
    idle_phase<15>();
  }
}

__global__ void k_mark(unsigned long long* p) { if (threadIdx.x == 0) p[0] = __builtin_amdgcn_s_memrealtime(); }
__global__ void k_report(const char* ws) {
  __shared__ unsigned long long mn[256], mx[256];
  const unsigned long long tnow = __builtin_amdgcn_s_memrealtime();
  int t = threadIdx.x;
  mn[t] = *(const unsigned long long*)(ws + WS_STAMP + ((size_t)t * 8 + 0) * 256);
  mx[t] = *(const unsigned long long*)(ws + WS_STAMP + ((size_t)t * 8 + 0) * 256 + 20 * 8);
  __syncthreads();
  if (t == 0) { unsigned long long a = ~0ull, b = 0; for (int i = 0; i < 256; ++i) { a = mn[i] < a ? mn[i] : a; b = mx[i] > b ? mx[i] : b; }
    const unsigned long long mk = *(const unsigned long long*)(ws + WS_STAMP + 0x90000);
    unsigned long long span = 0; if (span > 20000) span = 20000;
    unsigned long long t0 = __builtin_amdgcn_s_memrealtime(); while (__builtin_amdgcn_s_memrealtime() - t0 < span * 50) __builtin_amdgcn_s_sleep(4); }
}
extern "C" void kernel_launch(void* const* d_in, const int* in_sizes, int n_in, void* d_out, int out_size, void* d_ws, size_t ws_size,
                              hipStream_t stream) {
  Params p;
  p.x = (const u16*)d_in[0]; p.wsre = (const u16*)d_in[1]; p.wsim = (const u16*)d_in[2]; p.wdre = (const u16*)d_in[3]; p.wdim = (const u16*)d_in[4];
  p.win = (const u16*)d_in[6]; p.wsp = (const u16*)d_in[7]; p.bsp = (const u16*)d_in[8]; p.wout = (const u16*)d_in[9];
  p.c = (const u16*)d_in[10]; p.wmod = (const u16*)d_in[11];
  p.out = (u16*)d_out; p.ws = (char*)d_ws;
  (void)hipFuncSetAttribute((const void*)adaLN_cond__spectral_fnet__mixer_gmlp__prompt_kernel, hipFuncAttributeMaxDynamicSharedMemorySize, LDS_BYTES);
  adaLN_cond__spectral_fnet__mixer_gmlp__prompt_kernel<<<256, 512, LDS_BYTES, stream>>>(p);
  k_report<<<1, 256, 0, stream>>>((const char*)d_ws);
}
```

```cpp
#define STAMPS
#include <hip/hip_runtime.h>
#include <hip/hip_bf16.h>

typedef unsigned short u16;
typedef unsigned long long u64;
typedef short bf16x8 __attribute__((ext_vector_type(8)));
typedef short s16x4 __attribute__((ext_vector_type(4)));
typedef __bf16 bf16x2 __attribute__((ext_vector_type(2)));
typedef float f32x2 __attribute__((ext_vector_type(2)));
typedef float f32x4 __attribute__((ext_vector_type(4)));
typedef unsigned u32x2 __attribute__((ext_vector_type(2)));
typedef unsigned u32x4 __attribute__((ext_vector_type(4)));
typedef __amdgpu_buffer_rsrc_t rsrc_t;
#define LAS __attribute__((address_space(3)))
#define GAS __attribute__((address_space(1)))
#define DEVI __device__ __forceinline__

constexpr int SEQ = 2048, DM = 640, FF = 1728, FH = 864;

constexpr unsigned WS_EPOCH = 0x0;
constexpr unsigned WS_TMO   = 0x100;
constexpr unsigned WS_FLAG0 = 0x1000;
constexpr unsigned WS_FLAGB = 0x2000;
constexpr unsigned WS_FLAG1 = 0x4000;
constexpr unsigned WS_FLAG2 = 0x6000;
constexpr unsigned WS_FLAG4 = 0x8000;
constexpr unsigned WS_FLAG3 = 0xA000;
constexpr unsigned WS_MODP  = 0x10000;
constexpr unsigned WS_UV    = 0x20000;
constexpr unsigned WS_ST1   = 0x30000;
constexpr unsigned WS_ST2   = 0x50000;
constexpr unsigned WS_XNT   = 0x100000;
constexpr unsigned WS_PQ    = 0x400000;
constexpr unsigned WS_X1C   = 0xA00000;
constexpr unsigned WS_Z2T   = 0xD00000;
constexpr unsigned WS_U     = 0x1100000;
constexpr unsigned WS_WINR  = 0x1500000;
constexpr unsigned WS_WOUTR = 0x1800000;
constexpr unsigned WS_Z1    = 0x1A00000;
constexpr unsigned WS_X1    = 0x2000000;
constexpr unsigned WS_STAMP = 0x2400000;
constexpr unsigned WS_USED  = 0x2500000;

constexpr int LDS_BYTES = 163840;
constexpr int L_MISC = 155648;
constexpr int L_CNT  = L_MISC;
constexpr int L_TAB  = L_MISC + 256;
constexpr int L_TABE = L_MISC + 2048;
constexpr int L_UV   = L_MISC + 4608;
constexpr int L_P0T  = 114688;
constexpr int L_RT   = 114688 + 12288;
constexpr int P0T_RS = 1296;

constexpr unsigned SPIN_MAX = 1u << 17;

struct Params {
  const u16 *x, *wsre, *wsim, *wdre, *wdim, *win, *wsp, *bsp, *wout, *c, *wmod;
  u16* out;
  char* ws;
};

DEVI rsrc_t mkrs(const void* p, unsigned bytes) { return __builtin_amdgcn_make_buffer_rsrc((void*)p, 0, (int)bytes, 0x00020000); }
template <int AUX> DEVI u32x4 ld16(rsrc_t r, unsigned v, unsigned s) { return __builtin_amdgcn_raw_buffer_load_b128(r, (int)v, (int)s, AUX); }
template <int AUX> DEVI u32x2 ld8(rsrc_t r, unsigned v, unsigned s) { return __builtin_amdgcn_raw_buffer_load_b64(r, (int)v, (int)s, AUX); }
template <int AUX> DEVI unsigned ld4(rsrc_t r, unsigned v, unsigned s) { return __builtin_amdgcn_raw_buffer_load_b32(r, (int)v, (int)s, AUX); }
template <int AUX> DEVI unsigned ld2(rsrc_t r, unsigned v, unsigned s) { return (unsigned)__builtin_amdgcn_raw_buffer_load_b16(r, (int)v, (int)s, AUX) & 0xffffu; }
template <int AUX> DEVI void st16(u32x4 d, rsrc_t r, unsigned v, unsigned s) { __builtin_amdgcn_raw_buffer_store_b128(d, r, (int)v, (int)s, AUX); }
template <int AUX> DEVI void st8(u32x2 d, rsrc_t r, unsigned v, unsigned s) { __builtin_amdgcn_raw_buffer_store_b64(d, r, (int)v, (int)s, AUX); }
template <int AUX> DEVI void dma16(rsrc_t r, LAS char* dst, unsigned v, unsigned s) { __builtin_amdgcn_raw_ptr_buffer_load_lds(r, (LAS void*)dst, 16, (int)v, (int)s, 0, AUX); }
constexpr int SC1 = 16;
constexpr unsigned OOB = 0x7ffffff0u;

template <int N> DEVI void wait_vm() { asm volatile("s_waitcnt vmcnt(%0)" ::"n"(N > 63 ? 63 : N) : "memory"); }
DEVI void wait_lgkm0() { __builtin_amdgcn_s_waitcnt(0xC07F); }
DEVI void BAR() { asm volatile("s_barrier" ::: "memory"); }
DEVI void CFENCE() { asm volatile("" ::: "memory"); }
#define SCHED() __builtin_amdgcn_sched_barrier(0)

DEVI float bflo(unsigned v) { return __uint_as_float(v << 16); }
DEVI float bfhi(unsigned v) { return __uint_as_float(v & 0xffff0000u); }
DEVI unsigned pk2(float lo, float hi) { f32x2 f = {lo, hi}; return __builtin_bit_cast(unsigned, __builtin_convertvector(f, bf16x2)); }
DEVI float rbf(float v) { return bflo(pk2(v, 0.f)); }
DEVI f32x4 mfma(bf16x8 a, bf16x8 b, f32x4 c) { return __builtin_amdgcn_mfma_f32_16x16x32_bf16(a, b, c, 0, 0, 0); }
DEVI bf16x8 asfrag(u32x4 v) { return __builtin_bit_cast(bf16x8, v); }
DEVI bf16x8 ldsfrag(LAS char* p) { return *(LAS bf16x8*)p; }

template <int CTRL> DEVI float dpp_add(float v) { return v + __int_as_float(__builtin_amdgcn_update_dpp(0, __float_as_int(v), CTRL, 0xf, 0xf, false)); }
DEVI float row16_sum(float v) { v = dpp_add<0x128>(v); v = dpp_add<0x124>(v); v = dpp_add<0x122>(v); return dpp_add<0x121>(v); }
DEVI float xq_sum(float v) { v += __shfl_xor(v, 16); v += __shfl_xor(v, 32); return v; }
DEVI float wave_sum(float v) { return xq_sum(row16_sum(v)); }

DEVI float gelu_tanh(float x) {
  const float u = x * (1.0f + 0.044715f * x * x) * (-2.0f * 0.7978845608028654f * 1.4426950408889634f);
  return x * __builtin_amdgcn_rcpf(1.0f + __builtin_amdgcn_exp2f(u));
}

DEVI u32x4 pair_swap(u32x2 a, u32x2 b) {
  auto s0 = __builtin_amdgcn_permlane16_swap(a.x, b.x, false, false);
  auto s1 = __builtin_amdgcn_permlane16_swap(a.y, b.y, false, false);
  return (u32x4){s0[0], s1[0], s0[1], s1[1]};
}

DEVI void note_timeout(char* ws, unsigned code, int lane) {
  if (lane == 0) __hip_atomic_store((GAS unsigned*)(ws + WS_TMO), code, __ATOMIC_RELAXED, __HIP_MEMORY_SCOPE_AGENT);
}
DEVI void poll_flags(char* ws, unsigned off, int n, u64 T, int lane) {
  for (unsigned spins = 0;; ++spins) {
    u64 f = T;
    if (lane < n) f = __hip_atomic_load((GAS u64*)(ws + off) + lane, __ATOMIC_RELAXED, __HIP_MEMORY_SCOPE_AGENT);
    if (__all(f == T)) break;
    if (spins > SPIN_MAX) { note_timeout(ws, 0x100u | (off >> 12), lane); break; }
    __builtin_amdgcn_s_sleep(1);
  }
  CFENCE();
}
DEVI void poll_flags256(char* ws, unsigned off, u64 T, int lane) {
  for (unsigned spins = 0;; ++spins) {
    bool ok = true;
#pragma unroll
    for (int i = 0; i < 4; ++i) ok &= __hip_atomic_load((GAS u64*)(ws + off) + i * 64 + lane, __ATOMIC_RELAXED, __HIP_MEMORY_SCOPE_AGENT) == T;
    if (__all(ok)) break;
    if (spins > SPIN_MAX) { note_timeout(ws, 0x200u | (off >> 12), lane); break; }
    __builtin_amdgcn_s_sleep(1);
  }
  CFENCE();
}
DEVI void set_flag(char* ws, unsigned off, u64 T, int lane) {
  if (lane == 0) __hip_atomic_store((GAS u64*)(ws + off), T, __ATOMIC_RELAXED, __HIP_MEMORY_SCOPE_AGENT);
}
DEVI bool arrive_last(LAS char* lds, int idx, unsigned n, int lane) {
  unsigned old = 0;
  if (lane == 0) old = __hip_atomic_fetch_add((LAS unsigned*)(lds + L_CNT) + idx, 1u, __ATOMIC_RELAXED, __HIP_MEMORY_SCOPE_WORKGROUP);
  return (unsigned)__builtin_amdgcn_readfirstlane(old) == n - 1;
}
DEVI void publish(LAS char* lds, int cidx, unsigned nwaves, char* ws, unsigned flag_off, u64 T, int lane) {
  wait_vm<0>();
  if (arrive_last(lds, cidx, nwaves, lane)) set_flag(ws, flag_off, T, lane);
}

#define LANE_IDS() int tz_ = threadIdx.x; asm volatile("" : "+v"(tz_)); const int lane = tz_ & 63, l15 = lane & 15, q = lane >> 4; (void)l15; (void)q

#define STAMP(k) do { if (((k) == 20) && lane == 0) *(GAS u64*)(p.ws + WS_STAMP + ((size_t)b * 8 + wid) * 256 + (k) * 8) = __builtin_amdgcn_s_memrealtime(); } while (0)
#define STAMPC(k) do { } while (0)

template <int I, int NT, int RING, int PC> struct LoaderTail {
  static DEVI void run() {
    if constexpr (I <= NT - 2) { wait_vm<(NT - I - 2) * PC>(); BAR(); LoaderTail<I + 1, NT, RING, PC>::run(); }
  }
};
template <int NT, int RING, int PC, class Issue> DEVI void loader_prefill(Issue issue) {
  constexpr int PRE = RING < NT ? RING : NT;
#pragma unroll 1
  for (int t = 0; t < PRE; ++t) { if (PRE * PC > 63) wait_vm<63 - PC>(); issue(t, t); }
}
template <int NT, int RING, int PC, class Issue> DEVI void loader_run(Issue issue) {
  constexpr int PRE = RING < NT ? RING : NT;
  wait_vm<(PRE - 1) * PC>(); BAR();
  if constexpr (NT > RING) {
    int slot = 0;
#pragma unroll 1
    for (int i = 0; i < NT - RING; ++i) {
      wait_vm<((RING - 2) * PC > 63 - PC ? 63 - PC : (RING - 2) * PC)>(); BAR();
      issue(i + RING, slot);
      slot = (slot + 1 == RING) ? 0 : slot + 1;
    }
  }
  LoaderTail<(NT > RING ? NT - RING : 0), NT, RING, PC>::run();
  BAR();
}
template <int NBAR> DEVI void idle_phase() {
#pragma unroll 1
  for (int i = 0; i < NBAR; ++i) BAR();
}

template <int I, int NMF, int NRD, int PER> struct Interleave {
  static DEVI void run() {
    if constexpr (I < NMF) {
      __builtin_amdgcn_sched_group_barrier(0x8, 1, 0);
      constexpr int left = NRD - I * PER, now = left < PER ? left : PER;
      if constexpr (now > 0) __builtin_amdgcn_sched_group_barrier(0x100, now, 0);
      Interleave<I + 1, NMF, NRD, PER>::run();
    }
  }
};
template <int NMF, int NRD, int PER> DEVI void interleave() { Interleave<0, NMF, NRD, PER>::run(); }

constexpr int P1_SA = 7, P1_DB = 4, P1_DOFF = 114688;
constexpr int P2_RING = 15;
constexpr int P3_RING = 10;
constexpr int P4_SA = 10, P4_DB = 5, P4_DOFF = 81920;
constexpr int P5_RING = 14;

__global__ __launch_bounds__(512, 2) void adaLN_cond__spectral_fnet__mixer_gmlp__prompt_kernel(Params p) {
  unsigned long long t_entry; asm volatile("s_memrealtime %0\n\ts_waitcnt lgkmcnt(0)" : "=s"(t_entry));
  extern __shared__ __attribute__((aligned(16))) char smem_[];
  LAS char* lds = (LAS char*)smem_;
  { char* w0 = p.ws; asm volatile("" : "+s"(w0)); unsigned long long t_args; asm volatile("s_memrealtime %0\n\ts_waitcnt lgkmcnt(0)" : "=s"(t_args) : "s"(w0));
    if (threadIdx.x == 0) { *(GAS u64*)(w0 + WS_STAMP + (size_t)blockIdx.x * 8 * 256) = t_entry; *(GAS u64*)(w0 + WS_STAMP + (size_t)blockIdx.x * 8 * 256 + 8) = t_args; } }
  const int tid = threadIdx.x, lane = tid & 63, wid = __builtin_amdgcn_readfirstlane(tid >> 6);
  const int b = blockIdx.x, xg = b & 7, i8 = b >> 3;
  const int g = xg * 4 + (i8 >> 3), j = i8 & 7;
  const int l15 = lane & 15, q = lane >> 4;
  char* const ws = p.ws;
  const int rot = xg * 4;

  STAMP(0); STAMPC(30);
  const u64 Ein = __hip_atomic_load((GAS u64*)(ws + WS_EPOCH), __ATOMIC_RELAXED, __HIP_MEMORY_SCOPE_AGENT);
  if (tid < 32) *((LAS unsigned*)(lds + L_CNT) + tid) = 0u;

  if (wid < 4) {
    const int w = wid;
    {
      const rsrc_t rx = mkrs(p.x, SEQ * DM * 2);
      u32x4 v[2][2];
#pragma unroll
      for (int r = 0; r < 2; ++r)
#pragma unroll
        for (int h = 0; h < 2; ++h) v[r][h] = ld16<0>(rx, lane < 40 ? (unsigned)((lane + 40 * h) * 16) : OOB, (unsigned)(64 * g + 8 * j + 2 * w + r) * (DM * 2));
      wait_lgkm0();
      BAR();
#pragma unroll
      for (int r = 0; r < 2; ++r) {
        float f[16];
#pragma unroll
        for (int h = 0; h < 2; ++h)
#pragma unroll
          for (int i = 0; i < 4; ++i) { f[h * 8 + 2 * i] = bflo(v[r][h][i]); f[h * 8 + 2 * i + 1] = bfhi(v[r][h][i]); }
        float s = 0.f;
#pragma unroll
        for (int i = 0; i < 16; ++i) s += f[i];
        const float mean = wave_sum(s) * (1.0f / DM);
        float m2 = 0.f;
#pragma unroll
        for (int i = 0; i < 16; ++i) { f[i] -= mean; m2 += f[i] * f[i]; }
        if (lane >= 40) m2 = 0.f;
        const float rstd = 1.0f / sqrtf(wave_sum(m2) * (1.0f / DM) + 1e-5f);
        if (lane < 40) {
#pragma unroll
          for (int h = 0; h < 2; ++h) {
            u32x4 o;
#pragma unroll
            for (int i = 0; i < 4; ++i) o[i] = pk2(f[h * 8 + 2 * i] * rstd, f[h * 8 + 2 * i + 1] * rstd);
            *(LAS u32x4*)(lds + L_P0T + (2 * w + r) * P0T_RS + (lane + 40 * h) * 16) = o;
          }
        }
      }
      wait_lgkm0(); CFENCE();
      if (lane == 0) __hip_atomic_fetch_add((LAS unsigned*)(lds + L_CNT) + 0, 1u, __ATOMIC_RELAXED, __HIP_MEMORY_SCOPE_WORKGROUP);
      for (unsigned spins = 0; spins < SPIN_MAX; ++spins) {
        if (__hip_atomic_load((LAS unsigned*)(lds + L_CNT) + 0, __ATOMIC_RELAXED, __HIP_MEMORY_SCOPE_WORKGROUP) >= 4u) break;
      }
      CFENCE();
      const rsrc_t rxn = mkrs(ws + WS_XNT, 8 * 32 * 10240);
#pragma unroll
      for (int it = 0; it < 3; ++it) {
        const int pc = 4 * it + q, nf = 10 * w + (pc < 10 ? pc : 9);
        const int qq = l15 >> 2, pp = l15 & 3;
        LAS char* a0 = lds + L_P0T + qq * P0T_RS + (16 * nf + 4 * pp) * 2;
        const u32x2 lo = __builtin_bit_cast(u32x2, __builtin_amdgcn_ds_read_tr16_b64_v4i16((LAS s16x4*)a0));
        const u32x2 hi = __builtin_bit_cast(u32x2, __builtin_amdgcn_ds_read_tr16_b64_v4i16((LAS s16x4*)(a0 + 4 * P0T_RS)));
        const int jj = nf / 5, nfl = nf - 5 * jj;
        const unsigned off = (unsigned)(((jj * 32 + g) * 10 + (j >> 2) * 5 + nfl) * 1024 + ((j & 3) * 16 + l15) * 16);
        st16<SC1>((u32x4){lo.x, lo.y, hi.x, hi.y}, rxn, pc < 10 ? off : OOB, 0);
      }
    }
    const u64 T = (((u64)(unsigned)__builtin_amdgcn_readfirstlane((unsigned)(Ein >> 32)) << 32) | (unsigned)__builtin_amdgcn_readfirstlane((unsigned)Ein)) + 1;
    publish(lds, 1, 4, ws, WS_FLAG0 + (g * 8 + j) * 8, T, lane);
    STAMP(1);

    {
      LANE_IDS();
      f32x4 acc[2][5];
#pragma unroll
      for (int m = 0; m < 2; ++m)
#pragma unroll
        for (int n = 0; n < 5; ++n) acc[m][n] = (f32x4){0.f, 0.f, 0.f, 0.f};
      float rs[2] = {0.f, 0.f};
      unsigned ones = 0x3f803f80u; asm volatile("" : "+v"(ones));
      const unsigned aoff0 = (w * 16 + l15) * 128 + ((q ^ (l15 >> 1)) << 4), aoff1 = aoff0 ^ 64;
      const unsigned boff = P1_DOFF + lane * 16;
      bf16x8 XA[2], XB[5], YA[2], YB[5];
      auto rd = [&](bf16x8(&A)[2], bf16x8(&B)[5], int sa, int sb, int kk) {
        LAS char* pa = lds + sa * 16384 + (kk ? aoff1 : aoff0);
        LAS char* pb = lds + sb * 10240 + boff + kk * 5120;
#pragma unroll
        for (int n = 0; n < 5; ++n) B[n] = ldsfrag(pb + n * 1024);
        A[0] = ldsfrag(pa); A[1] = ldsfrag(pa + 8192);
      };
      auto mm = [&](const bf16x8(&A)[2], const bf16x8(&B)[5]) {
#pragma unroll
        for (int m = 0; m < 2; ++m) {
#pragma unroll
          for (int n = 0; n < 5; ++n) acc[m][n] = mfma(B[n], A[m], acc[m][n]);
          const u32x4 au = __builtin_bit_cast(u32x4, A[m]);
#pragma unroll
          for (int i = 0; i < 4; ++i) { const unsigned ai = au[i]; asm("v_dot2c_f32_bf16 %0, %1, %2" : "+v"(rs[m]) : "v"(ai), "v"(ones)); }
        }
      };
      BAR();
      STAMP(2); STAMPC(28);
      rd(XA, XB, 0, 0, 0); wait_lgkm0();
      int sa = 0, sb = 0;
      auto steps = [&](int n) {
#pragma unroll 1
        for (int i = 0; i < n; ++i) {
          rd(YA, YB, sa, sb, 1); mm(XA, XB); interleave<10, 7, 1>(); SCHED(); wait_lgkm0();
          BAR();
          sa = (sa + 1 == P1_SA) ? 0 : sa + 1; sb = (sb + 1 == P1_DB) ? 0 : sb + 1;
          rd(XA, XB, sa, sb, 0); mm(YA, YB); interleave<10, 7, 1>(); SCHED(); wait_lgkm0();
        }
      };
      steps(8);
      u64 fb[4];
#pragma unroll
      for (int i = 0; i < 4; ++i) fb[i] = __hip_atomic_load((GAS u64*)(ws + WS_FLAGB) + i * 64 + lane, __ATOMIC_RELAXED, __HIP_MEMORY_SCOPE_AGENT);
      steps(8);
      if (!__all(fb[0] == T && fb[1] == T && fb[2] == T && fb[3] == T)) poll_flags256(ws, WS_FLAGB, T, lane);
      CFENCE();
      f32x4 mp[2][4];
      const rsrc_t rm = mkrs(ws + WS_MODP, 4 * 3840 * 4);
      const int vsel = lane < 32 ? (w == 0 ? 1 : w == 1 ? 0 : w == 2 ? 2 : 4) : 5;
      const bool vact = (lane < 20) || (w == 0 && lane >= 32 && lane < 52);
#pragma unroll
      for (int k = 0; k < 4; ++k) mp[0][k] = __builtin_bit_cast(f32x4, ld16<SC1>(rm, vact ? (unsigned)((vsel * DM + 80 * j + (lane & 31) * 4) * 4) : OOB, k * 3840 * 4));
      steps(16);
      STAMP(3); STAMPC(29);
      {
        f32x4 s = (mp[0][0] + mp[0][1]) + (mp[0][2] + mp[0][3]);
        const bool plus1 = (vsel == 1) || (vsel == 4);
#pragma unroll
        for (int i = 0; i < 4; ++i) { s[i] = rbf(s[i]); if (plus1) s[i] = rbf(1.0f + s[i]); }
        const int trow = lane < 32 ? w : 4;
        if (vact) *(LAS f32x4*)(lds + L_TAB + (trow * 80 + (lane & 31) * 4) * 4) = s;
        wait_lgkm0(); CFENCE();
        if (lane == 0) __hip_atomic_fetch_add((LAS unsigned*)(lds + L_CNT) + 2, 1u, __ATOMIC_RELAXED, __HIP_MEMORY_SCOPE_WORKGROUP);
        for (unsigned spins = 0; spins < SPIN_MAX; ++spins) {
          if (__hip_atomic_load((LAS unsigned*)(lds + L_CNT) + 2, __ATOMIC_RELAXED, __HIP_MEMORY_SCOPE_WORKGROUP) >= 4u) break;
        }
        CFENCE();
      }
      rs[0] = xq_sum(rs[0]); rs[1] = -xq_sum(rs[1]);
      u32x2 pkd[2][5];
#pragma unroll
      for (int n = 0; n < 5; ++n) {
        const f32x4 c1 = *(LAS f32x4*)(lds + L_TAB + (0 * 80 + 16 * n + 4 * q) * 4), sa4 = *(LAS f32x4*)(lds + L_TAB + (1 * 80 + 16 * n + 4 * q) * 4);
        const f32x4 pv = acc[0][n] * c1 + rs[0] * sa4, qv = rs[1] * sa4 - acc[1][n] * c1;
        pkd[0][n] = (u32x2){pk2(pv[0], pv[1]), pk2(pv[2], pv[3])};
        pkd[1][n] = (u32x2){pk2(qv[0], qv[1]), pk2(qv[2], qv[3])};
      }
      const rsrc_t rpq = mkrs(ws + WS_PQ, 32 * 40 * 4 * 1024);
      auto pq_off = [&](int m, int nf) { const int c = 10 * j + 2 * nf + (q >> 1); return (unsigned)(((g * 40 + m * 20 + (c >> 2)) * 4 + w) * 1024 + ((c & 3) * 16 + l15) * 16); };
      const int odd = q & 1;
      st16<SC1>(pair_swap(pkd[0][0], pkd[0][1]), rpq, pq_off(0, odd), 0);
      st16<SC1>(pair_swap(pkd[0][2], pkd[0][3]), rpq, pq_off(0, 2 + odd), 0);
      st16<SC1>(pair_swap(pkd[1][0], pkd[1][1]), rpq, pq_off(1, odd), 0);
      st16<SC1>(pair_swap(pkd[1][2], pkd[1][3]), rpq, pq_off(1, 2 + odd), 0);
      st16<SC1>(pair_swap(pkd[0][4], pkd[1][4]), rpq, pq_off(odd, 4), 0);
      publish(lds, 3, 4, ws, WS_FLAG1 + g * 256 + j * 8, T, lane);
      STAMP(4);
    }

    {
      LANE_IDS();
      constexpr int NT = 20, W = 6;
      const rsrc_t rx = mkrs(p.x, SEQ * DM * 2);
      u32x2 xin[5];
#pragma unroll
      for (int n = 0; n < 5; ++n) xin[n] = ld8<0>(rx, (unsigned)((64 * g + 16 * w + l15) * (DM * 2) + (80 * j + 16 * n + 4 * q) * 2), 0);
      poll_flags(ws, WS_FLAG1 + g * 256, 8, T, lane);
      STAMP(5);
      const rsrc_t ra = mkrs(ws + WS_PQ + (size_t)g * 40 * 4 * 1024, 40 * 4 * 1024);
      u32x4 A[NT][2];
#pragma unroll
      for (int t = 0; t < W; ++t) { A[t][0] = ld16<SC1>(ra, lane * 16, ((2 * t) * 4 + w) * 1024); A[t][1] = ld16<SC1>(ra, lane * 16, ((2 * t + 1) * 4 + w) * 1024); }
      f32x4 acc[5];
#pragma unroll
      for (int n = 0; n < 5; ++n) acc[n] = (f32x4){0.f, 0.f, 0.f, 0.f};
      const unsigned boff0 = l15 * 128 + ((q ^ (l15 >> 1)) << 4), boff1 = boff0 ^ 64;
      bf16x8 XB[5], YB[5];
      auto rd = [&](bf16x8(&B)[5], int slot, int kk) {
        LAS char* pb = lds + slot * 10240 + (kk ? boff1 : boff0);
#pragma unroll
        for (int n = 0; n < 5; ++n) B[n] = ldsfrag(pb + n * 2048);
      };
      BAR();
      STAMP(6);
      rd(XB, 0, 0); wait_lgkm0();
#pragma unroll
      for (int t = 0; t < NT; ++t) {
        if (t + W < NT) { A[t + W][0] = ld16<SC1>(ra, lane * 16, ((2 * (t + W)) * 4 + w) * 1024); A[t + W][1] = ld16<SC1>(ra, lane * 16, ((2 * (t + W) + 1) * 4 + w) * 1024); }
        rd(YB, t % P2_RING, 1);
#pragma unroll
        for (int n = 0; n < 5; ++n) acc[n] = mfma(XB[n], asfrag(A[t][0]), acc[n]);
        interleave<5, 5, 1>(); SCHED(); wait_lgkm0();
        BAR();
        rd(XB, (t + 1) % P2_RING, 0);
#pragma unroll
        for (int n = 0; n < 5; ++n) acc[n] = mfma(YB[n], asfrag(A[t][1]), acc[n]);
        interleave<5, 5, 1>(); SCHED(); wait_lgkm0();
      }
      STAMP(7);
      float x1f[5][4]; float s = 0.f;
      u32x2 cp[5], x1p[5];
      const rsrc_t rx1 = mkrs(ws + WS_X1 + (size_t)b * 4 * 5 * 512, 4 * 5 * 512);
#pragma unroll
      for (int n = 0; n < 5; ++n) {
        const f32x4 ga = *(LAS f32x4*)(lds + L_TAB + (2 * 80 + 16 * n + 4 * q) * 4), cb1 = *(LAS f32x4*)(lds + L_TAB + (3 * 80 + 16 * n + 4 * q) * 4);
        const float x0 = bflo(xin[n].x), x1_ = bfhi(xin[n].x), x2 = bflo(xin[n].y), x3 = bfhi(xin[n].y);
        x1p[n] = (u32x2){pk2(x0 + ga[0] * acc[n][0], x1_ + ga[1] * acc[n][1]), pk2(x2 + ga[2] * acc[n][2], x3 + ga[3] * acc[n][3])};
        st8<0>(x1p[n], rx1, lane * 8, (w * 5 + n) * 512);
        x1f[n][0] = bflo(x1p[n].x); x1f[n][1] = bfhi(x1p[n].x); x1f[n][2] = bflo(x1p[n].y); x1f[n][3] = bfhi(x1p[n].y);
        cp[n] = (u32x2){pk2(x1f[n][0] * cb1[0], x1f[n][1] * cb1[1]), pk2(x1f[n][2] * cb1[2], x1f[n][3] * cb1[3])};
        s += (x1f[n][0] + x1f[n][1]) + (x1f[n][2] + x1f[n][3]);
      }
      const float mean = xq_sum(s) * (1.0f / 80.0f);
      float m2 = 0.f;
#pragma unroll
      for (int n = 0; n < 5; ++n)
#pragma unroll
        for (int r = 0; r < 4; ++r) { const float d = x1f[n][r] - mean; m2 += d * d; }
      m2 = xq_sum(m2);
      const rsrc_t rc = mkrs(ws + WS_X1C, 32 * 20 * 4 * 1024);
      auto c_off = [&](int nf) { const int c = 10 * j + 2 * nf + (q >> 1); return (unsigned)(((g * 20 + (c >> 2)) * 4 + w) * 1024 + ((c & 3) * 16 + l15) * 16); };
      const int odd = q & 1;
      st16<SC1>(pair_swap(cp[0], cp[1]), rc, c_off(odd), 0);
      st16<SC1>(pair_swap(cp[2], cp[3]), rc, c_off(2 + odd), 0);
      st8<SC1>(cp[4], rc, c_off(4) + odd * 8, 0);
      const rsrc_t rs1 = mkrs(ws + WS_ST1, SEQ * 8 * 8);
      st8<SC1>((u32x2){__float_as_uint(mean), __float_as_uint(m2)}, rs1, q == 0 ? (unsigned)(((64 * g + 16 * w + l15) * 8 + j) * 8) : OOB, 0);
      publish(lds, 4, 4, ws, WS_FLAG2 + g * 256 + j * 8, T, lane);
      STAMP(8);
    }

    {
      LANE_IDS();
      constexpr int NT = 10;
      {
        const rsrc_t ruv = mkrs(ws + WS_UV, 2 * 896 * 16);
        const int col = w * 56 + lane, part = col >= 112 ? 1 : 0, cc = col - 112 * part;
        const unsigned off = lane < 56 ? (unsigned)((part * 896 + 112 * j + cc) * 16) : OOB;
        u32x4 gv;
        for (unsigned spins = 0;; ++spins) {
          gv = ld16<SC1>(ruv, off, 0);
          const bool ok = lane >= 56 || (gv.z == (unsigned)T && gv.w == (unsigned)(T >> 32));
          if (__all(ok)) break;
          if (spins > SPIN_MAX) { note_timeout(ws, 0x300, lane); break; }
          __builtin_amdgcn_s_sleep(1); CFENCE();
        }
        if (lane < 56) { *(LAS unsigned*)(lds + L_UV + col * 4) = gv.x; *(LAS unsigned*)(lds + L_UV + (224 + col) * 4) = gv.y; }
      }
      poll_flags(ws, WS_FLAG2 + g * 256, 8, T, lane);
      STAMP(9);
      const rsrc_t ra = mkrs(ws + WS_X1C + (size_t)g * 20 * 4 * 1024, 20 * 4 * 1024);
      u32x4 A[20];
#pragma unroll
      for (int k = 0; k < 20; ++k) A[k] = ld16<SC1>(ra, lane * 16, (k * 4 + w) * 1024);
      const rsrc_t rs1 = mkrs(ws + WS_ST1, SEQ * 8 * 8);
      const u32x4 st = ld16<SC1>(rs1, (unsigned)(((64 * g + 16 * w + l15) * 8 + 2 * q) * 8), 0);
      f32x4 acc[7];
      bf16x8 XB[7], YB[7];
      auto rd = [&](bf16x8(&B)[7], int slot, int kk) {
        LAS char* pb = lds + slot * 14336 + kk * 7168 + lane * 16;
#pragma unroll
        for (int n = 0; n < 7; ++n) B[n] = ldsfrag(pb + n * 1024);
      };
      wait_lgkm0();
      BAR();
      STAMP(10);
      rd(XB, 0, 0); wait_lgkm0();
      float mean1, rstd1;
      {
        const float ma = __uint_as_float(st.x), mb = __uint_as_float(st.z);
        float m = 0.5f * (ma + mb), d = ma - mb, M2 = __uint_as_float(st.y) + __uint_as_float(st.w) + d * d * 40.0f;
        float mo = __shfl_xor(m, 16), Mo = __shfl_xor(M2, 16); d = m - mo; M2 = M2 + Mo + d * d * 80.0f; m = 0.5f * (m + mo);
        mo = __shfl_xor(m, 32); Mo = __shfl_xor(M2, 32); d = m - mo; M2 = M2 + Mo + d * d * 160.0f; m = 0.5f * (m + mo);
        mean1 = m; rstd1 = 1.0f / sqrtf(M2 * (1.0f / DM) + 1e-5f);
      }
#pragma unroll
      for (int n = 0; n < 7; ++n) acc[n] = (f32x4){0.f, 0.f, 0.f, 0.f};
#pragma unroll
      for (int t = 0; t < NT; ++t) {
        rd(YB, t % P3_RING, 1);
#pragma unroll
        for (int n = 0; n < 7; ++n) acc[n] = mfma(asfrag(A[2 * t]), XB[n], acc[n]);
        interleave<7, 7, 2>(); SCHED(); wait_lgkm0();
        BAR();
        rd(XB, (t + 1) % P3_RING, 0);
#pragma unroll
        for (int n = 0; n < 7; ++n) acc[n] = mfma(asfrag(A[2 * t + 1]), YB[n], acc[n]);
        interleave<7, 7, 2>(); SCHED(); wait_lgkm0();
      }
      u32x2 z2p[7];
      const int nval = (j == 7) ? 5 : 7;
      {
        float mr[4], rr[4];
#pragma unroll
        for (int r = 0; r < 4; ++r) { mr[r] = __shfl(mean1, 4 * q + r); rr[r] = __shfl(rstd1, 4 * q + r); }
        float zs[4] = {0.f, 0.f, 0.f, 0.f};
#pragma unroll
        for (int n = 0; n < 7; ++n) {
          const float u = *(LAS float*)(lds + L_UV + (16 * n + l15) * 4), v = *(LAS float*)(lds + L_UV + (224 + 16 * n + l15) * 4);
          float z[4];
#pragma unroll
          for (int r = 0; r < 4; ++r) z[r] = gelu_tanh(rr[r] * (acc[n][r] - mr[r] * u) + v);
          z2p[n] = (u32x2){pk2(z[0], z[1]), pk2(z[2], z[3])};
          if (n < nval) { zs[0] += bflo(z2p[n].x); zs[1] += bfhi(z2p[n].x); zs[2] += bflo(z2p[n].y); zs[3] += bfhi(z2p[n].y); }
        }
        const float inv = 1.0f / (16.0f * nval);
        float mz[4], M2[4];
#pragma unroll
        for (int r = 0; r < 4; ++r) { mz[r] = row16_sum(zs[r]) * inv; M2[r] = 0.f; }
#pragma unroll
        for (int n = 0; n < 7; ++n) if (n < nval) {
          const float d0 = bflo(z2p[n].x) - mz[0], d1 = bfhi(z2p[n].x) - mz[1], d2 = bflo(z2p[n].y) - mz[2], d3 = bfhi(z2p[n].y) - mz[3];
          M2[0] += d0 * d0; M2[1] += d1 * d1; M2[2] += d2 * d2; M2[3] += d3 * d3;
        }
#pragma unroll
        for (int r = 0; r < 4; ++r) M2[r] = row16_sum(M2[r]);
        const float ms = l15 == 0 ? mz[0] : l15 == 1 ? mz[1] : l15 == 2 ? mz[2] : mz[3];
        const float Ms = l15 == 0 ? M2[0] : l15 == 1 ? M2[1] : l15 == 2 ? M2[2] : M2[3];
        const rsrc_t rs2 = mkrs(ws + WS_ST2, SEQ * 8 * 16);
        st16<SC1>((u32x4){__float_as_uint(ms), __float_as_uint(Ms), (unsigned)T, (unsigned)(T >> 32)}, rs2,
                  l15 < 4 ? (unsigned)(((64 * g + 16 * w + 4 * q + l15) * 8 + j) * 16) : OOB, 0);
      }
      STAMP(11);
#pragma unroll
      for (int n = 0; n < 7; ++n) acc[n] = (f32x4){0.f, 0.f, 0.f, 0.f};
#pragma unroll
      for (int t = 0; t < NT; ++t) {
        rd(YB, (NT + t) % P3_RING, 1);
#pragma unroll
        for (int n = 0; n < 7; ++n) acc[n] = mfma(XB[n], asfrag(A[2 * t]), acc[n]);
        interleave<7, 7, 2>(); SCHED(); wait_lgkm0();
        BAR();
        rd(XB, (NT + t + 1) % P3_RING, 0);
#pragma unroll
        for (int n = 0; n < 7; ++n) acc[n] = mfma(YB[n], asfrag(A[2 * t + 1]), acc[n]);
        interleave<7, 7, 2>(); SCHED(); wait_lgkm0();
      }
      STAMP(12);
      const rsrc_t rs2 = mkrs(ws + WS_ST2, SEQ * 8 * 16);
      u32x4 ga_, gb_;
      const unsigned soff = (unsigned)(((64 * g + 16 * w + l15) * 8 + 2 * q) * 16);
      ga_ = ld16<SC1>(rs2, soff, 0); gb_ = ld16<SC1>(rs2, soff + 16, 0);
      {
        const rsrc_t rz = mkrs(ws + WS_Z1 + (size_t)b * 4 * 7 * 512, 4 * 7 * 512);
#pragma unroll
        for (int n = 0; n < 7; ++n) {
          const f32x4 u = *(LAS f32x4*)(lds + L_UV + (112 + 16 * n + 4 * q) * 4), v = *(LAS f32x4*)(lds + L_UV + (224 + 112 + 16 * n + 4 * q) * 4);
          float z[4];
#pragma unroll
          for (int r = 0; r < 4; ++r) z[r] = gelu_tanh(rstd1 * (acc[n][r] - mean1 * u[r]) + v[r]);
          st8<0>((u32x2){pk2(z[0], z[1]), pk2(z[2], z[3])}, rz, lane * 8, (w * 7 + n) * 512);
        }
      }
      for (unsigned spins = 0;; ++spins) {
        const bool ok = ga_.z == (unsigned)T && ga_.w == (unsigned)(T >> 32) && gb_.z == (unsigned)T && gb_.w == (unsigned)(T >> 32);
        if (__all(ok)) break;
        if (spins > SPIN_MAX) { note_timeout(ws, 0x400, lane); break; }
        __builtin_amdgcn_s_sleep(1); CFENCE();
        ga_ = ld16<SC1>(rs2, soff, 0); gb_ = ld16<SC1>(rs2, soff + 16, 0);
      }
      float mean2, rstd2;
      {
        const float na = 112.0f, nb = (q == 3) ? 80.0f : 112.0f;
        float ma = __uint_as_float(ga_.x), mb = __uint_as_float(gb_.x);
        float n = na + nb, d = mb - ma, m = ma + d * (nb / n), M2 = __uint_as_float(ga_.y) + __uint_as_float(gb_.y) + d * d * (na * nb / n);
#pragma unroll
        for (int sft = 16; sft <= 32; sft <<= 1) {
          const float no = __shfl_xor(n, sft), mo = __shfl_xor(m, sft), Mo = __shfl_xor(M2, sft);
          const float nt = n + no; d = mo - m;
          M2 = M2 + Mo + d * d * (n * no / nt);
          m = (n * m + no * mo) / nt; n = nt;
        }
        mean2 = m; rstd2 = 1.0f / sqrtf(M2 * (1.0f / FH) + 1e-5f);
      }
      {
        float mr[4], rr[4];
#pragma unroll
        for (int r = 0; r < 4; ++r) { mr[r] = __shfl(mean2, 4 * q + r); rr[r] = __shfl(rstd2, 4 * q + r); }
        u32x2 zn[7];
#pragma unroll
        for (int n = 0; n < 7; ++n)
          zn[n] = (u32x2){pk2((bflo(z2p[n].x) - mr[0]) * rr[0], (bfhi(z2p[n].x) - mr[1]) * rr[1]), pk2((bflo(z2p[n].y) - mr[2]) * rr[2], (bfhi(z2p[n].y) - mr[3]) * rr[3])};
        const rsrc_t rz2 = mkrs(ws + WS_Z2T, 8 * 32 * 14336);
        auto z_off = [&](int nf) { return (unsigned)(((j * 32 + g) * 14 + (w >> 1) * 7 + nf) * 1024 + ((2 * (w & 1) + (q >> 1)) * 16 + l15) * 16); };
        const int odd = q & 1;
        st16<SC1>(pair_swap(zn[0], zn[1]), rz2, z_off(odd), 0);
        st16<SC1>(pair_swap(zn[2], zn[3]), rz2, z_off(2 + odd), 0);
        st16<SC1>(pair_swap(zn[4], zn[5]), rz2, z_off(4 + odd), 0);
        st8<SC1>(zn[6], rz2, z_off(6) + odd * 8, 0);
      }
      publish(lds, 5, 4, ws, WS_FLAG3 + j * 256 + g * 8, T, lane);
      STAMP(13);
    }

    {
      LANE_IDS();
      const int mh = w >> 1, nh = w & 1;
      f32x4 acc[2][4];
#pragma unroll
      for (int m = 0; m < 2; ++m)
#pragma unroll
        for (int n = 0; n < 4; ++n) acc[m][n] = (f32x4){0.f, 0.f, 0.f, 0.f};
      const unsigned aoff0 = (mh * 32 + l15) * 128 + ((q ^ (l15 >> 1)) << 4), aoff1 = aoff0 ^ 64;
      const unsigned boff = P4_DOFF + nh * 4096 + lane * 16;
      const rsrc_t rb = mkrs(p.bsp, SEQ * 2);
      const float bs0 = bflo(ld2<0>(rb, (64 * g + 32 * mh + l15) * 2, 0)), bs1 = bflo(ld2<0>(rb, (64 * g + 32 * mh + 16 + l15) * 2, 0));
      bf16x8 XA[2], XB[4], YA[2], YB[4];
      if (nh == 0) {
        auto rd = [&](bf16x8(&A)[2], bf16x8(&B)[4], int sa, int sb, int kk) {
          LAS char* pa = lds + sa * 8192 + (kk ? aoff1 : aoff0);
          LAS char* pb = lds + sb * 14336 + boff + kk * 7168;
#pragma unroll
          for (int n = 0; n < 4; ++n) B[n] = ldsfrag(pb + n * 1024);
          A[0] = ldsfrag(pa); A[1] = ldsfrag(pa + 2048);
        };
        auto mm = [&](const bf16x8(&A)[2], const bf16x8(&B)[4]) {
#pragma unroll
          for (int m = 0; m < 2; ++m)
#pragma unroll
            for (int n = 0; n < 4; ++n) acc[m][n] = mfma(B[n], A[m], acc[m][n]);
        };
        BAR();
        STAMP(14);
        rd(XA, XB, 0, 0, 0); wait_lgkm0();
        int sa = 0, sb = 0;
#pragma unroll 1
        for (int i = 0; i < 32; ++i) {
          rd(YA, YB, sa, sb, 1); mm(XA, XB); interleave<8, 6, 1>(); SCHED(); wait_lgkm0();
          BAR();
          sa = (sa + 1 == P4_SA) ? 0 : sa + 1; sb = (sb + 1 == P4_DB) ? 0 : sb + 1;
          rd(XA, XB, sa, sb, 0); mm(YA, YB); interleave<8, 6, 1>(); SCHED(); wait_lgkm0();
        }
      } else {
        auto rd = [&](bf16x8(&A)[2], bf16x8(&B)[4], int sa, int sb, int kk) {
          LAS char* pa = lds + sa * 8192 + (kk ? aoff1 : aoff0);
          LAS char* pb = lds + sb * 14336 + boff + kk * 7168;
#pragma unroll
          for (int n = 0; n < 3; ++n) B[n] = ldsfrag(pb + n * 1024);
          A[0] = ldsfrag(pa); A[1] = ldsfrag(pa + 2048);
        };
        auto mm = [&](const bf16x8(&A)[2], const bf16x8(&B)[4]) {
#pragma unroll
          for (int m = 0; m < 2; ++m)
#pragma unroll
            for (int n = 0; n < 3; ++n) acc[m][n] = mfma(B[n], A[m], acc[m][n]);
        };
        BAR();
        STAMP(14);
        rd(XA, XB, 0, 0, 0); wait_lgkm0();
        int sa = 0, sb = 0;
#pragma unroll 1
        for (int i = 0; i < 32; ++i) {
          rd(YA, YB, sa, sb, 1); mm(XA, XB); interleave<6, 5, 1>(); SCHED(); wait_lgkm0();
          BAR();
          sa = (sa + 1 == P4_SA) ? 0 : sa + 1; sb = (sb + 1 == P4_DB) ? 0 : sb + 1;
          rd(XA, XB, sa, sb, 0); mm(YA, YB); interleave<6, 5, 1>(); SCHED(); wait_lgkm0();
        }
      }
      STAMP(15);
      const rsrc_t rz = mkrs(ws + WS_Z1 + (size_t)b * 4 * 7 * 512, 4 * 7 * 512);
      u32x2 up[2][4];
#pragma unroll
      for (int m = 0; m < 2; ++m)
#pragma unroll
        for (int n = 0; n < 4; ++n) {
          const int nf = 4 * nh + n;
          const u32x2 z = ld8<SC1>(rz, nf < 7 ? (unsigned)(lane * 8 + ((2 * mh + m) * 7 + nf) * 512) : OOB, 0);
          const float bs = m ? bs1 : bs0;
          up[m][n] = (u32x2){pk2(bflo(z.x) * rbf(rbf(acc[m][n][0]) + bs), bfhi(z.x) * rbf(rbf(acc[m][n][1]) + bs)),
                             pk2(bflo(z.y) * rbf(rbf(acc[m][n][2]) + bs), bfhi(z.y) * rbf(rbf(acc[m][n][3]) + bs))};
        }
      const rsrc_t ru = mkrs(ws + WS_U, 32 * 28 * 4 * 1024);
      auto u_off = [&](int mf, int nf) { const int c = 14 * j + 2 * nf + (q >> 1); return (unsigned)(((g * 28 + (c >> 2)) * 4 + mf) * 1024 + ((c & 3) * 16 + l15) * 16); };
      const int odd = q & 1;
      if (nh == 0) {
#pragma unroll
        for (int m = 0; m < 2; ++m) {
          st16<SC1>(pair_swap(up[m][0], up[m][1]), ru, u_off(2 * mh + m, odd), 0);
          st16<SC1>(pair_swap(up[m][2], up[m][3]), ru, u_off(2 * mh + m, 2 + odd), 0);
        }
      } else {
#pragma unroll
        for (int m = 0; m < 2; ++m) st16<SC1>(pair_swap(up[m][0], up[m][1]), ru, u_off(2 * mh + m, 4 + odd), 0);
        st16<SC1>(pair_swap(up[0][2], up[1][2]), ru, u_off(2 * mh + odd, 6), 0);
      }
      publish(lds, 6, 4, ws, WS_FLAG4 + g * 256 + j * 8, T, lane);
      if (b == 0 && w == 0 && lane == 0) __hip_atomic_store((GAS u64*)(ws + WS_EPOCH), T, __ATOMIC_RELAXED, __HIP_MEMORY_SCOPE_AGENT);
      STAMP(16);
    }

    {
      LANE_IDS();
      constexpr int NT = 14, W = 7;
      const rsrc_t rx1 = mkrs(ws + WS_X1 + (size_t)b * 4 * 5 * 512, 4 * 5 * 512);
      u32x2 x1p[5];
#pragma unroll
      for (int n = 0; n < 5; ++n) x1p[n] = ld8<SC1>(rx1, lane * 8, (w * 5 + n) * 512);
      poll_flags(ws, WS_FLAG4 + g * 256, 8, T, lane);
      STAMP(17);
      const rsrc_t ra = mkrs(ws + WS_U + (size_t)g * 28 * 4 * 1024, 28 * 4 * 1024);
      u32x4 A[NT][2];
#pragma unroll
      for (int t = 0; t < W; ++t) { A[t][0] = ld16<SC1>(ra, lane * 16, ((2 * t) * 4 + w) * 1024); A[t][1] = ld16<SC1>(ra, lane * 16, ((2 * t + 1) * 4 + w) * 1024); }
      f32x4 acc[5];
#pragma unroll
      for (int n = 0; n < 5; ++n) acc[n] = (f32x4){0.f, 0.f, 0.f, 0.f};
      bf16x8 XB[5], YB[5];
      auto rd = [&](bf16x8(&B)[5], int slot, int kk) {
        LAS char* pb = lds + slot * 10240 + kk * 5120 + lane * 16;
#pragma unroll
        for (int n = 0; n < 5; ++n) B[n] = ldsfrag(pb + n * 1024);
      };
      BAR();
      STAMP(18);
      rd(XB, 0, 0); wait_lgkm0();
#pragma unroll
      for (int t = 0; t < NT; ++t) {
        if (t + W < NT) { A[t + W][0] = ld16<SC1>(ra, lane * 16, ((2 * (t + W)) * 4 + w) * 1024); A[t + W][1] = ld16<SC1>(ra, lane * 16, ((2 * (t + W) + 1) * 4 + w) * 1024); }
        rd(YB, t, 1);
#pragma unroll
        for (int n = 0; n < 5; ++n) acc[n] = mfma(XB[n], asfrag(A[t][0]), acc[n]);
        interleave<5, 5, 1>(); SCHED(); wait_lgkm0();
        BAR();
        rd(XB, t + 1 < NT ? t + 1 : 0, 0);
#pragma unroll
        for (int n = 0; n < 5; ++n) acc[n] = mfma(YB[n], asfrag(A[t][1]), acc[n]);
        interleave<5, 5, 1>(); SCHED(); wait_lgkm0();
      }
      STAMP(19);
      u32x2 op[5];
#pragma unroll
      for (int n = 0; n < 5; ++n) {
        const f32x4 gb = *(LAS f32x4*)(lds + L_TAB + (4 * 80 + 16 * n + 4 * q) * 4);
        op[n] = (u32x2){pk2(bflo(x1p[n].x) + rbf(gb[0] * rbf(acc[n][0])), bfhi(x1p[n].x) + rbf(gb[1] * rbf(acc[n][1]))),
                        pk2(bflo(x1p[n].y) + rbf(gb[2] * rbf(acc[n][2])), bfhi(x1p[n].y) + rbf(gb[3] * rbf(acc[n][3])))};
      }
      const rsrc_t ro = mkrs(p.out, SEQ * DM * 2);
      const unsigned rowb = (unsigned)((64 * g + 16 * w + l15) * (DM * 2) + 80 * j * 2);
      const int odd = q & 1;
      st16<0>(pair_swap(op[0], op[1]), ro, rowb + (16 * odd + 8 * (q >> 1)) * 2, 0);
      st16<0>(pair_swap(op[2], op[3]), ro, rowb + (16 * (2 + odd) + 8 * (q >> 1)) * 2, 0);
      st8<0>(op[4], ro, rowb + (64 + 4 * q) * 2, 0);
      STAMP(20); STAMPC(31);
    }
  } else if (wid < 6) {
    const int h = wid - 4;
    const unsigned sw0 = (unsigned)(((lane & 7) ^ (lane >> 4)) * 16), sw1 = sw0 ^ 64;
    wait_lgkm0();
    BAR();
    {
      const rsrc_t r = mkrs((h ? p.wsim : p.wsre) + (size_t)g * 64 * SEQ, 64 * SEQ * 2);
      const unsigned v0 = (lane >> 3) * 4096 + sw0, v1 = (lane >> 3) * 4096 + sw1;
      auto issue = [&](int t, int slot) {
        const unsigned k = (unsigned)((t + rot) & 31) * 128;
#pragma unroll
        for (int pc = 0; pc < 8; ++pc) dma16<0>(r, lds + slot * 16384 + (h * 8 + pc) * 1024, (pc & 1) ? v1 : v0, k + pc * 32768);
      };
      loader_prefill<32, P1_SA, 8>(issue);
      STAMP(1);
      loader_run<32, P1_SA, 8>(issue);
    }
    {
      const unsigned v0 = (lane >> 3) * 1280 + (h ? sw1 : sw0), v1 = (lane >> 3) * 1280 + (h ? sw0 : sw1);
      auto issue = [&](int t, int slot) {
        const int im = t >= 10;
        const rsrc_t r = mkrs((im ? p.wdim : p.wdre) + (size_t)j * 80 * DM, 80 * DM * 2);
        const unsigned k = (unsigned)(t - 10 * im) * 128 + h * 5 * 8 * 1280;
#pragma unroll
        for (int pc = 0; pc < 5; ++pc) dma16<0>(r, lds + slot * 10240 + (h * 5 + pc) * 1024, (pc & 1) ? v1 : v0, k + pc * 8 * 1280);
      };
      loader_prefill<20, P2_RING, 5>(issue);
      STAMP(2);
      loader_run<20, P2_RING, 5>(issue);
    }
    {
      const rsrc_t r = mkrs(ws + WS_WINR + (size_t)j * 20 * 14336, 20 * 14336);
      auto issue = [&](int t, int slot) {
#pragma unroll
        for (int pc = 0; pc < 7; ++pc) dma16<SC1>(r, lds + slot * 14336 + (h * 7 + pc) * 1024, lane * 16, (unsigned)(t * 14336 + (h * 7 + pc) * 1024));
      };
      loader_prefill<20, P3_RING, 7>(issue);
      STAMP(3);
      loader_run<20, P3_RING, 7>(issue);
    }
    {
      const rsrc_t r = mkrs(p.wsp + (size_t)g * 64 * SEQ, 64 * SEQ * 2);
      const unsigned v0 = (lane >> 3) * 4096 + sw0, v1 = (lane >> 3) * 4096 + sw1;
      auto issue = [&](int t, int slot) {
        const unsigned k = (unsigned)((t + rot) & 31) * 128 + h * 4 * 32768;
#pragma unroll
        for (int pc = 0; pc < 4; ++pc) dma16<0>(r, lds + slot * 8192 + (h * 4 + pc) * 1024, (pc & 1) ? v1 : v0, k + pc * 32768);
      };
      loader_prefill<32, P4_SA, 4>(issue);
      STAMP(4);
      loader_run<32, P4_SA, 4>(issue);
    }
    {
      const rsrc_t r = mkrs(ws + WS_WOUTR + (size_t)j * 14 * 10240, 14 * 10240);
      auto issue = [&](int t, int slot) {
#pragma unroll
        for (int pc = 0; pc < 5; ++pc) dma16<SC1>(r, lds + slot * 10240 + (h * 5 + pc) * 1024, lane * 16, (unsigned)(t * 10240 + (h * 5 + pc) * 1024));
      };
      loader_prefill<14, P5_RING, 5>(issue);
      STAMP(5);
      loader_run<14, P5_RING, 5>(issue);
    }
  } else {
    const int h = wid - 6;
    wait_lgkm0();
    BAR();
    if (h == 0 && b < 240) {
      const int cg = b % 60, kq = b / 60;
      const rsrc_t rw = mkrs(p.wmod, DM * 3840 * 2), rc = mkrs(p.c, DM * 2);
      u32x4 wv[20]; unsigned cv[20];
#pragma unroll
      for (int it = 0; it < 20; ++it) {
        const int row = kq * 160 + it * 8 + (lane >> 3);
        wv[it] = ld16<0>(rw, (unsigned)(row * 7680 + cg * 128 + (lane & 7) * 16), 0);
        cv[it] = ld2<0>(rc, row * 2, 0);
      }
      float a8[8] = {0.f, 0.f, 0.f, 0.f, 0.f, 0.f, 0.f, 0.f};
#pragma unroll
      for (int it = 0; it < 20; ++it) {
        const float cf = bflo(cv[it]);
        const float sl = rbf(cf * __builtin_amdgcn_rcpf(1.0f + __builtin_amdgcn_exp2f(-1.4426950408889634f * cf)));
#pragma unroll
        for (int i = 0; i < 4; ++i) { a8[2 * i] += sl * bflo(wv[it][i]); a8[2 * i + 1] += sl * bfhi(wv[it][i]); }
      }
#pragma unroll
      for (int i = 0; i < 8; ++i) { a8[i] += __shfl_xor(a8[i], 8); a8[i] += __shfl_xor(a8[i], 16); a8[i] += __shfl_xor(a8[i], 32); }
      const rsrc_t rm = mkrs(ws + WS_MODP, 4 * 3840 * 4);
      const unsigned mo = (unsigned)((kq * 3840 + cg * 64 + (lane & 7) * 8) * 4);
      st16<SC1>((u32x4){__float_as_uint(a8[0]), __float_as_uint(a8[1]), __float_as_uint(a8[2]), __float_as_uint(a8[3])}, rm, lane < 8 ? mo : OOB, 0);
      st16<SC1>((u32x4){__float_as_uint(a8[4]), __float_as_uint(a8[5]), __float_as_uint(a8[6]), __float_as_uint(a8[7])}, rm, lane < 8 ? mo + 16 : OOB, 0);
    }
    {
      LAS char* sc = lds + L_RT + h * 7168;
#pragma unroll 1
      for (int rep = 0; rep < 2; ++rep) {
        int u;
        if (h == 1) u = b + 256 * rep; else u = (rep == 0 && b < 32) ? 512 + b : 1000;
        if (u >= 544) continue;
        const int qq = l15 >> 2, pp = l15 & 3;
        if (u < 320) {
          const int jp = u / 20, kf = u - 20 * jp, jj = jp >> 1, part = jp & 1;
          const rsrc_t r = mkrs(p.win, DM * FF * 2);
#pragma unroll
          for (int pc = 0; pc < 7; ++pc) {
            const int idx = pc * 64 + lane, row = idx / 14, ch = idx - 14 * row;
            const bool ok = 112 * jj + 8 * ch < FH;
            dma16<0>(r, sc + pc * 1024, ok ? (unsigned)((32 * kf + row) * (FF * 2) + (part ? 0 : FH * 2) + 224 * jj + ch * 16) : OOB, 0);
          }
          wait_vm<0>(); CFENCE();
          const rsrc_t ro = mkrs(ws + WS_WINR, 8 * 20 * 14336);
#pragma unroll
          for (int nf = 0; nf < 7; ++nf) {
            LAS char* a0 = sc + (8 * q + qq) * 224 + (16 * nf + 4 * pp) * 2;
            const u32x2 lo = __builtin_bit_cast(u32x2, __builtin_amdgcn_ds_read_tr16_b64_v4i16((LAS s16x4*)a0));
            const u32x2 hi = __builtin_bit_cast(u32x2, __builtin_amdgcn_ds_read_tr16_b64_v4i16((LAS s16x4*)(a0 + 4 * 224)));
            st16<SC1>((u32x4){lo.x, lo.y, hi.x, hi.y}, ro, (unsigned)((((jj * 2 + part) * 20 + kf) * 7 + nf) * 1024 + lane * 16), 0);
          }
        } else {
          const int uu = u - 320, jj = uu / 28, kf = uu - 28 * jj;
          const rsrc_t r = mkrs(p.wout, FH * DM * 2);
#pragma unroll
          for (int pc = 0; pc < 5; ++pc) {
            const int idx = pc * 64 + lane, row = idx / 10, ch = idx - 10 * row;
            dma16<0>(r, sc + pc * 1024, (unsigned)((32 * kf + row) * (DM * 2) + 160 * jj + ch * 16), 0);
          }
          wait_vm<0>(); CFENCE();
          const rsrc_t ro = mkrs(ws + WS_WOUTR, 8 * 14 * 10240);
#pragma unroll
          for (int nf = 0; nf < 5; ++nf) {
            LAS char* a0 = sc + (8 * q + qq) * 160 + (16 * nf + 4 * pp) * 2;
            const u32x2 lo = __builtin_bit_cast(u32x2, __builtin_amdgcn_ds_read_tr16_b64_v4i16((LAS s16x4*)a0));
            const u32x2 hi = __builtin_bit_cast(u32x2, __builtin_amdgcn_ds_read_tr16_b64_v4i16((LAS s16x4*)(a0 + 4 * 160)));
            st16<SC1>((u32x4){lo.x, lo.y, hi.x, hi.y}, ro, (unsigned)(((jj * 28 + kf) * 5 + nf) * 1024 + lane * 16), 0);
          }
        }
        wait_lgkm0(); CFENCE();
      }
    }
    const u64 T = (((u64)(unsigned)__builtin_amdgcn_readfirstlane((unsigned)(Ein >> 32)) << 32) | (unsigned)__builtin_amdgcn_readfirstlane((unsigned)Ein)) + 1;
    publish(lds, 7, 2, ws, WS_FLAGB + (g * 8 + j) * 8, T, lane);
    STAMP(1);
    {
      poll_flags256(ws, WS_FLAG0, T, lane);
      STAMP(2);
      const rsrc_t r = mkrs(ws + WS_XNT + (size_t)j * 32 * 10240, 32 * 10240);
      auto issue = [&](int t, int slot) {
        const unsigned k = (unsigned)((t + rot) & 31) * 10240;
#pragma unroll
        for (int pc = 0; pc < 5; ++pc) dma16<SC1>(r, lds + P1_DOFF + slot * 10240 + (h * 5 + pc) * 1024, lane * 16, k + (h * 5 + pc) * 1024);
      };
      loader_prefill<32, P1_DB, 5>(issue);
      loader_run<32, P1_DB, 5>(issue);
    }
    if (h == 0 && b < 112) {
      const int jj = b / 14, pn = b - 14 * jj, part = pn / 7, nf = pn - 7 * part;
      const rsrc_t rw = mkrs(ws + WS_WINR, 8 * 20 * 14336), rm = mkrs(ws + WS_MODP, 4 * 3840 * 4);
      u32x4 wf[20];
#pragma unroll
      for (int k = 0; k < 20; ++k) wf[k] = ld16<SC1>(rw, lane * 16, (unsigned)((((jj * 2 + part) * 20 + k) * 7 + nf) * 1024));
#pragma unroll
      for (int it = 0; it < 5; ++it) {
        const int c4 = (it * 64 + lane) * 4, isS = c4 >= DM, cc = c4 - DM * isS;
        const unsigned o = (unsigned)(((isS ? 3 : 4) * DM + cc) * 4);
        const f32x4 m0 = __builtin_bit_cast(f32x4, ld16<SC1>(rm, o, 0)), m1 = __builtin_bit_cast(f32x4, ld16<SC1>(rm, o, 3840 * 4)),
                    m2 = __builtin_bit_cast(f32x4, ld16<SC1>(rm, o, 2 * 3840 * 4)), m3 = __builtin_bit_cast(f32x4, ld16<SC1>(rm, o, 3 * 3840 * 4));
        f32x4 s = (m0 + m1) + (m2 + m3);
#pragma unroll
        for (int i = 0; i < 4; ++i) { s[i] = rbf(s[i]); if (!isS) s[i] = 1.0f + s[i]; }
        *(LAS u32x2*)(lds + L_TABE + (isS * DM + cc) * 2) = (u32x2){pk2(s[0], s[1]), pk2(s[2], s[3])};
      }
      wait_lgkm0(); CFENCE();
      f32x4 acc = {0.f, 0.f, 0.f, 0.f};
#pragma unroll
      for (int k = 0; k < 20; ++k) {
        u32x4 e = *(LAS u32x4*)(lds + L_TABE + ((l15 & 1) * DM + 32 * k + 8 * q) * 2);
        if (l15 >= 2) e = (u32x4){0u, 0u, 0u, 0u};
        acc = mfma(asfrag(e), asfrag(wf[k]), acc);
      }
      const rsrc_t ruv = mkrs(ws + WS_UV, 2 * 896 * 16);
      st16<SC1>((u32x4){__float_as_uint(acc[0]), __float_as_uint(acc[1]), (unsigned)T, (unsigned)(T >> 32)}, ruv,
                q == 0 ? (unsigned)((part * 896 + 112 * jj + 16 * nf + l15) * 16) : OOB, 0);
    }
    STAMP(3);
    idle_phase<21>();
    idle_phase<21>();
    {
      poll_flags(ws, WS_FLAG3 + j * 256, 32, T, lane);
      STAMP(4);
      const rsrc_t r = mkrs(ws + WS_Z2T + (size_t)j * 32 * 14336, 32 * 14336);
      auto issue = [&](int t, int slot) {
        const unsigned k = (unsigned)((t + rot) & 31) * 14336;
#pragma unroll
        for (int pc = 0; pc < 7; ++pc) dma16<SC1>(r, lds + P4_DOFF + slot * 14336 + (h * 7 + pc) * 1024, lane * 16, k + (h * 7 + pc) * 1024);
      };
      loader_prefill<32, P4_DB, 7>(issue);
      loader_run<32, P4_DB, 7>(issue);
    }
    idle_phase<15>();
  }
}

__global__ void k_mark(unsigned long long* p) { if (threadIdx.x == 0) p[0] = __builtin_amdgcn_s_memrealtime(); }
__global__ void k_report(const char* ws) {
  __shared__ unsigned long long mn[256], mx[256], ar[256];
  int t = threadIdx.x;
  mn[t] = *(const unsigned long long*)(ws + WS_STAMP + ((size_t)t * 8 + 0) * 256);
  ar[t] = *(const unsigned long long*)(ws + WS_STAMP + ((size_t)t * 8 + 0) * 256 + 8);
  mx[t] = *(const unsigned long long*)(ws + WS_STAMP + ((size_t)t * 8 + 0) * 256 + 20 * 8);
  __syncthreads();
  if (t == 0) { unsigned long long a = ~0ull, b = 0, c = 0, amax = 0; for (int i = 0; i < 256; ++i) { a = mn[i] < a ? mn[i] : a; amax = mn[i] > amax ? mn[i] : amax; b = mx[i] > b ? mx[i] : b; c = ar[i] > c ? ar[i] : c; }
    const unsigned long long mk = *(const unsigned long long*)(ws + WS_STAMP + 0x90000);
    unsigned long long span = amax - a; if (span > 20000) span = 20000;
    unsigned long long t0 = __builtin_amdgcn_s_memrealtime(); while (__builtin_amdgcn_s_memrealtime() - t0 < span * 50) __builtin_amdgcn_s_sleep(4); }
}
extern "C" void kernel_launch(void* const* d_in, const int* in_sizes, int n_in, void* d_out, int out_size, void* d_ws, size_t ws_size,
                              hipStream_t stream) {
  Params p;
  p.x = (const u16*)d_in[0]; p.wsre = (const u16*)d_in[1]; p.wsim = (const u16*)d_in[2]; p.wdre = (const u16*)d_in[3]; p.wdim = (const u16*)d_in[4];
  p.win = (const u16*)d_in[6]; p.wsp = (const u16*)d_in[7]; p.bsp = (const u16*)d_in[8]; p.wout = (const u16*)d_in[9];
  p.c = (const u16*)d_in[10]; p.wmod = (const u16*)d_in[11];
  p.out = (u16*)d_out; p.ws = (char*)d_ws;
  (void)hipFuncSetAttribute((const void*)adaLN_cond__spectral_fnet__mixer_gmlp__prompt_kernel, hipFuncAttributeMaxDynamicSharedMemorySize, LDS_BYTES);
  adaLN_cond__spectral_fnet__mixer_gmlp__prompt_kernel<<<256, 512, LDS_BYTES, stream>>>(p);
  k_report<<<1, 256, 0, stream>>>((const char*)d_ws);
}
```

```cpp
#include <hip/hip_runtime.h>
#include <hip/hip_bf16.h>

typedef unsigned short u16;
typedef unsigned long long u64;
typedef short bf16x8 __attribute__((ext_vector_type(8)));
typedef short s16x4 __attribute__((ext_vector_type(4)));
typedef __bf16 bf16x2 __attribute__((ext_vector_type(2)));
typedef float f32x2 __attribute__((ext_vector_type(2)));
typedef float f32x4 __attribute__((ext_vector_type(4)));
typedef unsigned u32x2 __attribute__((ext_vector_type(2)));
typedef unsigned u32x4 __attribute__((ext_vector_type(4)));
typedef __amdgpu_buffer_rsrc_t rsrc_t;
#define LAS __attribute__((address_space(3)))
#define GAS __attribute__((address_space(1)))
#define DEVI __device__ __forceinline__

constexpr int SEQ = 2048, DM = 640, FF = 1728, FH = 864;

constexpr unsigned WS_EPOCH = 0x0;
constexpr unsigned WS_TMO   = 0x100;
constexpr unsigned WS_FLAG0 = 0x1000;
constexpr unsigned WS_FLAGB = 0x5000;
constexpr unsigned WS_FLAG1 = 0x9000;
constexpr unsigned WS_FLAG2 = 0xB000;
constexpr unsigned WS_FLAG4 = 0xD000;
constexpr unsigned WS_FLAG3 = 0xF000;
constexpr unsigned WS_MODP  = 0x10000;
constexpr unsigned WS_UV    = 0x20000;
constexpr unsigned WS_ST1   = 0x40000;
constexpr unsigned WS_ST2   = 0x80000;
constexpr unsigned WS_XNT   = 0x100000;
constexpr unsigned WS_PQ    = 0x400000;
constexpr unsigned WS_X1C   = 0xA00000;
constexpr unsigned WS_Z2T   = 0xD00000;
constexpr unsigned WS_U     = 0x1100000;
constexpr unsigned WS_WINR  = 0x1500000;
constexpr unsigned WS_WOUTR = 0x1800000;
constexpr unsigned WS_STAMP = 0x2400000;
constexpr unsigned WS_USED  = 0x2600000;

constexpr int LDS_BYTES = 163840;
constexpr int L_MISC = 159744;
constexpr int L_CNT  = L_MISC;
constexpr int L_TAB  = L_MISC + 128;
constexpr int L_UV   = L_MISC + 1728;
constexpr int L_SINK = L_MISC + 3584;
constexpr int L_TABE = 153600;
constexpr int L_P0T  = 114688;
constexpr int L_RT   = 114688 + 12288;
constexpr int P0T_RS = 1296;

constexpr unsigned SPIN_MAX = 1u << 17;

struct Params {
  const u16 *x, *wsre, *wsim, *wdre, *wdim, *win, *wsp, *bsp, *wout, *c, *wmod;
  u16* out;
  char* ws;
};

DEVI rsrc_t mkrs(const void* p, unsigned bytes) { return __builtin_amdgcn_make_buffer_rsrc((void*)p, 0, (int)bytes, 0x00020000); }
template <int AUX> DEVI u32x4 ld16(rsrc_t r, unsigned v, unsigned s) { return __builtin_amdgcn_raw_buffer_load_b128(r, (int)v, (int)s, AUX); }
template <int AUX> DEVI u32x2 ld8(rsrc_t r, unsigned v, unsigned s) { return __builtin_amdgcn_raw_buffer_load_b64(r, (int)v, (int)s, AUX); }
template <int AUX> DEVI unsigned ld4(rsrc_t r, unsigned v, unsigned s) { return __builtin_amdgcn_raw_buffer_load_b32(r, (int)v, (int)s, AUX); }
template <int AUX> DEVI unsigned ld2(rsrc_t r, unsigned v, unsigned s) { return (unsigned)__builtin_amdgcn_raw_buffer_load_b16(r, (int)v, (int)s, AUX) & 0xffffu; }
template <int AUX> DEVI void st16(u32x4 d, rsrc_t r, unsigned v, unsigned s) { __builtin_amdgcn_raw_buffer_store_b128(d, r, (int)v, (int)s, AUX); }
template <int AUX> DEVI void st8(u32x2 d, rsrc_t r, unsigned v, unsigned s) { __builtin_amdgcn_raw_buffer_store_b64(d, r, (int)v, (int)s, AUX); }
template <int AUX> DEVI void dma16(rsrc_t r, LAS char* dst, unsigned v, unsigned s) { __builtin_amdgcn_raw_ptr_buffer_load_lds(r, (LAS void*)dst, 16, (int)v, (int)s, 0, AUX); }
constexpr int SC1 = 16;
constexpr unsigned OOB = 0x7ffffff0u;

template <int N> DEVI void wait_vm() { asm volatile("s_waitcnt vmcnt(%0)" ::"n"(N > 63 ? 63 : N) : "memory"); }
DEVI void wait_lgkm0() { __builtin_amdgcn_s_waitcnt(0xC07F); }
DEVI void BAR() { asm volatile("s_barrier" ::: "memory"); }
DEVI void CFENCE() { asm volatile("" ::: "memory"); }
#define SCHED() __builtin_amdgcn_sched_barrier(0)

DEVI float bflo(unsigned v) { return __uint_as_float(v << 16); }
DEVI float bfhi(unsigned v) { return __uint_as_float(v & 0xffff0000u); }
DEVI unsigned pk2(float lo, float hi) { f32x2 f = {lo, hi}; return __builtin_bit_cast(unsigned, __builtin_convertvector(f, bf16x2)); }
DEVI float rbf(float v) { return bflo(pk2(v, 0.f)); }
DEVI f32x4 mfma(bf16x8 a, bf16x8 b, f32x4 c) { return __builtin_amdgcn_mfma_f32_16x16x32_bf16(a, b, c, 0, 0, 0); }
DEVI bf16x8 asfrag(u32x4 v) { return __builtin_bit_cast(bf16x8, v); }
DEVI bf16x8 ldsfrag(LAS char* p) { return *(LAS bf16x8*)p; }

template <int CTRL> DEVI float dpp_add(float v) { return v + __int_as_float(__builtin_amdgcn_update_dpp(0, __float_as_int(v), CTRL, 0xf, 0xf, false)); }
DEVI float row16_sum(float v) { v = dpp_add<0x128>(v); v = dpp_add<0x124>(v); v = dpp_add<0x122>(v); return dpp_add<0x121>(v); }
DEVI float xq_sum(float v) { v += __shfl_xor(v, 16); v += __shfl_xor(v, 32); return v; }
DEVI float wave_sum(float v) { return xq_sum(row16_sum(v)); }

DEVI float gelu_tanh(float x) {
  const float u = x * (1.0f + 0.044715f * x * x) * (-2.0f * 0.7978845608028654f * 1.4426950408889634f);
  return x * __builtin_amdgcn_rcpf(1.0f + __builtin_amdgcn_exp2f(u));
}

DEVI u32x4 pair_swap(u32x2 a, u32x2 b) {
  auto s0 = __builtin_amdgcn_permlane16_swap(a.x, b.x, false, false);
  auto s1 = __builtin_amdgcn_permlane16_swap(a.y, b.y, false, false);
  return (u32x4){s0[0], s1[0], s0[1], s1[1]};
}

DEVI void note_timeout(char* ws, unsigned code, int lane) {
  if (lane == 0) __hip_atomic_store((GAS unsigned*)(ws + WS_TMO), code, __ATOMIC_RELAXED, __HIP_MEMORY_SCOPE_AGENT);
}
DEVI void poll_flags(char* ws, unsigned off, int n, u64 T, int lane) {
  for (unsigned spins = 0;; ++spins) {
    u64 f = T;
    if (lane < n) f = __hip_atomic_load((GAS u64*)(ws + off) + lane, __ATOMIC_RELAXED, __HIP_MEMORY_SCOPE_AGENT);
    if (__all(f == T)) break;
    if (spins > SPIN_MAX) { note_timeout(ws, 0x100u | (off >> 12), lane); break; }
    __builtin_amdgcn_s_sleep(1);
  }
  CFENCE();
}
DEVI void poll_flags256(char* ws, unsigned off, u64 T, int lane) {
  for (unsigned spins = 0;; ++spins) {
    bool ok = true;
#pragma unroll
    for (int i = 0; i < 4; ++i) ok &= __hip_atomic_load((GAS u64*)(ws + off) + i * 64 + lane, __ATOMIC_RELAXED, __HIP_MEMORY_SCOPE_AGENT) == T;
    if (__all(ok)) break;
    if (spins > SPIN_MAX) { note_timeout(ws, 0x200u | (off >> 12), lane); break; }
    __builtin_amdgcn_s_sleep(4);
  }
  CFENCE();
}
DEVI void set_flag(char* ws, unsigned off, u64 T, int lane) {
  if (lane == 0) __hip_atomic_store((GAS u64*)(ws + off), T, __ATOMIC_RELAXED, __HIP_MEMORY_SCOPE_AGENT);
}
DEVI void set_flag_rep(char* ws, unsigned off, int idx, u64 T, int lane) {
  if (lane < 8) __hip_atomic_store((GAS u64*)(ws + off + lane * 2048) + idx, T, __ATOMIC_RELAXED, __HIP_MEMORY_SCOPE_AGENT);
}
DEVI void lds_signal(LAS char* lds, int idx, int lane) {
  CFENCE();
  if (lane == 0) __hip_atomic_store((LAS unsigned*)(lds + L_CNT) + idx, 1u, __ATOMIC_RELAXED, __HIP_MEMORY_SCOPE_WORKGROUP);
}
DEVI void lds_wait(LAS char* lds, int idx) {
  for (unsigned spins = 0; spins < (SPIN_MAX << 6); ++spins) {
    if (__hip_atomic_load((LAS unsigned*)(lds + L_CNT) + idx, __ATOMIC_RELAXED, __HIP_MEMORY_SCOPE_WORKGROUP) != 0u) break;
    __builtin_amdgcn_s_sleep(1);
  }
  CFENCE();
}
DEVI void group_wait(LAS char* lds, int idx, char* ws, unsigned off, int n, u64 T, bool poller, int lane) {
  if (poller) { poll_flags(ws, off, n, T, lane); lds_signal(lds, idx, lane); } else lds_wait(lds, idx);
}
DEVI bool arrive_last(LAS char* lds, int idx, unsigned n, int lane) {
  unsigned old = 0;
  if (lane == 0) old = __hip_atomic_fetch_add((LAS unsigned*)(lds + L_CNT) + idx, 1u, __ATOMIC_RELAXED, __HIP_MEMORY_SCOPE_WORKGROUP);
  return (unsigned)__builtin_amdgcn_readfirstlane(old) == n - 1;
}
DEVI void publish(LAS char* lds, int cidx, unsigned nwaves, char* ws, unsigned flag_off, u64 T, int lane) {
  wait_vm<0>();
  if (arrive_last(lds, cidx, nwaves, lane)) set_flag(ws, flag_off, T, lane);
}

#define LANE_IDS() int tz_ = threadIdx.x; asm volatile("" : "+v"(tz_)); const int lane = tz_ & 63, l15 = lane & 15, q = lane >> 4; (void)l15; (void)q

#define LANE_IDS2() int tz2_ = threadIdx.x; asm volatile("" : "+v"(tz2_)); const int laneb = tz2_ & 63, l15b = laneb & 15, qb = laneb >> 4; (void)l15b; (void)qb

#define STAMP(k) do { } while (0)
#define STAMPC(k) do { } while (0)

template <int I, int NT, int RING, int PC> struct LoaderTail {
  static DEVI void run() {
    if constexpr (I <= NT - 2) { wait_vm<(NT - I - 2) * PC>(); BAR(); LoaderTail<I + 1, NT, RING, PC>::run(); }
  }
};
template <int NT, int RING, int PC, class Issue> DEVI void loader_prefill(Issue issue) {
  constexpr int PRE = RING < NT ? RING : NT;
#pragma unroll 1
  for (int t = 0; t < PRE; ++t) { if (PRE * PC > 63) wait_vm<63 - PC>(); issue(t, t); }
}
template <int NT, int RING, int PC, class Issue> DEVI void loader_run(Issue issue) {
  constexpr int PRE = RING < NT ? RING : NT;
  wait_vm<(PRE - 1) * PC>(); BAR();
  if constexpr (NT > RING) {
    int slot = 0;
#pragma unroll 1
    for (int i = 0; i < NT - RING; ++i) {
      wait_vm<((RING - 2) * PC > 63 - PC ? 63 - PC : (RING - 2) * PC)>(); BAR();
      issue(i + RING, slot);
      slot = (slot + 1 == RING) ? 0 : slot + 1;
    }
  }
  LoaderTail<(NT > RING ? NT - RING : 0), NT, RING, PC>::run();
}
template <int NBAR> DEVI void idle_phase() {
#pragma unroll 1
  for (int i = 0; i < NBAR; ++i) BAR();
}

template <int I, int NMF, int NRD, int PER> struct Interleave {
  static DEVI void run() {
    if constexpr (I < NMF) {
      __builtin_amdgcn_sched_group_barrier(0x8, 1, 0);
      constexpr int left = NRD - I * PER, now = left < PER ? left : PER;
      if constexpr (now > 0) __builtin_amdgcn_sched_group_barrier(0x100, now, 0);
      Interleave<I + 1, NMF, NRD, PER>::run();
    }
  }
};
template <int NMF, int NRD, int PER> DEVI void interleave() { Interleave<0, NMF, NRD, PER>::run(); }

constexpr int P1_SA = 7, P1_DB = 4, P1_DOFF = 114688, P1_X = 65536;
constexpr int P2_RING = 15, P2_X = 51200;
constexpr int P3_RING = 8, P3_X = 114688;
constexpr int P4_SA = 9, P4_DB = 5, P4_DOFF = 73728, P4_X = 40960;
constexpr int L_X1 = 145408;
constexpr int P5_RING = 14, P5_X = 0;

DEVI f32x4 sel4(int c, f32x4 a, f32x4 b) { asm volatile("" : "+v"(a), "+v"(b)); return c ? a : b; }
DEVI float sel1(int c, float a, float b) { asm volatile("" : "+v"(a), "+v"(b)); return c ? a : b; }
DEVI void lds_put(LAS char* p, f32x4 v) { *(LAS f32x4*)p = v; }
DEVI f32x4 lds_get(LAS char* p) { return *(LAS f32x4*)p; }
DEVI u64 epoch_tag(u64 e) {
  return (((u64)(unsigned)__builtin_amdgcn_readfirstlane((unsigned)(e >> 32)) << 32) | (unsigned)__builtin_amdgcn_readfirstlane((unsigned)e)) + 1;
}
DEVI void chan(float& n, float& m, float& M2, float no, float mo, float Mo) {
  const float nt = n + no, d = mo - m, r = __builtin_amdgcn_rcpf(nt);
  M2 = M2 + Mo + d * d * (n * no * r);
  m = m + d * (no * r);
  n = nt;
}

__global__ __launch_bounds__(768, 3) void adaLN_cond__spectral_fnet__mixer_gmlp__prompt_kernel(Params p) {
  extern __shared__ __attribute__((aligned(16))) char smem_[];
  LAS char* lds = (LAS char*)smem_;
  const int tid = threadIdx.x, wid = __builtin_amdgcn_readfirstlane(tid >> 6);
  const int b = blockIdx.x, xg = b & 7, i8 = b >> 3;
  const int g = xg * 4 + (i8 >> 3), j = i8 & 7;
  char* const ws = p.ws;
  const int rot = xg * 4;

  { LANE_IDS(); STAMP(0); STAMPC(30); }
  const u64 Ein = __hip_atomic_load((GAS u64*)(ws + WS_EPOCH), __ATOMIC_RELAXED, __HIP_MEMORY_SCOPE_AGENT);
  if (tid < 32) *((LAS unsigned*)(lds + L_CNT) + tid) = 0u;

  if (wid < 8) {
    const int w = wid, m = w & 3, kh = w >> 2;
    __builtin_amdgcn_s_setprio(2);
    {
      LANE_IDS();
      const rsrc_t rx = mkrs(p.x, SEQ * DM * 2);
      u32x4 v[2];
#pragma unroll
      for (int h = 0; h < 2; ++h) v[h] = ld16<0>(rx, lane < 40 ? (unsigned)((lane + 40 * h) * 16) : OOB, (unsigned)(64 * g + 8 * j + w) * (DM * 2));
      wait_lgkm0();
      BAR();
      float f[16];
#pragma unroll
      for (int h = 0; h < 2; ++h)
#pragma unroll
        for (int i = 0; i < 4; ++i) { f[h * 8 + 2 * i] = bflo(v[h][i]); f[h * 8 + 2 * i + 1] = bfhi(v[h][i]); }
      float s = 0.f;
#pragma unroll
      for (int i = 0; i < 16; ++i) s += f[i];
      const float mean = wave_sum(s) * (1.0f / DM);
      float m2 = 0.f;
#pragma unroll
      for (int i = 0; i < 16; ++i) { f[i] -= mean; m2 += f[i] * f[i]; }
      if (lane >= 40) m2 = 0.f;
      const float rstd = 1.0f / sqrtf(wave_sum(m2) * (1.0f / DM) + 1e-5f);
      if (lane < 40) {
#pragma unroll
        for (int h = 0; h < 2; ++h) {
          u32x4 o;
#pragma unroll
          for (int i = 0; i < 4; ++i) o[i] = pk2(f[h * 8 + 2 * i] * rstd, f[h * 8 + 2 * i + 1] * rstd);
          *(LAS u32x4*)(lds + L_P0T + w * P0T_RS + (lane + 40 * h) * 16) = o;
        }
      }
      wait_lgkm0(); CFENCE();
      if (lane == 0) __hip_atomic_fetch_add((LAS unsigned*)(lds + L_CNT) + 0, 1u, __ATOMIC_RELAXED, __HIP_MEMORY_SCOPE_WORKGROUP);
      for (unsigned spins = 0; spins < SPIN_MAX; ++spins) {
        if (__hip_atomic_load((LAS unsigned*)(lds + L_CNT) + 0, __ATOMIC_RELAXED, __HIP_MEMORY_SCOPE_WORKGROUP) >= 8u) break;
      }
      CFENCE();
      const rsrc_t rxn = mkrs(ws + WS_XNT, 8 * 32 * 10240);
#pragma unroll
      for (int it = 0; it < 2; ++it) {
        const int pc = it == 0 ? q : 4, nf = 5 * w + pc;
        const int qq = l15 >> 2, pp = l15 & 3;
        LAS char* a0 = lds + L_P0T + qq * P0T_RS + (16 * nf + 4 * pp) * 2;
        const u32x2 lo = __builtin_bit_cast(u32x2, __builtin_amdgcn_ds_read_tr16_b64_v4i16((LAS s16x4*)a0));
        const u32x2 hi = __builtin_bit_cast(u32x2, __builtin_amdgcn_ds_read_tr16_b64_v4i16((LAS s16x4*)(a0 + 4 * P0T_RS)));
        const unsigned off = (unsigned)(((w * 32 + g) * 10 + (j >> 2) * 5 + pc) * 1024 + ((j & 3) * 16 + l15) * 16);
        st16<SC1>((u32x4){lo.x, lo.y, hi.x, hi.y}, rxn, (it == 0 || q == 0) ? off : OOB, 0);
      }
    }
    const u64 T = epoch_tag(Ein);
    { LANE_IDS(); wait_vm<0>(); if (arrive_last(lds, 1, 8, lane)) set_flag_rep(ws, WS_FLAG0, g * 8 + j, T, lane); STAMP(1); }

    unsigned z1p[8];

    {
      LANE_IDS();
      f32x4 acc[2][5];
#pragma unroll
      for (int a = 0; a < 2; ++a)
#pragma unroll
        for (int n = 0; n < 5; ++n) acc[a][n] = (f32x4){0.f, 0.f, 0.f, 0.f};
      float rs[2] = {0.f, 0.f};
      unsigned ones = 0x3f803f80u; asm volatile("" : "+v"(ones));
      const unsigned aoff = (m * 16 + l15) * 128 + (((kh * 4 + q) ^ (l15 >> 1)) << 4);
      const unsigned boff = P1_DOFF + kh * 5120 + lane * 16;
      bf16x8 XA[2], XB[5], YA[2], YB[5];
      int sa = 0, sb = 0;
      auto rd = [&](bf16x8(&A)[2], bf16x8(&B)[5]) {
        LAS char* pa = lds + sa * 16384 + aoff;
        LAS char* pb = lds + sb * 10240 + boff;
        A[0] = ldsfrag(pa); A[1] = ldsfrag(pa + 8192);
#pragma unroll
        for (int n = 0; n < 5; ++n) B[n] = ldsfrag(pb + n * 1024);
        sa = (sa + 1 == P1_SA) ? 0 : sa + 1; sb = (sb + 1 == P1_DB) ? 0 : sb + 1;
      };
      auto mm = [&](const bf16x8(&A)[2], const bf16x8(&B)[5]) {
#pragma unroll
        for (int a = 0; a < 2; ++a) {
#pragma unroll
          for (int n = 0; n < 5; ++n) acc[a][n] = mfma(B[n], A[a], acc[a][n]);
          const u32x4 au = __builtin_bit_cast(u32x4, A[a]);
#pragma unroll
          for (int i = 0; i < 4; ++i) { const unsigned ai = au[i]; asm("v_dot2c_f32_bf16 %0, %1, %2" : "+v"(rs[a]) : "v"(ai), "v"(ones)); }
        }
      };
      auto pairs = [&](int n) {
#pragma unroll 1
        for (int i = 0; i < n; ++i) {
          BAR(); rd(YA, YB); mm(XA, XB); interleave<10, 7, 1>(); SCHED(); wait_lgkm0();
          BAR(); rd(XA, XB); mm(YA, YB); interleave<10, 7, 1>(); SCHED(); wait_lgkm0();
        }
      };
      BAR();
      STAMP(2); STAMPC(28);
      rd(XA, XB); wait_lgkm0();
      pairs(4);
      u64 fb[4];
      if (w == 0) {
#pragma unroll
        for (int i = 0; i < 4; ++i) fb[i] = __hip_atomic_load((GAS u64*)(ws + WS_FLAGB + xg * 2048) + i * 64 + lane, __ATOMIC_RELAXED, __HIP_MEMORY_SCOPE_AGENT);
      }
      pairs(4);
      f32x4 mp[2][4];
      const int vs0 = lane < 20 ? 1 : lane < 40 ? 0 : 2, vs1 = lane < 20 ? 4 : 5;
      const int vl = lane < 20 ? lane : lane < 40 ? lane - 20 : lane - 40;
      if (w == 0) {
        if (!__all(fb[0] == T && fb[1] == T && fb[2] == T && fb[3] == T)) poll_flags256(ws, WS_FLAGB + xg * 2048, T, lane);
        CFENCE();
        const rsrc_t rm = mkrs(ws + WS_MODP, 4 * 3840 * 4);
#pragma unroll
        for (int k = 0; k < 4; ++k) {
          mp[0][k] = __builtin_bit_cast(f32x4, ld16<SC1>(rm, lane < 60 ? (unsigned)((vs0 * DM + 80 * j + vl * 4) * 4) : OOB, k * 3840 * 4));
          mp[1][k] = __builtin_bit_cast(f32x4, ld16<SC1>(rm, lane < 40 ? (unsigned)((vs1 * DM + 80 * j + vl * 4) * 4) : OOB, k * 3840 * 4));
        }
      }
      pairs(4);
      if (w == 0) {
        f32x4 s0 = (mp[0][0] + mp[0][1]) + (mp[0][2] + mp[0][3]), s1 = (mp[1][0] + mp[1][1]) + (mp[1][2] + mp[1][3]);
#pragma unroll
        for (int i = 0; i < 4; ++i) {
          s0[i] = rbf(s0[i]); if (lane < 20) s0[i] = rbf(1.0f + s0[i]);
          s1[i] = rbf(s1[i]); if (lane < 20) s1[i] = rbf(1.0f + s1[i]);
        }
        if (lane < 60) lds_put(lds + L_TAB + ((lane < 20 ? 0 : lane < 40 ? 1 : 2) * 80 + vl * 4) * 4, s0);
        if (lane < 40) lds_put(lds + L_TAB + ((lane < 20 ? 3 : 4) * 80 + vl * 4) * 4, s1);
      }
      pairs(3);
      BAR(); rd(YA, YB); mm(XA, XB); interleave<10, 7, 1>(); SCHED(); wait_lgkm0();
      mm(YA, YB);
      STAMP(3); STAMPC(29);
      {
        LAS char* xo = lds + P1_X + w * 6144 + lane * 16;
        LAS char* xi = lds + P1_X + (w ^ 4) * 6144 + lane * 16;
#pragma unroll
        for (int n = 0; n < 5; ++n) lds_put(xo + n * 1024, sel4(kh, acc[0][n], acc[1][n]));
        *(LAS float*)(lds + P1_X + w * 6144 + 5120 + lane * 4) = sel1(kh, rs[0], rs[1]);
        wait_lgkm0();
        BAR();
        float rsum = sel1(kh, rs[1], rs[0]) + *(LAS float*)(lds + P1_X + (w ^ 4) * 6144 + 5120 + lane * 4);
        rsum = xq_sum(rsum);
        u32x2 pkd[5];
#pragma unroll
        for (int n = 0; n < 5; ++n) {
          const f32x4 a = sel4(kh, acc[1][n], acc[0][n]) + lds_get(xi + n * 1024);
          const f32x4 c1 = lds_get(lds + L_TAB + (0 * 80 + 16 * n + 4 * q) * 4), sa4 = lds_get(lds + L_TAB + (1 * 80 + 16 * n + 4 * q) * 4);
          f32x4 v = a * c1 + rsum * sa4;
          if (kh) v = -v;
          pkd[n] = (u32x2){pk2(v[0], v[1]), pk2(v[2], v[3])};
        }
        const rsrc_t rpq = mkrs(ws + WS_PQ, 32 * 40 * 4 * 1024);
        auto pq_off = [&](int nf) { const int c = 10 * j + 2 * nf + (q >> 1); return (unsigned)(((g * 40 + kh * 20 + (c >> 2)) * 4 + m) * 1024 + ((c & 3) * 16 + l15) * 16); };
        const int odd = q & 1;
        st16<SC1>(pair_swap(pkd[0], pkd[1]), rpq, pq_off(odd), 0);
        st16<SC1>(pair_swap(pkd[2], pkd[3]), rpq, pq_off(2 + odd), 0);
        st8<SC1>(pkd[4], rpq, pq_off(4) + odd * 8, 0);
      }
      BAR();
      publish(lds, 3, 8, ws, WS_FLAG1 + g * 256 + j * 8, T, lane);
      STAMP(4);
    }

    {
      LANE_IDS();
      constexpr int NT = 20;
      const int nb = kh ? 3 : 0;
      group_wait(lds, 8, ws, WS_FLAG1 + g * 256, 8, T, w == 0, lane);
      STAMP(5);
      const rsrc_t ra = mkrs(ws + WS_PQ + (size_t)g * 40 * 4 * 1024, 40 * 4 * 1024);
      u32x4 A[NT];
#pragma unroll
      for (int t = 0; t < NT; ++t) A[t] = ld16<SC1>(ra, lane * 16, ((2 * t + kh) * 4 + m) * 1024);
      f32x4 acc[5];
#pragma unroll
      for (int n = 0; n < 5; ++n) acc[n] = (f32x4){0.f, 0.f, 0.f, 0.f};
      const unsigned boff = l15 * 128 + (((kh * 4 + q) ^ (l15 >> 1)) << 4);
      bf16x8 B[2][5];
      auto rd = [&](bf16x8(&Bv)[5], int slot) {
#pragma unroll
        for (int n = 0; n < 5; ++n) Bv[n] = ldsfrag(lds + slot * 10240 + boff + n * 2048);
      };
      BAR();
      STAMP(6);
      rd(B[0], 0); wait_lgkm0();
#pragma unroll
      for (int t = 0; t < NT; ++t) {
        if (t + 1 < NT) { BAR(); rd(B[(t + 1) & 1], (t + 1) % P2_RING); }
#pragma unroll
        for (int n = 0; n < 5; ++n) acc[n] = mfma(B[t & 1][n], asfrag(A[t]), acc[n]);
        interleave<5, 5, 2>(); SCHED(); wait_lgkm0();
      }
      STAMP(7);
      LANE_IDS2();
      const rsrc_t rx = mkrs(p.x, SEQ * DM * 2);
      u32x2 xin[3];
#pragma unroll
      for (int n = 0; n < 3; ++n) xin[n] = ld8<0>(rx, (kh && n == 2) ? OOB : (unsigned)((64 * g + 16 * m + l15b) * (DM * 2) + (80 * j + 16 * (nb + n) + 4 * qb) * 2), 0);
      LAS char* xo = lds + P2_X + w * 3072 + lane * 16;
      LAS char* xi = lds + P2_X + (w ^ 4) * 3072 + lane * 16;
      if (kh) { lds_put(xo, acc[0]); lds_put(xo + 1024, acc[1]); lds_put(xo + 2048, acc[2]); }
      else { lds_put(xo, acc[3]); lds_put(xo + 1024, acc[4]); }
      wait_lgkm0();
      BAR();
      float x1f[3][4]; float s = 0.f;
      u32x2 cp[3]; unsigned x1p[6];
#pragma unroll
      for (int n = 0; n < 3; ++n) {
        const f32x4 y = sel4(kh, acc[n == 2 ? 4 : 3 + n], acc[n]) + lds_get(xi + n * 1024);
        const int col = (kh && n == 2) ? 0 : 16 * (nb + n) + 4 * q;
        const f32x4 ga = lds_get(lds + L_TAB + (2 * 80 + col) * 4), cb1 = lds_get(lds + L_TAB + (3 * 80 + col) * 4);
        x1p[2 * n] = pk2(bflo(xin[n].x) + ga[0] * y[0], bfhi(xin[n].x) + ga[1] * y[1]);
        x1p[2 * n + 1] = pk2(bflo(xin[n].y) + ga[2] * y[2], bfhi(xin[n].y) + ga[3] * y[3]);
        x1f[n][0] = bflo(x1p[2 * n]); x1f[n][1] = bfhi(x1p[2 * n]); x1f[n][2] = bflo(x1p[2 * n + 1]); x1f[n][3] = bfhi(x1p[2 * n + 1]);
        cp[n] = (u32x2){pk2(x1f[n][0] * cb1[0], x1f[n][1] * cb1[1]), pk2(x1f[n][2] * cb1[2], x1f[n][3] * cb1[3])};
        if (!(kh && n == 2)) s += (x1f[n][0] + x1f[n][1]) + (x1f[n][2] + x1f[n][3]);
      }
      const float mean = xq_sum(s) * (kh ? 1.0f / 32.0f : 1.0f / 48.0f);
      float m2 = 0.f;
#pragma unroll
      for (int n = 0; n < 3; ++n)
        if (!(kh && n == 2)) {
#pragma unroll
          for (int r = 0; r < 4; ++r) { const float d = x1f[n][r] - mean; m2 += d * d; }
        }
      m2 = xq_sum(m2);
      const rsrc_t rc = mkrs(ws + WS_X1C, 32 * 20 * 4 * 1024);
      auto c_off = [&](int nf) { const int c = 10 * j + 2 * nf + (q >> 1); return (unsigned)(((g * 20 + (c >> 2)) * 4 + m) * 1024 + ((c & 3) * 16 + l15) * 16); };
      const int odd = q & 1;
      st16<SC1>(pair_swap(cp[0], cp[1]), rc, c_off(nb + odd), 0);
      st8<SC1>(cp[2], rc, kh ? OOB : c_off(2) + odd * 8, 0);
      const rsrc_t rs1 = mkrs(ws + WS_ST1, SEQ * 16 * 8);
      st8<SC1>((u32x2){__float_as_uint(mean), __float_as_uint(m2)}, rs1, q == 0 ? (unsigned)(((64 * g + 16 * m + l15) * 16 + 2 * j + kh) * 8) : OOB, 0);
#pragma unroll
      for (int n = 0; n < 3; ++n) *(LAS u32x2*)(lds + L_X1 + (w * 3 + n) * 512 + lane * 8) = (u32x2){x1p[2 * n], x1p[2 * n + 1]};
      wait_lgkm0();
      BAR();
      publish(lds, 4, 8, ws, WS_FLAG2 + g * 256 + j * 8, T, lane);
      STAMP(8);
    }

    {
      LANE_IDS();
      constexpr int NT = 10;
      const int nb = kh ? 4 : 0, nn = kh ? 3 : 4;
      {
        const rsrc_t ruv = mkrs(ws + WS_UV, 2 * 896 * 16);
        const int col = w * 28 + lane, part = col >= 112 ? 1 : 0, cc = col - 112 * part;
        const unsigned off = lane < 28 ? (unsigned)((part * 896 + 112 * j + cc) * 16) : OOB;
        u32x4 gv;
        for (unsigned spins = 0;; ++spins) {
          gv = ld16<SC1>(ruv, off, 0);
          const bool ok = lane >= 28 || (gv.z == (unsigned)T && gv.w == (unsigned)(T >> 32));
          if (__all(ok)) break;
          if (spins > SPIN_MAX) { note_timeout(ws, 0x300, lane); break; }
          __builtin_amdgcn_s_sleep(1); CFENCE();
        }
        if (lane < 28) { *(LAS unsigned*)(lds + L_UV + col * 4) = gv.x; *(LAS unsigned*)(lds + L_UV + (224 + col) * 4) = gv.y; }
      }
      group_wait(lds, 9, ws, WS_FLAG2 + g * 256, 8, T, w == 0, lane);
      STAMP(9);
      const rsrc_t ra = mkrs(ws + WS_X1C + (size_t)g * 20 * 4 * 1024, 20 * 4 * 1024);
      u32x4 A[NT];
#pragma unroll
      for (int t = 0; t < NT; ++t) A[t] = ld16<SC1>(ra, lane * 16, ((2 * t + kh) * 4 + m) * 1024);
      const rsrc_t rs1 = mkrs(ws + WS_ST1, SEQ * 16 * 8);
      const unsigned s1o = (unsigned)(((64 * g + 16 * m + l15) * 16 + 4 * q) * 8);
      const u32x4 st0 = ld16<SC1>(rs1, s1o, 0), st1 = ld16<SC1>(rs1, s1o + 16, 0);
      f32x4 acc[7];
      bf16x8 B[2][7];
      auto rd = [&](bf16x8(&Bv)[7], int slot) {
#pragma unroll
        for (int n = 0; n < 7; ++n) Bv[n] = ldsfrag(lds + slot * 14336 + kh * 7168 + n * 1024 + lane * 16);
      };
      wait_lgkm0();
      BAR();
      STAMP(10);
      rd(B[0], 0); wait_lgkm0();
      float mean1, rstd1;
      {
        float n = 48.f, mm = __uint_as_float(st0.x), M2 = __uint_as_float(st0.y);
        chan(n, mm, M2, 32.f, __uint_as_float(st0.z), __uint_as_float(st0.w));
        chan(n, mm, M2, 48.f, __uint_as_float(st1.x), __uint_as_float(st1.y));
        chan(n, mm, M2, 32.f, __uint_as_float(st1.z), __uint_as_float(st1.w));
        chan(n, mm, M2, n, __shfl_xor(mm, 16), __shfl_xor(M2, 16));
        chan(n, mm, M2, n, __shfl_xor(mm, 32), __shfl_xor(M2, 32));
        mean1 = mm; rstd1 = 1.0f / sqrtf(M2 * (1.0f / DM) + 1e-5f);
      }
      LAS char* xo = lds + P3_X + (kh ? 12288 + m * 4096 : m * 3072) + lane * 16;
      LAS char* xi = lds + P3_X + (kh ? m * 3072 : 12288 + m * 4096) + lane * 16;
#pragma unroll
      for (int n = 0; n < 7; ++n) acc[n] = (f32x4){0.f, 0.f, 0.f, 0.f};
#pragma unroll
      for (int t = 0; t < NT; ++t) {
        BAR(); if (t + 1 < NT) rd(B[(t + 1) & 1], (t + 1) % P3_RING);
#pragma unroll
        for (int n = 0; n < 7; ++n) acc[n] = mfma(asfrag(A[t]), B[t & 1][n], acc[n]);
        interleave<7, 7, 2>(); SCHED(); wait_lgkm0();
      }
      STAMP(25);
      if (kh) {
#pragma unroll
        for (int n = 0; n < 4; ++n) lds_put(xo + n * 1024, acc[n]);
      } else {
#pragma unroll
        for (int n = 0; n < 3; ++n) lds_put(xo + n * 1024, acc[4 + n]);
      }
      wait_lgkm0();
      BAR();
      u32x2 z2p[4];
      const int nval = (j == 7 && kh) ? 1 : nn;
      {
        float mr[4], rr[4];
#pragma unroll
        for (int r = 0; r < 4; ++r) { mr[r] = __shfl(mean1, 4 * q + r); rr[r] = __shfl(rstd1, 4 * q + r); }
        float zs[4] = {0.f, 0.f, 0.f, 0.f};
#pragma unroll
        for (int n = 0; n < 4; ++n) {
          const int nc = (kh && n == 3) ? 0 : n;
          const f32x4 a = sel4(kh, acc[n == 3 ? 6 : 4 + n], acc[n]) + lds_get(xi + nc * 1024);
          const int col = 16 * (nb + nc) + l15;
          const float u = *(LAS float*)(lds + L_UV + col * 4), v = *(LAS float*)(lds + L_UV + (224 + col) * 4);
          float z[4];
#pragma unroll
          for (int r = 0; r < 4; ++r) z[r] = gelu_tanh(rr[r] * (a[r] - mr[r] * u) + v);
          z2p[n] = (u32x2){pk2(z[0], z[1]), pk2(z[2], z[3])};
          if (n < nval) { zs[0] += bflo(z2p[n].x); zs[1] += bfhi(z2p[n].x); zs[2] += bflo(z2p[n].y); zs[3] += bfhi(z2p[n].y); }
        }
        const float inv = 1.0f / (16.0f * nval);
        float mz[4], M2[4];
#pragma unroll
        for (int r = 0; r < 4; ++r) { mz[r] = row16_sum(zs[r]) * inv; M2[r] = 0.f; }
#pragma unroll
        for (int n = 0; n < 4; ++n) if (n < nval) {
          const float d0 = bflo(z2p[n].x) - mz[0], d1 = bfhi(z2p[n].x) - mz[1], d2 = bflo(z2p[n].y) - mz[2], d3 = bfhi(z2p[n].y) - mz[3];
          M2[0] += d0 * d0; M2[1] += d1 * d1; M2[2] += d2 * d2; M2[3] += d3 * d3;
        }
#pragma unroll
        for (int r = 0; r < 4; ++r) M2[r] = row16_sum(M2[r]);
        const float ms = l15 == 0 ? mz[0] : l15 == 1 ? mz[1] : l15 == 2 ? mz[2] : mz[3];
        const float Ms = l15 == 0 ? M2[0] : l15 == 1 ? M2[1] : l15 == 2 ? M2[2] : M2[3];
        const rsrc_t rs2 = mkrs(ws + WS_ST2, SEQ * 16 * 16);
        st16<SC1>((u32x4){__float_as_uint(ms), __float_as_uint(Ms), (unsigned)T, (unsigned)(T >> 32)}, rs2,
                  l15 < 4 ? (unsigned)(((64 * g + 16 * m + 4 * q + l15) * 16 + 2 * j + kh) * 16) : OOB, 0);
      }
      STAMP(11);
      rd(B[0], NT % P3_RING); wait_lgkm0();
#pragma unroll
      for (int n = 0; n < 7; ++n) acc[n] = (f32x4){0.f, 0.f, 0.f, 0.f};
#pragma unroll
      for (int t = 0; t < NT; ++t) {
        if (t + 1 < NT) { BAR(); rd(B[(t + 1) & 1], (NT + t + 1) % P3_RING); }
#pragma unroll
        for (int n = 0; n < 7; ++n) acc[n] = mfma(B[t & 1][n], asfrag(A[t]), acc[n]);
        interleave<7, 7, 2>(); SCHED(); wait_lgkm0();
      }
      STAMP(12);
      const rsrc_t rs2 = mkrs(ws + WS_ST2, SEQ * 16 * 16);
      const unsigned soff = (unsigned)(((64 * g + 16 * m + l15) * 16 + 4 * q) * 16);
      u32x4 gs[4];
#pragma unroll
      for (int i = 0; i < 4; ++i) gs[i] = ld16<SC1>(rs2, soff + 16 * i, 0);
      if (kh) {
#pragma unroll
        for (int n = 0; n < 4; ++n) lds_put(xo + n * 1024, acc[n]);
      } else {
#pragma unroll
        for (int n = 0; n < 3; ++n) lds_put(xo + n * 1024, acc[4 + n]);
      }
      wait_lgkm0();
      BAR();
#pragma unroll
      for (int n = 0; n < 4; ++n) {
        const int nc = (kh && n == 3) ? 0 : n;
        const f32x4 a = sel4(kh, acc[n == 3 ? 6 : 4 + n], acc[n]) + lds_get(xi + nc * 1024);
        const int col = 112 + 16 * (nb + nc) + 4 * q;
        const f32x4 u = lds_get(lds + L_UV + col * 4), v = lds_get(lds + L_UV + (224 + col) * 4);
        float z[4];
#pragma unroll
        for (int r = 0; r < 4; ++r) z[r] = gelu_tanh(rstd1 * (a[r] - mean1 * u[r]) + v[r]);
        z1p[2 * n] = pk2(z[0], z[1]); z1p[2 * n + 1] = pk2(z[2], z[3]);
      }
#pragma unroll
      for (int i = 0; i < 8; ++i) asm volatile("" : "+v"(z1p[i]));
      STAMP(26);
      for (unsigned spins = 0;; ++spins) {
        bool ok = true;
#pragma unroll
        for (int i = 0; i < 4; ++i) ok &= gs[i].z == (unsigned)T && gs[i].w == (unsigned)(T >> 32);
        if (__all(ok)) break;
        if (spins > SPIN_MAX) { note_timeout(ws, 0x400, lane); break; }
        __builtin_amdgcn_s_sleep(1); CFENCE();
#pragma unroll
        for (int i = 0; i < 4; ++i) gs[i] = ld16<SC1>(rs2, soff + 16 * i, 0);
      }
      float mean2, rstd2;
      {
        float n = 64.f, mm = __uint_as_float(gs[0].x), M2 = __uint_as_float(gs[0].y);
        chan(n, mm, M2, 48.f, __uint_as_float(gs[1].x), __uint_as_float(gs[1].y));
        chan(n, mm, M2, 64.f, __uint_as_float(gs[2].x), __uint_as_float(gs[2].y));
        chan(n, mm, M2, q == 3 ? 16.f : 48.f, __uint_as_float(gs[3].x), __uint_as_float(gs[3].y));
        chan(n, mm, M2, __shfl_xor(n, 16), __shfl_xor(mm, 16), __shfl_xor(M2, 16));
        chan(n, mm, M2, __shfl_xor(n, 32), __shfl_xor(mm, 32), __shfl_xor(M2, 32));
        mean2 = mm; rstd2 = 1.0f / sqrtf(M2 * (1.0f / FH) + 1e-5f);
      }
      {
        float mr[4], rr[4];
#pragma unroll
        for (int r = 0; r < 4; ++r) { mr[r] = __shfl(mean2, 4 * q + r); rr[r] = __shfl(rstd2, 4 * q + r); }
        u32x2 zn[4];
#pragma unroll
        for (int n = 0; n < 4; ++n)
          zn[n] = (u32x2){pk2((bflo(z2p[n].x) - mr[0]) * rr[0], (bfhi(z2p[n].x) - mr[1]) * rr[1]), pk2((bflo(z2p[n].y) - mr[2]) * rr[2], (bfhi(z2p[n].y) - mr[3]) * rr[3])};
        const rsrc_t rz2 = mkrs(ws + WS_Z2T, 8 * 32 * 14336);
        auto z_off = [&](int nf) { return (unsigned)(((j * 32 + g) * 14 + (m >> 1) * 7 + nf) * 1024 + ((2 * (m & 1) + (q >> 1)) * 16 + l15) * 16); };
        const int odd = q & 1;
        st16<SC1>(pair_swap(zn[0], zn[1]), rz2, z_off(nb + odd), 0);
        if (kh) st8<SC1>(zn[2], rz2, z_off(6) + odd * 8, 0);
        else st16<SC1>(pair_swap(zn[2], zn[3]), rz2, z_off(2 + odd), 0);
      }
      STAMP(27);
      BAR();
      publish(lds, 5, 8, ws, WS_FLAG3 + j * 256 + g * 8, T, lane);
      STAMP(13);
    }

    {
      LANE_IDS();
      const int mh = m >> 1, k4 = m & 1, nh = kh;
      f32x4 acc[2][4];
#pragma unroll
      for (int a = 0; a < 2; ++a)
#pragma unroll
        for (int n = 0; n < 4; ++n) acc[a][n] = (f32x4){0.f, 0.f, 0.f, 0.f};
      const unsigned aoff = (mh * 32 + l15) * 128 + (((k4 * 4 + q) ^ (l15 >> 1)) << 4);
      const unsigned boff = P4_DOFF + k4 * 7168 + nh * 4096 + lane * 16;
      const rsrc_t rb = mkrs(p.bsp, SEQ * 2);
      const float bs = bflo(ld2<0>(rb, (64 * g + 16 * m + l15) * 2, 0));
      bf16x8 XA[2], XB[4], YA[2], YB[4];
      int sa = 0, sb = 0;
      if (nh == 0) {
        auto rd = [&](bf16x8(&A)[2], bf16x8(&B)[4]) {
          LAS char* pa = lds + sa * 8192 + aoff;
          LAS char* pb = lds + sb * 14336 + boff;
          A[0] = ldsfrag(pa); A[1] = ldsfrag(pa + 2048);
#pragma unroll
          for (int n = 0; n < 4; ++n) B[n] = ldsfrag(pb + n * 1024);
          sa = (sa + 1 == P4_SA) ? 0 : sa + 1; sb = (sb + 1 == P4_DB) ? 0 : sb + 1;
        };
        auto mm = [&](const bf16x8(&A)[2], const bf16x8(&B)[4]) {
#pragma unroll
          for (int a = 0; a < 2; ++a)
#pragma unroll
            for (int n = 0; n < 4; ++n) acc[a][n] = mfma(B[n], A[a], acc[a][n]);
        };
        BAR();
        STAMP(14);
        rd(XA, XB); wait_lgkm0();
#pragma unroll 1
        for (int i = 0; i < 15; ++i) {
          BAR(); rd(YA, YB); mm(XA, XB); interleave<8, 6, 1>(); SCHED(); wait_lgkm0();
          BAR(); rd(XA, XB); mm(YA, YB); interleave<8, 6, 1>(); SCHED(); wait_lgkm0();
        }
        BAR(); rd(YA, YB); mm(XA, XB); interleave<8, 6, 1>(); SCHED(); wait_lgkm0();
        mm(YA, YB);
      } else {
        auto rd = [&](bf16x8(&A)[2], bf16x8(&B)[4]) {
          LAS char* pa = lds + sa * 8192 + aoff;
          LAS char* pb = lds + sb * 14336 + boff;
          A[0] = ldsfrag(pa); A[1] = ldsfrag(pa + 2048);
#pragma unroll
          for (int n = 0; n < 3; ++n) B[n] = ldsfrag(pb + n * 1024);
          sa = (sa + 1 == P4_SA) ? 0 : sa + 1; sb = (sb + 1 == P4_DB) ? 0 : sb + 1;
        };
        auto mm = [&](const bf16x8(&A)[2], const bf16x8(&B)[4]) {
#pragma unroll
          for (int a = 0; a < 2; ++a)
#pragma unroll
            for (int n = 0; n < 3; ++n) acc[a][n] = mfma(B[n], A[a], acc[a][n]);
        };
        BAR();
        STAMP(14);
        rd(XA, XB); wait_lgkm0();
#pragma unroll 1
        for (int i = 0; i < 15; ++i) {
          BAR(); rd(YA, YB); mm(XA, XB); interleave<6, 5, 1>(); SCHED(); wait_lgkm0();
          BAR(); rd(XA, XB); mm(YA, YB); interleave<6, 5, 1>(); SCHED(); wait_lgkm0();
        }
        BAR(); rd(YA, YB); mm(XA, XB); interleave<6, 5, 1>(); SCHED(); wait_lgkm0();
        mm(YA, YB);
      }
      STAMP(15);
      LAS char* xo = lds + P4_X + w * 4096 + lane * 16;
      LAS char* xi = lds + P4_X + (w ^ 1) * 4096 + lane * 16;
#pragma unroll
      for (int n = 0; n < 4; ++n) lds_put(xo + n * 1024, sel4(k4, acc[0][n], acc[1][n]));
      wait_lgkm0();
      BAR();
      u32x2 up[4];
#pragma unroll
      for (int n = 0; n < 4; ++n) {
        const f32x4 gv = sel4(k4, acc[1][n], acc[0][n]) + lds_get(xi + n * 1024);
        up[n] = (u32x2){pk2(bflo(z1p[2 * n]) * rbf(rbf(gv[0]) + bs), bfhi(z1p[2 * n]) * rbf(rbf(gv[1]) + bs)),
                        pk2(bflo(z1p[2 * n + 1]) * rbf(rbf(gv[2]) + bs), bfhi(z1p[2 * n + 1]) * rbf(rbf(gv[3]) + bs))};
      }
      const rsrc_t ru = mkrs(ws + WS_U, 32 * 28 * 4 * 1024);
      auto u_off = [&](int nf) { const int c = 14 * j + 2 * nf + (q >> 1); return (unsigned)(((g * 28 + (c >> 2)) * 4 + m) * 1024 + ((c & 3) * 16 + l15) * 16); };
      const int odd = q & 1;
      st16<SC1>(pair_swap(up[0], up[1]), ru, u_off(4 * nh + odd), 0);
      if (nh) st8<SC1>(up[2], ru, u_off(6) + odd * 8, 0);
      else st16<SC1>(pair_swap(up[2], up[3]), ru, u_off(2 + odd), 0);
      STAMP(24);
      BAR();
      publish(lds, 6, 8, ws, WS_FLAG4 + g * 256 + j * 8, T, lane);
      if (b == 0 && w == 0 && lane == 0) __hip_atomic_store((GAS u64*)(ws + WS_EPOCH), T, __ATOMIC_RELAXED, __HIP_MEMORY_SCOPE_AGENT);
      STAMP(16);
    }

    {
      LANE_IDS();
      constexpr int NT = 14;
      const int nb = kh ? 3 : 0;
      group_wait(lds, 10, ws, WS_FLAG4 + g * 256, 8, T, w == 0, lane);
      STAMP(17);
      const rsrc_t ra = mkrs(ws + WS_U + (size_t)g * 28 * 4 * 1024, 28 * 4 * 1024);
      u32x4 A[NT];
#pragma unroll
      for (int t = 0; t < NT; ++t) A[t] = ld16<SC1>(ra, lane * 16, ((2 * t + kh) * 4 + m) * 1024);
      f32x4 acc[5];
#pragma unroll
      for (int n = 0; n < 5; ++n) acc[n] = (f32x4){0.f, 0.f, 0.f, 0.f};
      bf16x8 B[2][5];
      auto rd = [&](bf16x8(&Bv)[5], int slot) {
#pragma unroll
        for (int n = 0; n < 5; ++n) Bv[n] = ldsfrag(lds + slot * 10240 + kh * 5120 + n * 1024 + lane * 16);
      };
      BAR();
      STAMP(18);
      rd(B[0], 0); wait_lgkm0();
#pragma unroll
      for (int t = 0; t < NT; ++t) {
        if (t + 1 < NT) { BAR(); rd(B[(t + 1) & 1], t + 1); }
#pragma unroll
        for (int n = 0; n < 5; ++n) acc[n] = mfma(B[t & 1][n], asfrag(A[t]), acc[n]);
        interleave<5, 5, 2>(); SCHED(); wait_lgkm0();
      }
      STAMP(19);
      LAS char* xo = lds + P5_X + w * 3072 + lane * 16;
      LAS char* xi = lds + P5_X + (w ^ 4) * 3072 + lane * 16;
      if (kh) { lds_put(xo, acc[0]); lds_put(xo + 1024, acc[1]); lds_put(xo + 2048, acc[2]); }
      else { lds_put(xo, acc[3]); lds_put(xo + 1024, acc[4]); }
      wait_lgkm0();
      BAR();
      u32x2 op[3];
#pragma unroll
      for (int n = 0; n < 3; ++n) {
        const u32x2 x1v = *(LAS u32x2*)(lds + L_X1 + (w * 3 + n) * 512 + lane * 8);
        const unsigned x1p[6] = {x1v.x, x1v.y, x1v.x, x1v.y, x1v.x, x1v.y};
        const f32x4 o = sel4(kh, acc[n == 2 ? 4 : 3 + n], acc[n]) + lds_get(xi + n * 1024);
        const int col = (kh && n == 2) ? 0 : 16 * (nb + n) + 4 * q;
        const f32x4 gb = lds_get(lds + L_TAB + (4 * 80 + col) * 4);
        op[n] = (u32x2){pk2(bflo(x1p[2 * n]) + rbf(gb[0] * rbf(o[0])), bfhi(x1p[2 * n]) + rbf(gb[1] * rbf(o[1]))),
                        pk2(bflo(x1p[2 * n + 1]) + rbf(gb[2] * rbf(o[2])), bfhi(x1p[2 * n + 1]) + rbf(gb[3] * rbf(o[3])))};
      }
      const rsrc_t ro = mkrs(p.out, SEQ * DM * 2);
      const unsigned rowb = (unsigned)((64 * g + 16 * m + l15) * (DM * 2) + 80 * j * 2);
      const int odd = q & 1;
      st16<0>(pair_swap(op[0], op[1]), ro, rowb + (16 * (nb + odd) + 8 * (q >> 1)) * 2, 0);
      st8<0>(op[2], ro, kh ? OOB : rowb + (32 + 4 * q) * 2, 0);
      STAMP(20); STAMPC(31);
    }
  } else if (wid < 10) {
    LANE_IDS();
    const int h = wid - 8;
    const unsigned sw0 = (unsigned)(((lane & 7) ^ (lane >> 4)) * 16), sw1 = sw0 ^ 64;
    wait_lgkm0();
    BAR();
    {
      const rsrc_t r = mkrs((h ? p.wsim : p.wsre) + (size_t)g * 64 * SEQ, 64 * SEQ * 2);
      const unsigned v0 = (lane >> 3) * 4096 + sw0, v1 = (lane >> 3) * 4096 + sw1;
      auto issue = [&](int t, int slot) {
        const unsigned k = (unsigned)((t + rot) & 31) * 128;
#pragma unroll
        for (int pc = 0; pc < 8; ++pc) dma16<0>(r, lds + slot * 16384 + (h * 8 + pc) * 1024, (pc & 1) ? v1 : v0, k + pc * 32768);
      };
      loader_prefill<32, P1_SA, 8>(issue);
      STAMP(1);
      if (h == 0) {
        if (b < 240) {
          const int cg = b % 60, kq = b / 60;
          const rsrc_t rw = mkrs(p.wmod, DM * 3840 * 2), rc = mkrs(p.c, DM * 2);
          u32x4 wv[20]; unsigned cv[20];
#pragma unroll
          for (int it = 0; it < 20; ++it) {
            const int row = kq * 160 + it * 8 + (lane >> 3);
            wv[it] = ld16<0>(rw, (unsigned)(row * 7680 + cg * 128 + (lane & 7) * 16), 0);
            cv[it] = ld2<0>(rc, row * 2, 0);
          }
          float a8[8] = {0.f, 0.f, 0.f, 0.f, 0.f, 0.f, 0.f, 0.f};
#pragma unroll
          for (int it = 0; it < 20; ++it) {
            const float cf = bflo(cv[it]);
            const float sl = rbf(cf * __builtin_amdgcn_rcpf(1.0f + __builtin_amdgcn_exp2f(-1.4426950408889634f * cf)));
#pragma unroll
            for (int i = 0; i < 4; ++i) { a8[2 * i] += sl * bflo(wv[it][i]); a8[2 * i + 1] += sl * bfhi(wv[it][i]); }
          }
#pragma unroll
          for (int i = 0; i < 8; ++i) { a8[i] += __shfl_xor(a8[i], 8); a8[i] += __shfl_xor(a8[i], 16); a8[i] += __shfl_xor(a8[i], 32); }
          const rsrc_t rm = mkrs(ws + WS_MODP, 4 * 3840 * 4);
          const unsigned mo = (unsigned)((kq * 3840 + cg * 64 + (lane & 7) * 8) * 4);
          st16<SC1>((u32x4){__float_as_uint(a8[0]), __float_as_uint(a8[1]), __float_as_uint(a8[2]), __float_as_uint(a8[3])}, rm, lane < 8 ? mo : OOB, 0);
          st16<SC1>((u32x4){__float_as_uint(a8[4]), __float_as_uint(a8[5]), __float_as_uint(a8[6]), __float_as_uint(a8[7])}, rm, lane < 8 ? mo + 16 : OOB, 0);
        }
        wait_vm<0>();
        if (arrive_last(lds, 7, 3, lane)) set_flag_rep(ws, WS_FLAGB, g * 8 + j, epoch_tag(Ein), lane);
      }
      STAMP(6);
      loader_run<32, P1_SA, 8>(issue);
      BAR(); BAR();
    }
    {
      const unsigned v0 = (lane >> 3) * 1280 + (h ? sw1 : sw0), v1 = (lane >> 3) * 1280 + (h ? sw0 : sw1);
      auto issue = [&](int t, int slot) {
        const int im = t >= 10;
        const rsrc_t r = mkrs((im ? p.wdim : p.wdre) + (size_t)j * 80 * DM, 80 * DM * 2);
        const unsigned k = (unsigned)(t - 10 * im) * 128 + h * 5 * 8 * 1280;
#pragma unroll
        for (int pc = 0; pc < 5; ++pc) dma16<0>(r, lds + slot * 10240 + (h * 5 + pc) * 1024, (pc & 1) ? v1 : v0, k + pc * 8 * 1280);
      };
      loader_prefill<20, P2_RING, 5>(issue);
      STAMP(2);
      loader_run<20, P2_RING, 5>(issue);
      BAR(); BAR();
    }
    {
      const rsrc_t r = mkrs(ws + WS_WINR + (size_t)j * 20 * 14336, 20 * 14336);
      auto issue = [&](int t, int slot) {
#pragma unroll
        for (int pc = 0; pc < 7; ++pc) dma16<SC1>(r, lds + slot * 14336 + (h * 7 + pc) * 1024, lane * 16, (unsigned)(t * 14336 + (h * 7 + pc) * 1024));
      };
      loader_prefill<20, P3_RING, 7>(issue);
      STAMP(3);
      wait_vm<(P3_RING - 1) * 7>(); BAR();
      int slot = 0;
#pragma unroll 1
      for (int i = 0; i < 20 - P3_RING; ++i) {
        wait_vm<(P3_RING - 2) * 7>(); BAR();
        issue(i + P3_RING, slot);
        slot = (slot + 1 == P3_RING) ? 0 : slot + 1;
        if (i == 9) BAR();
      }
      LoaderTail<20 - P3_RING, 20, P3_RING, 7>::run();
      BAR(); BAR();
    }
    {
      const rsrc_t r = mkrs(p.wsp + (size_t)g * 64 * SEQ, 64 * SEQ * 2);
      const unsigned v0 = (lane >> 3) * 4096 + sw0, v1 = (lane >> 3) * 4096 + sw1;
      auto issue = [&](int t, int slot) {
        const unsigned k = (unsigned)((t + rot) & 31) * 128 + h * 4 * 32768;
#pragma unroll
        for (int pc = 0; pc < 4; ++pc) dma16<0>(r, lds + slot * 8192 + (h * 4 + pc) * 1024, (pc & 1) ? v1 : v0, k + pc * 32768);
      };
      loader_prefill<32, P4_SA, 4>(issue);
      STAMP(4);
      loader_run<32, P4_SA, 4>(issue);
      BAR(); BAR();
    }
    {
      const rsrc_t r = mkrs(ws + WS_WOUTR + (size_t)j * 14 * 10240, 14 * 10240);
      auto issue = [&](int t, int slot) {
#pragma unroll
        for (int pc = 0; pc < 5; ++pc) dma16<SC1>(r, lds + slot * 10240 + (h * 5 + pc) * 1024, lane * 16, (unsigned)(t * 10240 + (h * 5 + pc) * 1024));
      };
      loader_prefill<14, P5_RING, 5>(issue);
      STAMP(5);
      loader_run<14, P5_RING, 5>(issue);
      BAR();
    }
  } else {
    LANE_IDS();
    const int h = wid - 10;
    wait_lgkm0();
    BAR();
    {
      LAS char* sc = lds + L_RT + h * 10240;
      const int u = b * 2 + h;
      const int qq = l15 >> 2, pp = l15 & 3;
      if (u < 320) {
        const int jp = u / 20, kf = u - 20 * jp, jj = jp >> 1, part = jp & 1;
        const rsrc_t r = mkrs(p.win, DM * FF * 2);
#pragma unroll
        for (int pc = 0; pc < 7; ++pc) {
          const int idx = pc * 64 + lane, row = idx / 14, ch = idx - 14 * row;
          const bool ok = 112 * jj + 8 * ch < FH;
          dma16<0>(r, sc + pc * 1024, ok ? (unsigned)((32 * kf + row) * (FF * 2) + (part ? 0 : FH * 2) + 224 * jj + ch * 16) : OOB, 0);
        }
        wait_vm<0>(); CFENCE();
        const rsrc_t ro = mkrs(ws + WS_WINR, 8 * 20 * 14336);
#pragma unroll
        for (int nf = 0; nf < 7; ++nf) {
          LAS char* a0 = sc + (8 * q + qq) * 224 + (16 * nf + 4 * pp) * 2;
          const u32x2 lo = __builtin_bit_cast(u32x2, __builtin_amdgcn_ds_read_tr16_b64_v4i16((LAS s16x4*)a0));
          const u32x2 hi = __builtin_bit_cast(u32x2, __builtin_amdgcn_ds_read_tr16_b64_v4i16((LAS s16x4*)(a0 + 4 * 224)));
          st16<SC1>((u32x4){lo.x, lo.y, hi.x, hi.y}, ro, (unsigned)((((jj * 2 + part) * 20 + kf) * 7 + nf) * 1024 + lane * 16), 0);
        }
      } else if (u < 432) {
        const int uu = u - 320, jj = uu / 14, kp = uu - 14 * jj;
        const rsrc_t r = mkrs(p.wout, FH * DM * 2);
#pragma unroll
        for (int pc = 0; pc < 10; ++pc) {
          const int idx = pc * 64 + lane, row = idx / 10, ch = idx - 10 * row;
          dma16<0>(r, sc + pc * 1024, (unsigned)((64 * kp + row) * (DM * 2) + 160 * jj + ch * 16), 0);
        }
        wait_vm<0>(); CFENCE();
        const rsrc_t ro = mkrs(ws + WS_WOUTR, 8 * 14 * 10240);
#pragma unroll
        for (int f = 0; f < 10; ++f) {
          const int kk = f / 5, nf = f - 5 * kk;
          LAS char* a0 = sc + (32 * kk + 8 * q + qq) * 160 + (16 * nf + 4 * pp) * 2;
          const u32x2 lo = __builtin_bit_cast(u32x2, __builtin_amdgcn_ds_read_tr16_b64_v4i16((LAS s16x4*)a0));
          const u32x2 hi = __builtin_bit_cast(u32x2, __builtin_amdgcn_ds_read_tr16_b64_v4i16((LAS s16x4*)(a0 + 4 * 160)));
          st16<SC1>((u32x4){lo.x, lo.y, hi.x, hi.y}, ro, (unsigned)(((jj * 14 + kp) * 10 + f) * 1024 + lane * 16), 0);
        }
      }
      wait_lgkm0(); CFENCE();
    }
    const u64 T = epoch_tag(Ein);
    STAMP(1);
    {
      if (h == 0) { poll_flags256(ws, WS_FLAG0 + xg * 2048, T, lane); lds_signal(lds, 11, lane); } else lds_wait(lds, 11);
      STAMP(2);
      const rsrc_t r = mkrs(ws + WS_XNT + (size_t)j * 32 * 10240, 32 * 10240);
      auto issue = [&](int t, int slot) {
        const unsigned k = (unsigned)((t + rot) & 31) * 10240;
#pragma unroll
        for (int pc = 0; pc < 5; ++pc) dma16<SC1>(r, lds + P1_DOFF + slot * 10240 + (h * 5 + pc) * 1024, lane * 16, k + (h * 5 + pc) * 1024);
      };
      loader_prefill<32, P1_DB, 5>(issue);
      wait_vm<(P1_DB - 1) * 5>();
      if (arrive_last(lds, 7, 3, lane)) set_flag_rep(ws, WS_FLAGB, g * 8 + j, T, lane);
      loader_run<32, P1_DB, 5>(issue);
      BAR(); BAR();
    }
    if (h == 0 && b < 112) {
      const int jj = b / 14, pn = b - 14 * jj, part = pn / 7, nf = pn - 7 * part;
      const rsrc_t rw = mkrs(ws + WS_WINR, 8 * 20 * 14336), rm = mkrs(ws + WS_MODP, 4 * 3840 * 4);
      u32x4 wf[20];
#pragma unroll
      for (int k = 0; k < 20; ++k) wf[k] = ld16<SC1>(rw, lane * 16, (unsigned)((((jj * 2 + part) * 20 + k) * 7 + nf) * 1024));
#pragma unroll
      for (int it = 0; it < 5; ++it) {
        const int c4 = (it * 64 + lane) * 4, isS = c4 >= DM, cc = c4 - DM * isS;
        const unsigned o = (unsigned)(((isS ? 3 : 4) * DM + cc) * 4);
        const f32x4 m0 = __builtin_bit_cast(f32x4, ld16<SC1>(rm, o, 0)), m1 = __builtin_bit_cast(f32x4, ld16<SC1>(rm, o, 3840 * 4)),
                    m2 = __builtin_bit_cast(f32x4, ld16<SC1>(rm, o, 2 * 3840 * 4)), m3 = __builtin_bit_cast(f32x4, ld16<SC1>(rm, o, 3 * 3840 * 4));
        f32x4 s = (m0 + m1) + (m2 + m3);
#pragma unroll
        for (int i = 0; i < 4; ++i) { s[i] = rbf(s[i]); if (!isS) s[i] = 1.0f + s[i]; }
        *(LAS u32x2*)(lds + L_TABE + (isS * DM + cc) * 2) = (u32x2){pk2(s[0], s[1]), pk2(s[2], s[3])};
      }
      wait_lgkm0(); CFENCE();
      f32x4 acc = {0.f, 0.f, 0.f, 0.f};
#pragma unroll
      for (int k = 0; k < 20; ++k) {
        u32x4 e = *(LAS u32x4*)(lds + L_TABE + ((l15 & 1) * DM + 32 * k + 8 * q) * 2);
        if (l15 >= 2) e = (u32x4){0u, 0u, 0u, 0u};
        acc = mfma(asfrag(e), asfrag(wf[k]), acc);
      }
      const rsrc_t ruv = mkrs(ws + WS_UV, 2 * 896 * 16);
      st16<SC1>((u32x4){__float_as_uint(acc[0]), __float_as_uint(acc[1]), (unsigned)T, (unsigned)(T >> 32)}, ruv,
                q == 0 ? (unsigned)((part * 896 + 112 * jj + 16 * nf + l15) * 16) : OOB, 0);
    }
    STAMP(3);
    idle_phase<22>();
    idle_phase<23>();
    {
      group_wait(lds, 12, ws, WS_FLAG3 + j * 256, 32, T, h == 0, lane);
      STAMP(4);
      const rsrc_t r = mkrs(ws + WS_Z2T + (size_t)j * 32 * 14336, 32 * 14336);
      auto issue = [&](int t, int slot) {
        const unsigned k = (unsigned)((t + rot) & 31) * 14336;
#pragma unroll
        for (int pc = 0; pc < 7; ++pc) dma16<SC1>(r, lds + P4_DOFF + slot * 14336 + (h * 7 + pc) * 1024, lane * 16, k + (h * 7 + pc) * 1024);
      };
      loader_prefill<32, P4_DB, 7>(issue);
      loader_run<32, P4_DB, 7>(issue);
      BAR(); BAR();
    }
    idle_phase<15>();
  }
}

extern "C" void kernel_launch(void* const* d_in, const int* in_sizes, int n_in, void* d_out, int out_size, void* d_ws, size_t ws_size,
                              hipStream_t stream) {
  Params p;
  p.x = (const u16*)d_in[0]; p.wsre = (const u16*)d_in[1]; p.wsim = (const u16*)d_in[2]; p.wdre = (const u16*)d_in[3]; p.wdim = (const u16*)d_in[4];
  p.win = (const u16*)d_in[6]; p.wsp = (const u16*)d_in[7]; p.bsp = (const u16*)d_in[8]; p.wout = (const u16*)d_in[9];
  p.c = (const u16*)d_in[10]; p.wmod = (const u16*)d_in[11];
  p.out = (u16*)d_out; p.ws = (char*)d_ws + ((ws_size - WS_USED) & ~(size_t)0x1fffff);
  (void)hipFuncSetAttribute((const void*)adaLN_cond__spectral_fnet__mixer_gmlp__prompt_kernel, hipFuncAttributeMaxDynamicSharedMemorySize, LDS_BYTES);
  adaLN_cond__spectral_fnet__mixer_gmlp__prompt_kernel<<<256, 768, LDS_BYTES, stream>>>(p);
}
```

```cpp
#include <hip/hip_runtime.h>
#include <hip/hip_bf16.h>

typedef unsigned short u16;
typedef unsigned long long u64;
typedef short bf16x8 __attribute__((ext_vector_type(8)));
typedef short s16x4 __attribute__((ext_vector_type(4)));
typedef __bf16 bf16x2 __attribute__((ext_vector_type(2)));
typedef float f32x2 __attribute__((ext_vector_type(2)));
typedef float f32x4 __attribute__((ext_vector_type(4)));
typedef unsigned u32x2 __attribute__((ext_vector_type(2)));
typedef unsigned u32x4 __attribute__((ext_vector_type(4)));
typedef __amdgpu_buffer_rsrc_t rsrc_t;
#define LAS __attribute__((address_space(3)))
#define GAS __attribute__((address_space(1)))
#define DEVI __device__ __forceinline__

constexpr int SEQ = 2048, DM = 640, FF = 1728, FH = 864;

constexpr unsigned WS_EPOCH = 0x0;
constexpr unsigned WS_TMO   = 0x100;
constexpr unsigned WS_FLAG0 = 0x1A00000;
constexpr unsigned WS_FLAGB = 0x1A08000;
constexpr unsigned WS_FLAG1 = 0x1A10000;
constexpr unsigned WS_FLAG2 = 0x1A18000;
constexpr unsigned WS_FLAG4 = 0x1A20000;
constexpr unsigned WS_FLAG3 = 0x1A28000;
constexpr unsigned WS_MODP  = 0x10000;
constexpr unsigned WS_UV    = 0x20000;
constexpr unsigned WS_ST1   = 0x40000;
constexpr unsigned WS_ST2   = 0x80000;
constexpr unsigned WS_XNT   = 0x100000;
constexpr unsigned WS_PQ    = 0x400000;
constexpr unsigned WS_X1C   = 0xA00000;
constexpr unsigned WS_Z2T   = 0xD00000;
constexpr unsigned WS_U     = 0x1100000;
constexpr unsigned WS_WINR  = 0x1500000;
constexpr unsigned WS_WOUTR = 0x1800000;
constexpr unsigned WS_STAMP = 0x2400000;
constexpr unsigned WS_USED  = 0x2600000;

constexpr int LDS_BYTES = 163840;
constexpr int L_MISC = 159744;
constexpr int L_CNT  = L_MISC;
constexpr int L_TAB  = L_MISC + 128;
constexpr int L_UV   = L_MISC + 1728;
constexpr int L_SINK = L_MISC + 3584;
constexpr int L_TABE = 153600;
constexpr int L_P0T  = 98304;
constexpr int L_RT   = 118784;
constexpr int P0T_RS = 1296;

constexpr unsigned SPIN_MAX = 1u << 17;

struct Params {
  const u16 *x, *wsre, *wsim, *wdre, *wdim, *win, *wsp, *bsp, *wout, *c, *wmod;
  u16* out;
  char* ws;
};

DEVI rsrc_t mkrs(const void* p, unsigned bytes) { return __builtin_amdgcn_make_buffer_rsrc((void*)p, 0, (int)bytes, 0x00020000); }
template <int AUX> DEVI u32x4 ld16(rsrc_t r, unsigned v, unsigned s) { return __builtin_amdgcn_raw_buffer_load_b128(r, (int)v, (int)s, AUX); }
template <int AUX> DEVI u32x2 ld8(rsrc_t r, unsigned v, unsigned s) { return __builtin_amdgcn_raw_buffer_load_b64(r, (int)v, (int)s, AUX); }
template <int AUX> DEVI unsigned ld4(rsrc_t r, unsigned v, unsigned s) { return __builtin_amdgcn_raw_buffer_load_b32(r, (int)v, (int)s, AUX); }
template <int AUX> DEVI unsigned ld2(rsrc_t r, unsigned v, unsigned s) { return (unsigned)__builtin_amdgcn_raw_buffer_load_b16(r, (int)v, (int)s, AUX) & 0xffffu; }
template <int AUX> DEVI void st16(u32x4 d, rsrc_t r, unsigned v, unsigned s) { __builtin_amdgcn_raw_buffer_store_b128(d, r, (int)v, (int)s, AUX); }
template <int AUX> DEVI void st8(u32x2 d, rsrc_t r, unsigned v, unsigned s) { __builtin_amdgcn_raw_buffer_store_b64(d, r, (int)v, (int)s, AUX); }
template <int AUX> DEVI void dma16(rsrc_t r, LAS char* dst, unsigned v, unsigned s) { __builtin_amdgcn_raw_ptr_buffer_load_lds(r, (LAS void*)dst, 16, (int)v, (int)s, 0, AUX); }
constexpr int SC1 = 16;
constexpr unsigned OOB = 0x7ffffff0u;

template <int N> DEVI void wait_vm() { asm volatile("s_waitcnt vmcnt(%0)" ::"n"(N > 63 ? 63 : N) : "memory"); }
DEVI void wait_lgkm0() { __builtin_amdgcn_s_waitcnt(0xC07F); }
DEVI void BAR() { asm volatile("s_barrier" ::: "memory"); }
DEVI void CFENCE() { asm volatile("" ::: "memory"); }
#define SCHED() __builtin_amdgcn_sched_barrier(0)

DEVI float bflo(unsigned v) { return __uint_as_float(v << 16); }
DEVI float bfhi(unsigned v) { return __uint_as_float(v & 0xffff0000u); }
DEVI unsigned pk2(float lo, float hi) { f32x2 f = {lo, hi}; return __builtin_bit_cast(unsigned, __builtin_convertvector(f, bf16x2)); }
DEVI float rbf(float v) { return bflo(pk2(v, 0.f)); }
DEVI f32x4 mfma(bf16x8 a, bf16x8 b, f32x4 c) { return __builtin_amdgcn_mfma_f32_16x16x32_bf16(a, b, c, 0, 0, 0); }
DEVI bf16x8 asfrag(u32x4 v) { return __builtin_bit_cast(bf16x8, v); }
DEVI bf16x8 ldsfrag(LAS char* p) { return *(LAS bf16x8*)p; }

template <int CTRL> DEVI float dpp_add(float v) { return v + __int_as_float(__builtin_amdgcn_update_dpp(0, __float_as_int(v), CTRL, 0xf, 0xf, false)); }
DEVI float row16_sum(float v) { v = dpp_add<0x128>(v); v = dpp_add<0x124>(v); v = dpp_add<0x122>(v); return dpp_add<0x121>(v); }
DEVI f32x2 both16(float v) { auto r = __builtin_amdgcn_permlane16_swap(__float_as_uint(v), __float_as_uint(v), false, false); return (f32x2){__uint_as_float(r[0]), __uint_as_float(r[1])}; }
DEVI f32x2 both32(float v) { auto r = __builtin_amdgcn_permlane32_swap(__float_as_uint(v), __float_as_uint(v), false, false); return (f32x2){__uint_as_float(r[0]), __uint_as_float(r[1])}; }
DEVI float xq_sum(float v) { f32x2 a = both16(v); v = a.x + a.y; a = both32(v); return a.x + a.y; }
DEVI float rsq(float v) { return __builtin_amdgcn_rsqf(v); }
DEVI float wave_sum(float v) { return xq_sum(row16_sum(v)); }

DEVI float gelu_tanh(float x) {
  const float u = x * (1.0f + 0.044715f * x * x) * (-2.0f * 0.7978845608028654f * 1.4426950408889634f);
  return x * __builtin_amdgcn_rcpf(1.0f + __builtin_amdgcn_exp2f(u));
}

DEVI u32x4 pair_swap(u32x2 a, u32x2 b) {
  auto s0 = __builtin_amdgcn_permlane16_swap(a.x, b.x, false, false);
  auto s1 = __builtin_amdgcn_permlane16_swap(a.y, b.y, false, false);
  return (u32x4){s0[0], s1[0], s0[1], s1[1]};
}

DEVI void note_timeout(char* ws, unsigned code, int lane) {
  if (lane == 0) __hip_atomic_store((GAS unsigned*)(ws + WS_TMO), code, __ATOMIC_RELAXED, __HIP_MEMORY_SCOPE_AGENT);
}
DEVI void poll_flags(char* ws, unsigned off, int n, u64 T, int lane) {
  for (unsigned spins = 0;; ++spins) {
    u64 f = T;
    if (lane < n) f = __hip_atomic_load((GAS u64*)(ws + off + lane * 128), __ATOMIC_RELAXED, __HIP_MEMORY_SCOPE_AGENT);
    if (__all(f == T)) break;
    if (spins > SPIN_MAX) { note_timeout(ws, 0x100u | ((off >> 15) & 0xff), lane); break; }
    __builtin_amdgcn_s_sleep(1);
  }
  CFENCE();
}
DEVI void poll_flags256(char* ws, unsigned off, u64 T, int lane) {
  for (unsigned spins = 0;; ++spins) {
    bool ok = true;
#pragma unroll
    for (int i = 0; i < 4; ++i) ok &= __hip_atomic_load((GAS u64*)(ws + off + (i * 8 + (lane >> 3)) * 128 + (lane & 7) * 8), __ATOMIC_RELAXED, __HIP_MEMORY_SCOPE_AGENT) == T;
    if (__all(ok)) break;
    if (spins > SPIN_MAX) { note_timeout(ws, 0x200u | ((off >> 15) & 0xff), lane); break; }
    __builtin_amdgcn_s_sleep(4);
  }
  CFENCE();
}
DEVI void set_flag(char* ws, unsigned off, u64 T, int lane) {
  if (lane == 0) __hip_atomic_store((GAS u64*)(ws + off), T, __ATOMIC_RELAXED, __HIP_MEMORY_SCOPE_AGENT);
}
DEVI void lds_signal(LAS char* lds, int idx, int lane) {
  CFENCE();
  if (lane == 0) __hip_atomic_store((LAS unsigned*)(lds + L_CNT) + idx, 1u, __ATOMIC_RELAXED, __HIP_MEMORY_SCOPE_WORKGROUP);
}
DEVI void lds_wait(LAS char* lds, int idx) {
  for (unsigned spins = 0; spins < (SPIN_MAX << 6); ++spins) {
    if (__hip_atomic_load((LAS unsigned*)(lds + L_CNT) + idx, __ATOMIC_RELAXED, __HIP_MEMORY_SCOPE_WORKGROUP) != 0u) break;
    __builtin_amdgcn_s_sleep(1);
  }
  CFENCE();
}
DEVI void lds_wait_n(LAS char* lds, int idx, unsigned n) {
  for (unsigned spins = 0; spins < (SPIN_MAX << 6); ++spins) {
    if (__hip_atomic_load((LAS unsigned*)(lds + L_CNT) + idx, __ATOMIC_RELAXED, __HIP_MEMORY_SCOPE_WORKGROUP) >= n) break;
    __builtin_amdgcn_s_sleep(1);
  }
  CFENCE();
}
DEVI void group_wait(LAS char* lds, int idx, char* ws, unsigned off, int n, u64 T, bool poller, int lane) {
  if (poller) { poll_flags(ws, off, n, T, lane); lds_signal(lds, idx, lane); } else lds_wait(lds, idx);
}
DEVI bool arrive_last(LAS char* lds, int idx, unsigned n, int lane) {
  unsigned old = 0;
  if (lane == 0) old = __hip_atomic_fetch_add((LAS unsigned*)(lds + L_CNT) + idx, 1u, __ATOMIC_RELAXED, __HIP_MEMORY_SCOPE_WORKGROUP);
  return (unsigned)__builtin_amdgcn_readfirstlane(old) == n - 1;
}
DEVI void publish(LAS char* lds, int cidx, unsigned nwaves, char* ws, unsigned flag_off, u64 T, int lane) {
  wait_vm<0>();
  if (arrive_last(lds, cidx, nwaves, lane)) set_flag(ws, flag_off, T, lane);
}

#define LANE_IDS() int tz_ = threadIdx.x; asm volatile("" : "+v"(tz_)); const int lane = tz_ & 63, l15 = lane & 15, q = lane >> 4; (void)l15; (void)q

#define LANE_IDS2() int tz2_ = threadIdx.x; asm volatile("" : "+v"(tz2_)); const int laneb = tz2_ & 63, l15b = laneb & 15, qb = laneb >> 4; (void)l15b; (void)qb

#define STAMP(k) do { } while (0)
#define STAMPC(k) do { } while (0)

template <int NT, int RING, int P0, int CU> constexpr int issued(int i) {
  int n = P0 < NT ? P0 : NT;
  for (int k = 0; k <= i; ++k) { int m = n + CU; if (m > k + RING) m = k + RING; if (m > NT) m = NT; n = m; }
  return n;
}
constexpr int cap63(int n, int pc) { return n > 63 - pc ? 63 - pc : n; }
template <int I, int IEND, int NT, int RING, int PC, int P0, int CU, int XI, class Issue> DEVI void loader_steps(Issue& issue) {
  if constexpr (I < IEND) {
    constexpr int before = issued<NT, RING, P0, CU>(I - 1), after = issued<NT, RING, P0, CU>(I);
    wait_vm<cap63((before - (I + 2)) * PC, (after - before) * PC)>(); BAR();
    if constexpr (after > before) issue(before, before % RING);
    if constexpr (after > before + 1) issue(before + 1, (before + 1) % RING);
    if constexpr (after > before + 2) issue(before + 2, (before + 2) % RING);
    static_assert(after <= before + 3, "at most three tiles per step");
    if constexpr (I == XI) BAR();
    loader_steps<I + 1, IEND, NT, RING, PC, P0, CU, XI>(issue);
  }
}
template <int NT, int RING, int P0, int CU> constexpr int steady_from() {
  for (int i = 0; i < NT; ++i) if (issued<NT, RING, P0, CU>(i - 1) == i - 1 + RING) return i;
  return NT;
}
template <int T0, int T1, int RING, class Issue> DEVI void loader_fill(Issue& issue) {
#pragma unroll 1
  for (int t = T0; t < T1; ++t) issue(t, t);
}
struct NoHook { DEVI void operator()(int) const {} };
template <int NT, int RING, int PC, int P0, int CU, int XI, class Issue, class Hook = NoHook> DEVI void loader_run(Issue& issue, Hook hook = Hook()) {
  constexpr int P = P0 < NT ? P0 : NT;
  wait_vm<cap63((P - 1) * PC, 0)>(); BAR();
  constexpr int S0 = steady_from<NT, RING, P0, CU>(), S1 = NT - RING + 1;
  if constexpr (XI < 0 && S0 + 2 < S1) {
    loader_steps<0, S0, NT, RING, PC, P0, CU, XI>(issue);
    int slot = (S0 - 1 + RING) % RING;
#pragma unroll 1
    for (int i = S0; i < S1; ++i) {
      wait_vm<cap63((RING - 3) * PC, PC)>(); BAR();
      issue(i - 1 + RING, slot);
      hook(i);
      slot = (slot + 1 == RING) ? 0 : slot + 1;
    }
    loader_steps<S1, NT - 1, NT, RING, PC, P0, CU, XI>(issue);
  } else {
    loader_steps<0, NT - 1, NT, RING, PC, P0, CU, XI>(issue);
  }
}
template <int NBAR> DEVI void idle_phase() {
#pragma unroll 1
  for (int i = 0; i < NBAR; ++i) BAR();
}

template <int I, int NMF, int NRD, int PER> struct Interleave {
  static DEVI void run() {
    if constexpr (I < NMF) {
      __builtin_amdgcn_sched_group_barrier(0x8, 1, 0);
      constexpr int left = NRD - I * PER, now = left < PER ? left : PER;
      if constexpr (now > 0) __builtin_amdgcn_sched_group_barrier(0x100, now, 0);
      Interleave<I + 1, NMF, NRD, PER>::run();
    }
  }
};
template <int NMF, int NRD, int PER> DEVI void interleave() { Interleave<0, NMF, NRD, PER>::run(); }

#define AW 4
#define PF2 6
#define PF3 4
#define PF4 8
#define PF5 6
constexpr int P1_SA = 6, P1_DB = 5, P1_DOFF = 98304, P1_X = 32768;
constexpr int P2_RING = 15, P2_X = 51200;
constexpr int P3_RING = 8, P3_X = 114688;
constexpr int P4_SA = 9, P4_DB = 5, P4_DOFF = 73728, P4_X = 40960;
constexpr int L_X1 = 145408;
constexpr int P5_RING = 14, P5_X = 0;

DEVI f32x4 sel4(int c, f32x4 a, f32x4 b) { asm volatile("" : "+v"(a), "+v"(b)); return c ? a : b; }
DEVI float sel1(int c, float a, float b) { asm volatile("" : "+v"(a), "+v"(b)); return c ? a : b; }
DEVI int rotk(int t, int r, int n) { const int v = t + r; return v >= n ? v - n : v; }
DEVI void lds_put(LAS char* p, f32x4 v) { *(LAS f32x4*)p = v; }
DEVI f32x4 lds_get(LAS char* p) { return *(LAS f32x4*)p; }
DEVI u64 epoch_tag(u64 e) {
  return (((u64)(unsigned)__builtin_amdgcn_readfirstlane((unsigned)(e >> 32)) << 32) | (unsigned)__builtin_amdgcn_readfirstlane((unsigned)e)) + 1;
}
DEVI void chan(float& n, float& m, float& M2, float no, float mo, float Mo) {
  const float nt = n + no, d = mo - m, r = __builtin_amdgcn_rcpf(nt);
  M2 = M2 + Mo + d * d * (n * no * r);
  m = m + d * (no * r);
  n = nt;
}

__global__ __launch_bounds__(768, 3) void adaLN_cond__spectral_fnet__mixer_gmlp__prompt_kernel(Params p) {
  extern __shared__ __attribute__((aligned(16))) char smem_[];
  LAS char* lds = (LAS char*)smem_;
  const int tid = threadIdx.x, wid = __builtin_amdgcn_readfirstlane(tid >> 6);
  const int b = blockIdx.x, xg = b & 7, i8 = b >> 3;
  const int g = xg * 4 + (i8 >> 3), j = i8 & 7;
  char* const ws = p.ws;
  const int rot = xg * 4;
  const int r2 = 0, r3 = 0, r5 = 0;

  { LANE_IDS(); STAMP(0); STAMPC(30); }
  const u64 Ein = __hip_atomic_load((GAS u64*)(ws + WS_EPOCH), __ATOMIC_RELAXED, __HIP_MEMORY_SCOPE_AGENT);
  if (tid < 32) *((LAS unsigned*)(lds + L_CNT) + tid) = 0u;

  if (wid < 8) {
    const int w = wid, m = w & 3, kh = w >> 2;
    __builtin_amdgcn_s_setprio(2);
    {
      LANE_IDS();
      const rsrc_t rx = mkrs(p.x, SEQ * DM * 2);
      u32x4 v[2];
#pragma unroll
      for (int h = 0; h < 2; ++h) v[h] = ld16<0>(rx, lane < 40 ? (unsigned)((lane + 40 * h) * 16) : OOB, (unsigned)(64 * g + 8 * j + w) * (DM * 2));
      wait_lgkm0();
      BAR();
      float f[16];
#pragma unroll
      for (int h = 0; h < 2; ++h)
#pragma unroll
        for (int i = 0; i < 4; ++i) { f[h * 8 + 2 * i] = bflo(v[h][i]); f[h * 8 + 2 * i + 1] = bfhi(v[h][i]); }
      float s = 0.f;
#pragma unroll
      for (int i = 0; i < 16; ++i) s += f[i];
      const float mean = wave_sum(s) * (1.0f / DM);
      float m2 = 0.f;
#pragma unroll
      for (int i = 0; i < 16; ++i) { f[i] -= mean; m2 += f[i] * f[i]; }
      if (lane >= 40) m2 = 0.f;
      const float rstd = rsq(wave_sum(m2) * (1.0f / DM) + 1e-5f);
      if (lane < 40) {
#pragma unroll
        for (int h = 0; h < 2; ++h) {
          u32x4 o;
#pragma unroll
          for (int i = 0; i < 4; ++i) o[i] = pk2(f[h * 8 + 2 * i] * rstd, f[h * 8 + 2 * i + 1] * rstd);
          *(LAS u32x4*)(lds + L_P0T + w * P0T_RS + (lane + 40 * h) * 16) = o;
        }
      }
      wait_lgkm0(); CFENCE();
      if (lane == 0) __hip_atomic_fetch_add((LAS unsigned*)(lds + L_CNT) + 0, 1u, __ATOMIC_RELAXED, __HIP_MEMORY_SCOPE_WORKGROUP);
      for (unsigned spins = 0; spins < SPIN_MAX; ++spins) {
        if (__hip_atomic_load((LAS unsigned*)(lds + L_CNT) + 0, __ATOMIC_RELAXED, __HIP_MEMORY_SCOPE_WORKGROUP) >= 8u) break;
      }
      CFENCE();
      const rsrc_t rxn = mkrs(ws + WS_XNT, 8 * 32 * 10240);
#pragma unroll
      for (int it = 0; it < 2; ++it) {
        const int pc = it == 0 ? q : 4, nf = 5 * w + pc;
        const int qq = l15 >> 2, pp = l15 & 3;
        LAS char* a0 = lds + L_P0T + qq * P0T_RS + (16 * nf + 4 * pp) * 2;
        const u32x2 lo = __builtin_bit_cast(u32x2, __builtin_amdgcn_ds_read_tr16_b64_v4i16((LAS s16x4*)a0));
        const u32x2 hi = __builtin_bit_cast(u32x2, __builtin_amdgcn_ds_read_tr16_b64_v4i16((LAS s16x4*)(a0 + 4 * P0T_RS)));
        const unsigned off = (unsigned)(((w * 32 + g) * 10 + (j >> 2) * 5 + pc) * 1024 + ((j & 3) * 16 + l15) * 16);
        st16<SC1>((u32x4){lo.x, lo.y, hi.x, hi.y}, rxn, (it == 0 || q == 0) ? off : OOB, 0);
      }
    }
    { LANE_IDS(); if (lane == 0) __hip_atomic_fetch_add((LAS unsigned*)(lds + L_CNT) + 13, 1u, __ATOMIC_RELAXED, __HIP_MEMORY_SCOPE_WORKGROUP); }
    const u64 T = epoch_tag(Ein);
    { LANE_IDS(); wait_vm<0>(); if (arrive_last(lds, 1, 8, lane)) {
      if (lane < 8) __hip_atomic_store((GAS u64*)(ws + WS_FLAG0 + lane * 4096 + g * 128 + j * 8), T, __ATOMIC_RELAXED, __HIP_MEMORY_SCOPE_AGENT);
      lds_signal(lds, 14, lane); } STAMP(1); }

    unsigned z1p[8];

    {
      LANE_IDS();
      f32x4 acc[2][5];
#pragma unroll
      for (int a = 0; a < 2; ++a)
#pragma unroll
        for (int n = 0; n < 5; ++n) acc[a][n] = (f32x4){0.f, 0.f, 0.f, 0.f};
      f32x4 rs[2] = {{0.f, 0.f, 0.f, 0.f}, {0.f, 0.f, 0.f, 0.f}};
      u32x4 ones4 = {0x3f803f80u, 0x3f803f80u, 0x3f803f80u, 0x3f803f80u}; asm volatile("" : "+v"(ones4));
      const bf16x8 ones = asfrag(ones4);
      const unsigned aoff = (m * 16 + l15) * 128 + (((kh * 4 + q) ^ (l15 >> 1)) << 4);
      const unsigned boff = P1_DOFF + kh * 5120 + lane * 16;
      bf16x8 XA[2], XB[5], YA[2], YB[5];
      int sa = 0, sb = 0;
      auto rd = [&](bf16x8(&A)[2], bf16x8(&B)[5]) {
        LAS char* pa = lds + sa * 16384 + aoff;
        LAS char* pb = lds + sb * 10240 + boff;
        A[0] = ldsfrag(pa); A[1] = ldsfrag(pa + 8192);
#pragma unroll
        for (int n = 0; n < 5; ++n) B[n] = ldsfrag(pb + n * 1024);
        sa = (sa + 1 == P1_SA) ? 0 : sa + 1; sb = (sb + 1 == P1_DB) ? 0 : sb + 1;
      };
      auto mm = [&](const bf16x8(&A)[2], const bf16x8(&B)[5]) {
#pragma unroll
        for (int a = 0; a < 2; ++a) {
#pragma unroll
          for (int n = 0; n < 5; ++n) acc[a][n] = mfma(B[n], A[a], acc[a][n]);
          rs[a] = mfma(ones, A[a], rs[a]);
        }
      };
      auto pairs = [&](int n) {
#pragma unroll 1
        for (int i = 0; i < n; ++i) {
          BAR(); rd(YA, YB); mm(XA, XB); interleave<12, 7, 1>(); SCHED();
          BAR(); rd(XA, XB); mm(YA, YB); interleave<12, 7, 1>(); SCHED();
        }
      };
      BAR();
      STAMP(2); STAMPC(28);
      rd(XA, XB);
      pairs(6);
      u64 fb[4];
      if (w == 0) {
#pragma unroll
        for (int i = 0; i < 4; ++i) fb[i] = __hip_atomic_load((GAS u64*)(ws + WS_FLAGB + (i * 8 + (lane >> 3)) * 128 + (lane & 7) * 8), __ATOMIC_RELAXED, __HIP_MEMORY_SCOPE_AGENT);
      }
      pairs(3);
      f32x4 mp[2][4];
      const int vs0 = lane < 20 ? 1 : lane < 40 ? 0 : 2, vs1 = lane < 20 ? 4 : 5;
      const int vl = lane < 20 ? lane : lane < 40 ? lane - 20 : lane - 40;
      if (w == 0) {
        if (!__all(fb[0] == T && fb[1] == T && fb[2] == T && fb[3] == T)) poll_flags256(ws, WS_FLAGB, T, lane);
        CFENCE();
        const rsrc_t rm = mkrs(ws + WS_MODP, 4 * 3840 * 4);
#pragma unroll
        for (int k = 0; k < 4; ++k) {
          mp[0][k] = __builtin_bit_cast(f32x4, ld16<SC1>(rm, lane < 60 ? (unsigned)((vs0 * DM + 80 * j + vl * 4) * 4) : OOB, k * 3840 * 4));
          mp[1][k] = __builtin_bit_cast(f32x4, ld16<SC1>(rm, lane < 40 ? (unsigned)((vs1 * DM + 80 * j + vl * 4) * 4) : OOB, k * 3840 * 4));
        }
      }
      pairs(3);
      if (w == 0) {
        f32x4 s0 = (mp[0][0] + mp[0][1]) + (mp[0][2] + mp[0][3]), s1 = (mp[1][0] + mp[1][1]) + (mp[1][2] + mp[1][3]);
#pragma unroll
        for (int i = 0; i < 4; ++i) {
          s0[i] = rbf(s0[i]); if (lane < 20) s0[i] = rbf(1.0f + s0[i]);
          s1[i] = rbf(s1[i]); if (lane < 20) s1[i] = rbf(1.0f + s1[i]);
        }
        if (lane < 60) lds_put(lds + L_TAB + ((lane < 20 ? 0 : lane < 40 ? 1 : 2) * 80 + vl * 4) * 4, s0);
        if (lane < 40) lds_put(lds + L_TAB + ((lane < 20 ? 3 : 4) * 80 + vl * 4) * 4, s1);
      }
      pairs(3);
      BAR(); rd(YA, YB); mm(XA, XB); interleave<12, 7, 1>(); SCHED();
      mm(YA, YB);
      STAMP(3); STAMPC(29);
      {
        LAS char* xo = lds + P1_X + w * 6144 + lane * 16;
        LAS char* xi = lds + P1_X + (w ^ 4) * 6144 + lane * 16;
#pragma unroll
        for (int n = 0; n < 5; ++n) lds_put(xo + n * 1024, sel4(kh, acc[0][n], acc[1][n]));
        *(LAS float*)(lds + P1_X + w * 6144 + 5120 + lane * 4) = sel1(kh, rs[0][0], rs[1][0]);
        wait_lgkm0();
        BAR();
        const float rsum = sel1(kh, rs[1][0], rs[0][0]) + *(LAS float*)(lds + P1_X + (w ^ 4) * 6144 + 5120 + lane * 4);
        u32x2 pkd[5];
#pragma unroll
        for (int n = 0; n < 5; ++n) {
          const f32x4 a = sel4(kh, acc[1][n], acc[0][n]) + lds_get(xi + n * 1024);
          const f32x4 c1 = lds_get(lds + L_TAB + (0 * 80 + 16 * n + 4 * q) * 4), sa4 = lds_get(lds + L_TAB + (1 * 80 + 16 * n + 4 * q) * 4);
          f32x4 v = a * c1 + rsum * sa4;
          if (kh) v = -v;
          pkd[n] = (u32x2){pk2(v[0], v[1]), pk2(v[2], v[3])};
        }
        const rsrc_t rpq = mkrs(ws + WS_PQ, 32 * 40 * 4 * 1024);
        auto pq_off = [&](int nf) { const int c = 10 * j + 2 * nf + (q >> 1); return (unsigned)(((g * 40 + kh * 20 + (c >> 2)) * 4 + m) * 1024 + ((c & 3) * 16 + l15) * 16); };
        const int odd = q & 1;
        st16<SC1>(pair_swap(pkd[0], pkd[1]), rpq, pq_off(odd), 0);
        st16<SC1>(pair_swap(pkd[2], pkd[3]), rpq, pq_off(2 + odd), 0);
        st8<SC1>(pkd[4], rpq, pq_off(4) + odd * 8, 0);
      }
      BAR();
      publish(lds, 3, 8, ws, WS_FLAG1 + (g * 8 + j) * 128, T, lane);
      STAMP(4);
    }

    {
      LANE_IDS();
      constexpr int NT = 20;
      const int nb = kh ? 3 : 0;
      group_wait(lds, 8, ws, WS_FLAG1 + g * 1024, 8, T, w == 0, lane);
      STAMP(5);
      const rsrc_t ra = mkrs(ws + WS_PQ + (size_t)g * 40 * 4 * 1024, 40 * 4 * 1024);
      u32x4 A[NT];
#pragma unroll
      for (int t = 0; t < AW; ++t) A[t] = ld16<SC1>(ra, lane * 16, ((2 * rotk(t, r2, NT) + kh) * 4 + m) * 1024);
      f32x4 acc[5];
#pragma unroll
      for (int n = 0; n < 5; ++n) acc[n] = (f32x4){0.f, 0.f, 0.f, 0.f};
      const unsigned boff = l15 * 128 + (((kh * 4 + q) ^ (l15 >> 1)) << 4);
      bf16x8 B[2][5];
      auto rd = [&](bf16x8(&Bv)[5], int slot) {
#pragma unroll
        for (int n = 0; n < 5; ++n) Bv[n] = ldsfrag(lds + slot * 10240 + boff + n * 2048);
      };
      BAR();
      STAMP(6);
      rd(B[0], 0);
#pragma unroll
      for (int t = 0; t < NT; ++t) {
        if (t + 1 < NT) { BAR(); rd(B[(t + 1) & 1], (t + 1) % P2_RING); }
        if (t + AW < NT) A[t + AW] = ld16<SC1>(ra, lane * 16, ((2 * rotk(t + AW, r2, NT) + kh) * 4 + m) * 1024);
#pragma unroll
        for (int n = 0; n < 5; ++n) acc[n] = mfma(B[t & 1][n], asfrag(A[t]), acc[n]);
        interleave<5, 5, 1>(); SCHED();
      }
      STAMP(7);
      LANE_IDS2();
      const rsrc_t rx = mkrs(p.x, SEQ * DM * 2);
      u32x2 xin[3];
#pragma unroll
      for (int n = 0; n < 3; ++n) xin[n] = ld8<0>(rx, (kh && n == 2) ? OOB : (unsigned)((64 * g + 16 * m + l15b) * (DM * 2) + (80 * j + 16 * (nb + n) + 4 * qb) * 2), 0);
      LAS char* xo = lds + P2_X + w * 3072 + lane * 16;
      LAS char* xi = lds + P2_X + (w ^ 4) * 3072 + lane * 16;
      if (kh) { lds_put(xo, acc[0]); lds_put(xo + 1024, acc[1]); lds_put(xo + 2048, acc[2]); }
      else { lds_put(xo, acc[3]); lds_put(xo + 1024, acc[4]); }
      wait_lgkm0();
      BAR();
      float x1f[3][4]; float s = 0.f;
      u32x2 cp[3]; unsigned x1p[6];
#pragma unroll
      for (int n = 0; n < 3; ++n) {
        const f32x4 y = sel4(kh, acc[n == 2 ? 4 : 3 + n], acc[n]) + lds_get(xi + n * 1024);
        const int col = (kh && n == 2) ? 0 : 16 * (nb + n) + 4 * q;
        const f32x4 ga = lds_get(lds + L_TAB + (2 * 80 + col) * 4), cb1 = lds_get(lds + L_TAB + (3 * 80 + col) * 4);
        x1p[2 * n] = pk2(bflo(xin[n].x) + ga[0] * y[0], bfhi(xin[n].x) + ga[1] * y[1]);
        x1p[2 * n + 1] = pk2(bflo(xin[n].y) + ga[2] * y[2], bfhi(xin[n].y) + ga[3] * y[3]);
        x1f[n][0] = bflo(x1p[2 * n]); x1f[n][1] = bfhi(x1p[2 * n]); x1f[n][2] = bflo(x1p[2 * n + 1]); x1f[n][3] = bfhi(x1p[2 * n + 1]);
        cp[n] = (u32x2){pk2(x1f[n][0] * cb1[0], x1f[n][1] * cb1[1]), pk2(x1f[n][2] * cb1[2], x1f[n][3] * cb1[3])};
        if (!(kh && n == 2)) s += (x1f[n][0] + x1f[n][1]) + (x1f[n][2] + x1f[n][3]);
      }
      const float mean = xq_sum(s) * (kh ? 1.0f / 32.0f : 1.0f / 48.0f);
      float m2 = 0.f;
#pragma unroll
      for (int n = 0; n < 3; ++n)
        if (!(kh && n == 2)) {
#pragma unroll
          for (int r = 0; r < 4; ++r) { const float d = x1f[n][r] - mean; m2 += d * d; }
        }
      m2 = xq_sum(m2);
      const rsrc_t rc = mkrs(ws + WS_X1C, 32 * 20 * 4 * 1024);
      auto c_off = [&](int nf) { const int c = 10 * j + 2 * nf + (q >> 1); return (unsigned)(((g * 20 + (c >> 2)) * 4 + m) * 1024 + ((c & 3) * 16 + l15) * 16); };
      const int odd = q & 1;
      st16<SC1>(pair_swap(cp[0], cp[1]), rc, c_off(nb + odd), 0);
      st8<SC1>(cp[2], rc, kh ? OOB : c_off(2) + odd * 8, 0);
      const rsrc_t rs1 = mkrs(ws + WS_ST1, SEQ * 16 * 8);
      st8<SC1>((u32x2){__float_as_uint(mean), __float_as_uint(m2)}, rs1, q == 0 ? (unsigned)(((64 * g + 16 * m + l15) * 16 + 2 * j + kh) * 8) : OOB, 0);
#pragma unroll
      for (int n = 0; n < 3; ++n) *(LAS u32x2*)(lds + L_X1 + (w * 3 + n) * 512 + lane * 8) = (u32x2){x1p[2 * n], x1p[2 * n + 1]};
      wait_lgkm0();
      BAR();
      publish(lds, 4, 8, ws, WS_FLAG2 + (g * 8 + j) * 128, T, lane);
      STAMP(8);
    }

    {
      LANE_IDS();
      constexpr int NT = 10;
      const int nb = kh ? 4 : 0, nn = kh ? 3 : 4;
      {
        const rsrc_t ruv = mkrs(ws + WS_UV, 2 * 896 * 16);
        const int col = w * 28 + lane, part = col >= 112 ? 1 : 0, cc = col - 112 * part;
        const unsigned off = lane < 28 ? (unsigned)((part * 896 + 112 * j + cc) * 16) : OOB;
        u32x4 gv;
        for (unsigned spins = 0;; ++spins) {
          gv = ld16<SC1>(ruv, off, 0);
          const bool ok = lane >= 28 || (gv.z == (unsigned)T && gv.w == (unsigned)(T >> 32));
          if (__all(ok)) break;
          if (spins > SPIN_MAX) { note_timeout(ws, 0x300, lane); break; }
          __builtin_amdgcn_s_sleep(1); CFENCE();
        }
        if (lane < 28) { *(LAS unsigned*)(lds + L_UV + col * 4) = gv.x; *(LAS unsigned*)(lds + L_UV + (224 + col) * 4) = gv.y; }
      }
      group_wait(lds, 9, ws, WS_FLAG2 + g * 1024, 8, T, w == 0, lane);
      STAMP(9);
      const rsrc_t ra = mkrs(ws + WS_X1C + (size_t)g * 20 * 4 * 1024, 20 * 4 * 1024);
      u32x4 A[NT];
#pragma unroll
      for (int t = 0; t < AW; ++t) A[t] = ld16<SC1>(ra, lane * 16, ((2 * rotk(t, r3, NT) + kh) * 4 + m) * 1024);
      const rsrc_t rs1 = mkrs(ws + WS_ST1, SEQ * 16 * 8);
      const unsigned s1o = (unsigned)(((64 * g + 16 * m + l15) * 16 + 4 * q) * 8);
      const u32x4 st0 = ld16<SC1>(rs1, s1o, 0), st1 = ld16<SC1>(rs1, s1o + 16, 0);
      f32x4 acc[7];
      bf16x8 B[2][7];
      auto rd = [&](bf16x8(&Bv)[7], int slot) {
#pragma unroll
        for (int n = 0; n < 7; ++n) Bv[n] = ldsfrag(lds + slot * 14336 + kh * 7168 + n * 1024 + lane * 16);
      };
      wait_lgkm0();
      BAR();
      STAMP(10);
      rd(B[0], 0); wait_lgkm0();
      float mean1, rstd1;
      {
        float n = 48.f, mm = __uint_as_float(st0.x), M2 = __uint_as_float(st0.y);
        chan(n, mm, M2, 32.f, __uint_as_float(st0.z), __uint_as_float(st0.w));
        chan(n, mm, M2, 48.f, __uint_as_float(st1.x), __uint_as_float(st1.y));
        chan(n, mm, M2, 32.f, __uint_as_float(st1.z), __uint_as_float(st1.w));
        { const f32x2 a = both16(mm), c = both16(M2); mm = a.x; M2 = c.x; chan(n, mm, M2, n, a.y, c.y); }
        { const f32x2 a = both32(mm), c = both32(M2); mm = a.x; M2 = c.x; chan(n, mm, M2, n, a.y, c.y); }
        mean1 = mm; rstd1 = rsq(M2 * (1.0f / DM) + 1e-5f);
      }
      LAS char* xo = lds + P3_X + (kh ? 12288 + m * 4096 : m * 3072) + lane * 16;
      LAS char* xi = lds + P3_X + (kh ? m * 3072 : 12288 + m * 4096) + lane * 16;
#pragma unroll
      for (int n = 0; n < 7; ++n) acc[n] = (f32x4){0.f, 0.f, 0.f, 0.f};
#pragma unroll
      for (int t = 0; t < NT; ++t) {
        BAR(); if (t + 1 < NT) rd(B[(t + 1) & 1], (t + 1) % P3_RING);
        if (t + AW < NT) A[t + AW] = ld16<SC1>(ra, lane * 16, ((2 * rotk(t + AW, r3, NT) + kh) * 4 + m) * 1024);
#pragma unroll
        for (int n = 0; n < 7; ++n) acc[n] = mfma(asfrag(A[t]), B[t & 1][n], acc[n]);
        interleave<7, 7, 1>(); SCHED();
      }
      STAMP(25);
      if (kh) {
#pragma unroll
        for (int n = 0; n < 4; ++n) lds_put(xo + n * 1024, acc[n]);
      } else {
#pragma unroll
        for (int n = 0; n < 3; ++n) lds_put(xo + n * 1024, acc[4 + n]);
      }
      wait_lgkm0();
      BAR();
      u32x2 z2p[4];
      const int nval = (j == 7 && kh) ? 1 : nn;
      {
        float mr[4], rr[4];
#pragma unroll
        for (int r = 0; r < 4; ++r) { mr[r] = __shfl(mean1, 4 * q + r); rr[r] = __shfl(rstd1, 4 * q + r); }
        float zs[4] = {0.f, 0.f, 0.f, 0.f};
#pragma unroll
        for (int n = 0; n < 4; ++n) {
          const int nc = (kh && n == 3) ? 0 : n;
          const f32x4 a = sel4(kh, acc[n == 3 ? 6 : 4 + n], acc[n]) + lds_get(xi + nc * 1024);
          const int col = 16 * (nb + nc) + l15;
          const float u = *(LAS float*)(lds + L_UV + col * 4), v = *(LAS float*)(lds + L_UV + (224 + col) * 4);
          float z[4];
#pragma unroll
          for (int r = 0; r < 4; ++r) z[r] = gelu_tanh(rr[r] * (a[r] - mr[r] * u) + v);
          z2p[n] = (u32x2){pk2(z[0], z[1]), pk2(z[2], z[3])};
          if (n < nval) { zs[0] += bflo(z2p[n].x); zs[1] += bfhi(z2p[n].x); zs[2] += bflo(z2p[n].y); zs[3] += bfhi(z2p[n].y); }
        }
        const float inv = 1.0f / (16.0f * nval);
        float mz[4], M2[4];
#pragma unroll
        for (int r = 0; r < 4; ++r) { mz[r] = row16_sum(zs[r]) * inv; M2[r] = 0.f; }
#pragma unroll
        for (int n = 0; n < 4; ++n) if (n < nval) {
          const float d0 = bflo(z2p[n].x) - mz[0], d1 = bfhi(z2p[n].x) - mz[1], d2 = bflo(z2p[n].y) - mz[2], d3 = bfhi(z2p[n].y) - mz[3];
          M2[0] += d0 * d0; M2[1] += d1 * d1; M2[2] += d2 * d2; M2[3] += d3 * d3;
        }
#pragma unroll
        for (int r = 0; r < 4; ++r) M2[r] = row16_sum(M2[r]);
        const float ms = l15 == 0 ? mz[0] : l15 == 1 ? mz[1] : l15 == 2 ? mz[2] : mz[3];
        const float Ms = l15 == 0 ? M2[0] : l15 == 1 ? M2[1] : l15 == 2 ? M2[2] : M2[3];
        const rsrc_t rs2 = mkrs(ws + WS_ST2, SEQ * 16 * 16);
        st16<SC1>((u32x4){__float_as_uint(ms), __float_as_uint(Ms), (unsigned)T, (unsigned)(T >> 32)}, rs2,
                  l15 < 4 ? (unsigned)(((64 * g + 16 * m + 4 * q + l15) * 16 + 2 * j + kh) * 16) : OOB, 0);
      }
      STAMP(11);
      rd(B[0], NT % P3_RING); wait_lgkm0();
#pragma unroll
      for (int n = 0; n < 7; ++n) acc[n] = (f32x4){0.f, 0.f, 0.f, 0.f};
#pragma unroll
      for (int t = 0; t < NT; ++t) {
        if (t + 1 < NT) { BAR(); rd(B[(t + 1) & 1], (NT + t + 1) % P3_RING); }
#pragma unroll
        for (int n = 0; n < 7; ++n) acc[n] = mfma(B[t & 1][n], asfrag(A[t]), acc[n]);
        interleave<7, 7, 1>(); SCHED();
      }
      STAMP(12);
      const rsrc_t rs2 = mkrs(ws + WS_ST2, SEQ * 16 * 16);
      const unsigned soff = (unsigned)(((64 * g + 16 * m + l15) * 16 + 4 * q) * 16);
      u32x4 gs[4];
#pragma unroll
      for (int i = 0; i < 4; ++i) gs[i] = ld16<SC1>(rs2, soff + 16 * i, 0);
      if (kh) {
#pragma unroll
        for (int n = 0; n < 4; ++n) lds_put(xo + n * 1024, acc[n]);
      } else {
#pragma unroll
        for (int n = 0; n < 3; ++n) lds_put(xo + n * 1024, acc[4 + n]);
      }
      wait_lgkm0();
      BAR();
#pragma unroll
      for (int n = 0; n < 4; ++n) {
        const int nc = (kh && n == 3) ? 0 : n;
        const f32x4 a = sel4(kh, acc[n == 3 ? 6 : 4 + n], acc[n]) + lds_get(xi + nc * 1024);
        const int col = 112 + 16 * (nb + nc) + 4 * q;
        const f32x4 u = lds_get(lds + L_UV + col * 4), v = lds_get(lds + L_UV + (224 + col) * 4);
        float z[4];
#pragma unroll
        for (int r = 0; r < 4; ++r) z[r] = gelu_tanh(rstd1 * (a[r] - mean1 * u[r]) + v[r]);
        z1p[2 * n] = pk2(z[0], z[1]); z1p[2 * n + 1] = pk2(z[2], z[3]);
      }
#pragma unroll
      for (int i = 0; i < 8; ++i) asm volatile("" : "+v"(z1p[i]));
      STAMP(26);
      for (unsigned spins = 0;; ++spins) {
        bool ok = true;
#pragma unroll
        for (int i = 0; i < 4; ++i) ok &= gs[i].z == (unsigned)T && gs[i].w == (unsigned)(T >> 32);
        if (__all(ok)) break;
        if (spins > SPIN_MAX) { note_timeout(ws, 0x400, lane); break; }
        __builtin_amdgcn_s_sleep(1); CFENCE();
#pragma unroll
        for (int i = 0; i < 4; ++i) gs[i] = ld16<SC1>(rs2, soff + 16 * i, 0);
      }
      float mean2, rstd2;
      {
        float n = 64.f, mm = __uint_as_float(gs[0].x), M2 = __uint_as_float(gs[0].y);
        chan(n, mm, M2, 48.f, __uint_as_float(gs[1].x), __uint_as_float(gs[1].y));
        chan(n, mm, M2, 64.f, __uint_as_float(gs[2].x), __uint_as_float(gs[2].y));
        chan(n, mm, M2, q == 3 ? 16.f : 48.f, __uint_as_float(gs[3].x), __uint_as_float(gs[3].y));
        { const f32x2 k = both16(n), a = both16(mm), c = both16(M2); n = k.x; mm = a.x; M2 = c.x; chan(n, mm, M2, k.y, a.y, c.y); }
        { const f32x2 k = both32(n), a = both32(mm), c = both32(M2); n = k.x; mm = a.x; M2 = c.x; chan(n, mm, M2, k.y, a.y, c.y); }
        mean2 = mm; rstd2 = rsq(M2 * (1.0f / FH) + 1e-5f);
      }
      {
        float mr[4], rr[4];
#pragma unroll
        for (int r = 0; r < 4; ++r) { mr[r] = __shfl(mean2, 4 * q + r); rr[r] = __shfl(rstd2, 4 * q + r); }
        u32x2 zn[4];
#pragma unroll
        for (int n = 0; n < 4; ++n)
          zn[n] = (u32x2){pk2((bflo(z2p[n].x) - mr[0]) * rr[0], (bfhi(z2p[n].x) - mr[1]) * rr[1]), pk2((bflo(z2p[n].y) - mr[2]) * rr[2], (bfhi(z2p[n].y) - mr[3]) * rr[3])};
        const rsrc_t rz2 = mkrs(ws + WS_Z2T, 8 * 32 * 14336);
        auto z_off = [&](int nf) { return (unsigned)(((j * 32 + g) * 14 + (m >> 1) * 7 + nf) * 1024 + ((2 * (m & 1) + (q >> 1)) * 16 + l15) * 16); };
        const int odd = q & 1;
        st16<SC1>(pair_swap(zn[0], zn[1]), rz2, z_off(nb + odd), 0);
        if (kh) st8<SC1>(zn[2], rz2, z_off(6) + odd * 8, 0);
        else st16<SC1>(pair_swap(zn[2], zn[3]), rz2, z_off(2 + odd), 0);
      }
      STAMP(27);
      BAR();
      publish(lds, 5, 8, ws, WS_FLAG3 + (j * 32 + g) * 128, T, lane);
      STAMP(13);
    }

    {
      LANE_IDS();
      const int mh = m >> 1, k4 = m & 1, nh = kh;
      f32x4 acc[2][4];
#pragma unroll
      for (int a = 0; a < 2; ++a)
#pragma unroll
        for (int n = 0; n < 4; ++n) acc[a][n] = (f32x4){0.f, 0.f, 0.f, 0.f};
      const unsigned aoff = (mh * 32 + l15) * 128 + (((k4 * 4 + q) ^ (l15 >> 1)) << 4);
      const unsigned boff = P4_DOFF + k4 * 7168 + nh * 4096 + lane * 16;
      const rsrc_t rb = mkrs(p.bsp, SEQ * 2);
      const float bs = bflo(ld2<0>(rb, (64 * g + 16 * m + l15) * 2, 0));
      bf16x8 XA[2], XB[4], YA[2], YB[4];
      int sa = 0, sb = 0;
      if (nh == 0) {
        auto rd = [&](bf16x8(&A)[2], bf16x8(&B)[4]) {
          LAS char* pa = lds + sa * 8192 + aoff;
          LAS char* pb = lds + sb * 14336 + boff;
          A[0] = ldsfrag(pa); A[1] = ldsfrag(pa + 2048);
#pragma unroll
          for (int n = 0; n < 4; ++n) B[n] = ldsfrag(pb + n * 1024);
          sa = (sa + 1 == P4_SA) ? 0 : sa + 1; sb = (sb + 1 == P4_DB) ? 0 : sb + 1;
        };
        auto mm = [&](const bf16x8(&A)[2], const bf16x8(&B)[4]) {
#pragma unroll
          for (int a = 0; a < 2; ++a)
#pragma unroll
            for (int n = 0; n < 4; ++n) acc[a][n] = mfma(B[n], A[a], acc[a][n]);
        };
        BAR();
        STAMP(14);
        rd(XA, XB);
#pragma unroll 1
        for (int i = 0; i < 15; ++i) {
          BAR(); rd(YA, YB); mm(XA, XB); interleave<8, 6, 1>(); SCHED();
          BAR(); rd(XA, XB); mm(YA, YB); interleave<8, 6, 1>(); SCHED();
        }
        BAR(); rd(YA, YB); mm(XA, XB); interleave<8, 6, 1>(); SCHED();
        mm(YA, YB);
      } else {
        auto rd = [&](bf16x8(&A)[2], bf16x8(&B)[4]) {
          LAS char* pa = lds + sa * 8192 + aoff;
          LAS char* pb = lds + sb * 14336 + boff;
          A[0] = ldsfrag(pa); A[1] = ldsfrag(pa + 2048);
#pragma unroll
          for (int n = 0; n < 3; ++n) B[n] = ldsfrag(pb + n * 1024);
          sa = (sa + 1 == P4_SA) ? 0 : sa + 1; sb = (sb + 1 == P4_DB) ? 0 : sb + 1;
        };
        auto mm = [&](const bf16x8(&A)[2], const bf16x8(&B)[4]) {
#pragma unroll
          for (int a = 0; a < 2; ++a)
#pragma unroll
            for (int n = 0; n < 3; ++n) acc[a][n] = mfma(B[n], A[a], acc[a][n]);
        };
        BAR();
        STAMP(14);
        rd(XA, XB);
#pragma unroll 1
        for (int i = 0; i < 15; ++i) {
          BAR(); rd(YA, YB); mm(XA, XB); interleave<6, 5, 1>(); SCHED();
          BAR(); rd(XA, XB); mm(YA, YB); interleave<6, 5, 1>(); SCHED();
        }
        BAR(); rd(YA, YB); mm(XA, XB); interleave<6, 5, 1>(); SCHED();
        mm(YA, YB);
      }
      STAMP(15);
      LAS char* xo = lds + P4_X + w * 4096 + lane * 16;
      LAS char* xi = lds + P4_X + (w ^ 1) * 4096 + lane * 16;
#pragma unroll
      for (int n = 0; n < 4; ++n) lds_put(xo + n * 1024, sel4(k4, acc[0][n], acc[1][n]));
      wait_lgkm0();
      BAR();
      u32x2 up[4];
#pragma unroll
      for (int n = 0; n < 4; ++n) {
        const f32x4 gv = sel4(k4, acc[1][n], acc[0][n]) + lds_get(xi + n * 1024);
        up[n] = (u32x2){pk2(bflo(z1p[2 * n]) * rbf(rbf(gv[0]) + bs), bfhi(z1p[2 * n]) * rbf(rbf(gv[1]) + bs)),
                        pk2(bflo(z1p[2 * n + 1]) * rbf(rbf(gv[2]) + bs), bfhi(z1p[2 * n + 1]) * rbf(rbf(gv[3]) + bs))};
      }
      const rsrc_t ru = mkrs(ws + WS_U, 32 * 28 * 4 * 1024);
      auto u_off = [&](int nf) { const int c = 14 * j + 2 * nf + (q >> 1); return (unsigned)(((g * 28 + (c >> 2)) * 4 + m) * 1024 + ((c & 3) * 16 + l15) * 16); };
      const int odd = q & 1;
      st16<SC1>(pair_swap(up[0], up[1]), ru, u_off(4 * nh + odd), 0);
      if (nh) st8<SC1>(up[2], ru, u_off(6) + odd * 8, 0);
      else st16<SC1>(pair_swap(up[2], up[3]), ru, u_off(2 + odd), 0);
      STAMP(24);
      BAR();
      publish(lds, 6, 8, ws, WS_FLAG4 + (g * 8 + j) * 128, T, lane);
      if (b == 0 && w == 0 && lane == 0) __hip_atomic_store((GAS u64*)(ws + WS_EPOCH), T, __ATOMIC_RELAXED, __HIP_MEMORY_SCOPE_AGENT);
      STAMP(16);
    }

    {
      LANE_IDS();
      constexpr int NT = 14;
      const int nb = kh ? 3 : 0;
      group_wait(lds, 10, ws, WS_FLAG4 + g * 1024, 8, T, w == 0, lane);
      STAMP(17);
      const rsrc_t ra = mkrs(ws + WS_U + (size_t)g * 28 * 4 * 1024, 28 * 4 * 1024);
      u32x4 A[NT];
#pragma unroll
      for (int t = 0; t < AW; ++t) A[t] = ld16<SC1>(ra, lane * 16, ((2 * rotk(t, r5, NT) + kh) * 4 + m) * 1024);
      f32x4 acc[5];
#pragma unroll
      for (int n = 0; n < 5; ++n) acc[n] = (f32x4){0.f, 0.f, 0.f, 0.f};
      bf16x8 B[2][5];
      auto rd = [&](bf16x8(&Bv)[5], int slot) {
#pragma unroll
        for (int n = 0; n < 5; ++n) Bv[n] = ldsfrag(lds + slot * 10240 + kh * 5120 + n * 1024 + lane * 16);
      };
      BAR();
      STAMP(18);
      rd(B[0], 0);
#pragma unroll
      for (int t = 0; t < NT; ++t) {
        if (t + 1 < NT) { BAR(); rd(B[(t + 1) & 1], t + 1); }
        if (t + AW < NT) A[t + AW] = ld16<SC1>(ra, lane * 16, ((2 * rotk(t + AW, r5, NT) + kh) * 4 + m) * 1024);
#pragma unroll
        for (int n = 0; n < 5; ++n) acc[n] = mfma(B[t & 1][n], asfrag(A[t]), acc[n]);
        interleave<5, 5, 1>(); SCHED();
      }
      STAMP(19);
      LAS char* xo = lds + P5_X + w * 3072 + lane * 16;
      LAS char* xi = lds + P5_X + (w ^ 4) * 3072 + lane * 16;
      if (kh) { lds_put(xo, acc[0]); lds_put(xo + 1024, acc[1]); lds_put(xo + 2048, acc[2]); }
      else { lds_put(xo, acc[3]); lds_put(xo + 1024, acc[4]); }
      wait_lgkm0();
      BAR();
      u32x2 op[3];
#pragma unroll
      for (int n = 0; n < 3; ++n) {
        const u32x2 x1v = *(LAS u32x2*)(lds + L_X1 + (w * 3 + n) * 512 + lane * 8);
        const unsigned x1p[6] = {x1v.x, x1v.y, x1v.x, x1v.y, x1v.x, x1v.y};
        const f32x4 o = sel4(kh, acc[n == 2 ? 4 : 3 + n], acc[n]) + lds_get(xi + n * 1024);
        const int col = (kh && n == 2) ? 0 : 16 * (nb + n) + 4 * q;
        const f32x4 gb = lds_get(lds + L_TAB + (4 * 80 + col) * 4);
        op[n] = (u32x2){pk2(bflo(x1p[2 * n]) + rbf(gb[0] * rbf(o[0])), bfhi(x1p[2 * n]) + rbf(gb[1] * rbf(o[1]))),
                        pk2(bflo(x1p[2 * n + 1]) + rbf(gb[2] * rbf(o[2])), bfhi(x1p[2 * n + 1]) + rbf(gb[3] * rbf(o[3])))};
      }
      const rsrc_t ro = mkrs(p.out, SEQ * DM * 2);
      const unsigned rowb = (unsigned)((64 * g + 16 * m + l15) * (DM * 2) + 80 * j * 2);
      const int odd = q & 1;
      st16<16>(pair_swap(op[0], op[1]), ro, rowb + (16 * (nb + odd) + 8 * (q >> 1)) * 2, 0);
      st8<16>(op[2], ro, kh ? OOB : rowb + (32 + 4 * q) * 2, 0);
      STAMP(20); STAMPC(31);
    }
  } else if (wid < 10) {
    LANE_IDS();
    const int h = wid - 8;
    const unsigned sw0 = (unsigned)(((lane & 7) ^ (lane >> 4)) * 16), sw1 = sw0 ^ 64;
    wait_lgkm0();
    BAR();
    {
      const rsrc_t r = mkrs((h ? p.wsim : p.wsre) + (size_t)g * 64 * SEQ, 64 * SEQ * 2);
      const unsigned v0 = (lane >> 3) * 4096 + sw0, v1 = (lane >> 3) * 4096 + sw1;
      auto issue = [&](int t, int slot) {
        const unsigned k = (unsigned)((t + rot) & 31) * 128;
#pragma unroll
        for (int pc = 0; pc < 8; ++pc) dma16<0>(r, lds + slot * 16384 + (h * 8 + pc) * 1024, (pc & 1) ? v1 : v0, k + pc * 32768);
      };
      auto touch = [&](int it) {
        __builtin_amdgcn_raw_ptr_buffer_load_lds(r, (LAS void*)(lds + L_SINK), 4, (int)((j * 8 + (lane >> 3)) * 4096 + ((it * 8 + (lane & 7) + rot) & 31) * 128), 0, 0, 0);
      };
      lds_wait(lds, 15); lds_wait(lds, 16);
      touch(0); touch(1);
      lds_wait(lds, 11);
      loader_fill<0, 2, P1_SA>(issue);
      STAMP(1);
      loader_run<32, P1_SA, 8, 2, 2, -1>(issue, [&](int i) { if (i == 4) touch(2); if (i == 12) touch(3); });
      BAR(); BAR();
    }
    {
      const unsigned v0 = (lane >> 3) * 1280 + (h ? sw1 : sw0), v1 = (lane >> 3) * 1280 + (h ? sw0 : sw1);
      auto issue = [&](int t0, int slot) {
        const int t = rotk(t0, r2, 20), im = t >= 10;
        const rsrc_t r = mkrs((im ? p.wdim : p.wdre) + (size_t)j * 80 * DM, 80 * DM * 2);
        const unsigned k = (unsigned)(t - 10 * im) * 128 + h * 5 * 8 * 1280;
#pragma unroll
        for (int pc = 0; pc < 5; ++pc) dma16<0>(r, lds + slot * 10240 + (h * 5 + pc) * 1024, (pc & 1) ? v1 : v0, k + pc * 8 * 1280);
      };
      loader_fill<0, PF2, P2_RING>(issue);
      STAMP(2);
      loader_run<20, P2_RING, 5, PF2, 2, -1>(issue);
      BAR(); BAR();
    }
    {
      const rsrc_t r = mkrs(ws + WS_WINR + (size_t)j * 20 * 14336, 20 * 14336);
      auto issue = [&](int t0, int slot) {
        const int t = t0 < 10 ? rotk(t0, r3, 10) : 10 + rotk(t0 - 10, r3, 10);
#pragma unroll
        for (int pc = 0; pc < 7; ++pc) dma16<SC1>(r, lds + slot * 14336 + (h * 7 + pc) * 1024, lane * 16, (unsigned)(t * 14336 + (h * 7 + pc) * 1024));
      };
      loader_fill<0, PF3, P3_RING>(issue);
      STAMP(3);
      loader_run<20, P3_RING, 7, PF3, 2, 9>(issue);
      BAR(); BAR();
    }
    {
      const rsrc_t r = mkrs(p.wsp + (size_t)g * 64 * SEQ, 64 * SEQ * 2);
      const unsigned v0 = (lane >> 3) * 4096 + sw0, v1 = (lane >> 3) * 4096 + sw1;
      auto issue = [&](int t, int slot) {
        const unsigned k = (unsigned)((t + rot) & 31) * 128 + h * 4 * 32768;
#pragma unroll
        for (int pc = 0; pc < 4; ++pc) dma16<0>(r, lds + slot * 8192 + (h * 4 + pc) * 1024, (pc & 1) ? v1 : v0, k + pc * 32768);
      };
      loader_fill<0, PF4, P4_SA>(issue);
      STAMP(4);
      loader_run<32, P4_SA, 4, PF4, 2, -1>(issue);
      BAR(); BAR();
    }
    {
      const rsrc_t r = mkrs(ws + WS_WOUTR + (size_t)j * 14 * 10240, 14 * 10240);
      auto issue = [&](int t0, int slot) {
        const int t = rotk(t0, r5, 14);
#pragma unroll
        for (int pc = 0; pc < 5; ++pc) dma16<SC1>(r, lds + slot * 10240 + (h * 5 + pc) * 1024, lane * 16, (unsigned)(t * 10240 + (h * 5 + pc) * 1024));
      };
      loader_fill<0, PF5, P5_RING>(issue);
      STAMP(5);
      loader_run<14, P5_RING, 5, PF5, 2, -1>(issue);
      BAR();
    }
  } else {
    LANE_IDS();
    const int h = wid - 10;
    wait_lgkm0();
    BAR();
    {
      LAS char* sc = lds + L_RT + h * 10240;
      const int u = b * 2 + h;
      const int qq = l15 >> 2, pp = l15 & 3;
      u32x4 wv[20]; unsigned cv[20];
      const int cg = b % 60, kq = b / 60;
      if (h == 1) {
        const rsrc_t rw = mkrs(p.wmod, b < 240 ? DM * 3840 * 2 : 0), rc = mkrs(p.c, DM * 2);
#pragma unroll
        for (int it = 0; it < 20; ++it) {
          const int row = (kq & 3) * 160 + it * 8 + (lane >> 3);
          wv[it] = ld16<0>(rw, (unsigned)(row * 7680 + cg * 128 + (lane & 7) * 16), 0);
          cv[it] = ld2<0>(rc, row * 2, 0);
        }
      }
      if (u < 320) {
        const int jp = u / 20, kf = u - 20 * jp, jj = jp >> 1, part = jp & 1;
        const rsrc_t r = mkrs(p.win, DM * FF * 2);
#pragma unroll
        for (int pc = 0; pc < 7; ++pc) {
          const int idx = pc * 64 + lane, row = idx / 14, ch = idx - 14 * row;
          const bool ok = 112 * jj + 8 * ch < FH;
          dma16<0>(r, sc + pc * 1024, ok ? (unsigned)((32 * kf + row) * (FF * 2) + (part ? 0 : FH * 2) + 224 * jj + ch * 16) : OOB, 0);
        }
      } else if (u < 432) {
        const int uu = u - 320, jj = uu / 14, kp = uu - 14 * jj;
        const rsrc_t r = mkrs(p.wout, FH * DM * 2);
#pragma unroll
        for (int pc = 0; pc < 10; ++pc) {
          const int idx = pc * 64 + lane, row = idx / 10, ch = idx - 10 * row;
          dma16<0>(r, sc + pc * 1024, (unsigned)((64 * kp + row) * (DM * 2) + 160 * jj + ch * 16), 0);
        }
      }
      lds_signal(lds, 15 + h, lane);
      if (h == 1) {
        float a8[8] = {0.f, 0.f, 0.f, 0.f, 0.f, 0.f, 0.f, 0.f};
#pragma unroll
        for (int it = 0; it < 20; ++it) {
          const float cf = bflo(cv[it]);
          const float sl = rbf(cf * __builtin_amdgcn_rcpf(1.0f + __builtin_amdgcn_exp2f(-1.4426950408889634f * cf)));
#pragma unroll
          for (int i = 0; i < 4; ++i) { a8[2 * i] += sl * bflo(wv[it][i]); a8[2 * i + 1] += sl * bfhi(wv[it][i]); }
        }
#pragma unroll
        for (int i = 0; i < 8; ++i) { a8[i] += __shfl_xor(a8[i], 8); a8[i] = xq_sum(a8[i]); }
        const rsrc_t rm = mkrs(ws + WS_MODP, 4 * 3840 * 4);
        const unsigned mo = (unsigned)((kq * 3840 + cg * 64 + (lane & 7) * 8) * 4);
        const bool on = lane < 8 && b < 240;
        st16<SC1>((u32x4){__float_as_uint(a8[0]), __float_as_uint(a8[1]), __float_as_uint(a8[2]), __float_as_uint(a8[3])}, rm, on ? mo : OOB, 0);
        st16<SC1>((u32x4){__float_as_uint(a8[4]), __float_as_uint(a8[5]), __float_as_uint(a8[6]), __float_as_uint(a8[7])}, rm, on ? mo + 16 : OOB, 0);
      }
      if (h == 0) wait_vm<0>(); else wait_vm<2>();
      CFENCE();
      if (u < 320) {
        const int jp = u / 20, kf = u - 20 * jp, jj = jp >> 1, part = jp & 1;
        const rsrc_t ro = mkrs(ws + WS_WINR, 8 * 20 * 14336);
#pragma unroll
        for (int nf = 0; nf < 7; ++nf) {
          LAS char* a0 = sc + (8 * q + qq) * 224 + (16 * nf + 4 * pp) * 2;
          const u32x2 lo = __builtin_bit_cast(u32x2, __builtin_amdgcn_ds_read_tr16_b64_v4i16((LAS s16x4*)a0));
          const u32x2 hi = __builtin_bit_cast(u32x2, __builtin_amdgcn_ds_read_tr16_b64_v4i16((LAS s16x4*)(a0 + 4 * 224)));
          st16<SC1>((u32x4){lo.x, lo.y, hi.x, hi.y}, ro, (unsigned)((((jj * 2 + part) * 20 + kf) * 7 + nf) * 1024 + lane * 16), 0);
        }
      } else if (u < 432) {
        const int uu = u - 320, jj = uu / 14, kp = uu - 14 * jj;
        const rsrc_t ro = mkrs(ws + WS_WOUTR, 8 * 14 * 10240);
#pragma unroll
        for (int f = 0; f < 10; ++f) {
          const int kk = f / 5, nf = f - 5 * kk;
          LAS char* a0 = sc + (32 * kk + 8 * q + qq) * 160 + (16 * nf + 4 * pp) * 2;
          const u32x2 lo = __builtin_bit_cast(u32x2, __builtin_amdgcn_ds_read_tr16_b64_v4i16((LAS s16x4*)a0));
          const u32x2 hi = __builtin_bit_cast(u32x2, __builtin_amdgcn_ds_read_tr16_b64_v4i16((LAS s16x4*)(a0 + 4 * 160)));
          st16<SC1>((u32x4){lo.x, lo.y, hi.x, hi.y}, ro, (unsigned)(((jj * 14 + kp) * 10 + f) * 1024 + lane * 16), 0);
        }
      }
      wait_lgkm0(); CFENCE();
    }
    const u64 T = epoch_tag(Ein);
    STAMP(1);
    {
      if (h == 0) {
        lds_wait(lds, 14);
        __builtin_amdgcn_s_sleep(10);
        poll_flags256(ws, WS_FLAG0 + xg * 4096, T, lane);
        lds_signal(lds, 12, lane);
      } else {
        lds_wait(lds, 12);
      }
      STAMP(2);
      const rsrc_t r = mkrs(ws + WS_XNT + (size_t)j * 32 * 10240, 32 * 10240);
      auto issue = [&](int t, int slot) {
        const unsigned k = (unsigned)((t + rot) & 31) * 10240;
#pragma unroll
        for (int pc = 0; pc < 5; ++pc) dma16<SC1>(r, lds + P1_DOFF + slot * 10240 + (h * 5 + pc) * 1024, lane * 16, k + (h * 5 + pc) * 1024);
      };
      loader_fill<0, 1, P1_DB>(issue);
      if (h == 0) lds_signal(lds, 11, lane);
      loader_fill<1, 2, P1_DB>(issue);
      wait_vm<5>();
      if (arrive_last(lds, 7, 2, lane)) set_flag(ws, WS_FLAGB + g * 128 + j * 8, T, lane);
      loader_run<32, P1_DB, 5, 2, 2, -1>(issue, [&](int i) {
        if (i == 20 && h == 1) {
          const int bi = g * 8 + j;
          const rsrc_t r3 = mkrs(p.wdre, DM * DM * 2), r4 = mkrs(p.wdim, DM * DM * 2);
          __builtin_amdgcn_raw_ptr_buffer_load_lds(r3, (LAS void*)(lds + L_SINK), 4, lane < 25 ? (int)((bi * 25 + lane) * 128) : (int)OOB, 0, 0, 0);
          __builtin_amdgcn_raw_ptr_buffer_load_lds(r4, (LAS void*)(lds + L_SINK), 4, lane < 25 ? (int)((bi * 25 + lane) * 128) : (int)OOB, 0, 0, 0);
        }
      });
      BAR(); BAR();
    }
    if (h == 0 && b < 112) {
      const int jj = b / 14, pn = b - 14 * jj, part = pn / 7, nf = pn - 7 * part;
      const rsrc_t rw = mkrs(ws + WS_WINR, 8 * 20 * 14336), rm = mkrs(ws + WS_MODP, 4 * 3840 * 4);
      u32x4 wf[20];
#pragma unroll
      for (int k = 0; k < 20; ++k) wf[k] = ld16<SC1>(rw, lane * 16, (unsigned)((((jj * 2 + part) * 20 + k) * 7 + nf) * 1024));
#pragma unroll
      for (int it = 0; it < 5; ++it) {
        const int c4 = (it * 64 + lane) * 4, isS = c4 >= DM, cc = c4 - DM * isS;
        const unsigned o = (unsigned)(((isS ? 3 : 4) * DM + cc) * 4);
        const f32x4 m0 = __builtin_bit_cast(f32x4, ld16<SC1>(rm, o, 0)), m1 = __builtin_bit_cast(f32x4, ld16<SC1>(rm, o, 3840 * 4)),
                    m2 = __builtin_bit_cast(f32x4, ld16<SC1>(rm, o, 2 * 3840 * 4)), m3 = __builtin_bit_cast(f32x4, ld16<SC1>(rm, o, 3 * 3840 * 4));
        f32x4 s = (m0 + m1) + (m2 + m3);
#pragma unroll
        for (int i = 0; i < 4; ++i) { s[i] = rbf(s[i]); if (!isS) s[i] = 1.0f + s[i]; }
        *(LAS u32x2*)(lds + L_TABE + (isS * DM + cc) * 2) = (u32x2){pk2(s[0], s[1]), pk2(s[2], s[3])};
      }
      wait_lgkm0(); CFENCE();
      f32x4 acc = {0.f, 0.f, 0.f, 0.f};
#pragma unroll
      for (int k = 0; k < 20; ++k) {
        u32x4 e = *(LAS u32x4*)(lds + L_TABE + ((l15 & 1) * DM + 32 * k + 8 * q) * 2);
        if (l15 >= 2) e = (u32x4){0u, 0u, 0u, 0u};
        acc = mfma(asfrag(e), asfrag(wf[k]), acc);
      }
      const rsrc_t ruv = mkrs(ws + WS_UV, 2 * 896 * 16);
      st16<SC1>((u32x4){__float_as_uint(acc[0]), __float_as_uint(acc[1]), (unsigned)T, (unsigned)(T >> 32)}, ruv,
                q == 0 ? (unsigned)((part * 896 + 112 * jj + 16 * nf + l15) * 16) : OOB, 0);
    }
    STAMP(3);
    idle_phase<22>();
    idle_phase<4>();
    if (h == 1) {
      const rsrc_t r1 = mkrs(p.wsp + (size_t)(64 * g + 8 * j) * SEQ, 8 * SEQ * 2);
#pragma unroll
      for (int it = 0; it < 4; ++it)
        __builtin_amdgcn_raw_ptr_buffer_load_lds(r1, (LAS void*)(lds + L_SINK), 4, (int)((lane >> 3) * 4096 + ((it * 8 + (lane & 7) + rot) & 31) * 128), 0, 0, 0);
    }
    idle_phase<19>();
    {
      group_wait(lds, 17, ws, WS_FLAG3 + j * 4096, 32, T, h == 0, lane);
      STAMP(4);
      const rsrc_t r = mkrs(ws + WS_Z2T + (size_t)j * 32 * 14336, 32 * 14336);
      auto issue = [&](int t, int slot) {
        const unsigned k = (unsigned)((t + rot) & 31) * 14336;
#pragma unroll
        for (int pc = 0; pc < 7; ++pc) dma16<SC1>(r, lds + P4_DOFF + slot * 14336 + (h * 7 + pc) * 1024, lane * 16, k + (h * 7 + pc) * 1024);
      };
      loader_fill<0, 2, P4_DB>(issue);
      loader_run<32, P4_DB, 7, 2, 2, -1>(issue);
      BAR(); BAR();
    }
    idle_phase<15>();
  }
}

extern "C" void kernel_launch(void* const* d_in, const int* in_sizes, int n_in, void* d_out, int out_size, void* d_ws, size_t ws_size,
                              hipStream_t stream) {
  Params p;
  p.x = (const u16*)d_in[0]; p.wsre = (const u16*)d_in[1]; p.wsim = (const u16*)d_in[2]; p.wdre = (const u16*)d_in[3]; p.wdim = (const u16*)d_in[4];
  p.win = (const u16*)d_in[6]; p.wsp = (const u16*)d_in[7]; p.bsp = (const u16*)d_in[8]; p.wout = (const u16*)d_in[9];
  p.c = (const u16*)d_in[10]; p.wmod = (const u16*)d_in[11];
  p.out = (u16*)d_out; p.ws = (char*)d_ws + ((ws_size - WS_USED) & ~(size_t)0x1fffff);
  (void)hipFuncSetAttribute((const void*)adaLN_cond__spectral_fnet__mixer_gmlp__prompt_kernel, hipFuncAttributeMaxDynamicSharedMemorySize, LDS_BYTES);
  adaLN_cond__spectral_fnet__mixer_gmlp__prompt_kernel<<<256, 768, LDS_BYTES, stream>>>(p);
}
```

```cpp
#include <hip/hip_runtime.h>
#include <hip/hip_bf16.h>

typedef unsigned short u16;
typedef unsigned long long u64;
typedef short bf16x8 __attribute__((ext_vector_type(8)));
typedef short s16x4 __attribute__((ext_vector_type(4)));
typedef __bf16 bf16x2 __attribute__((ext_vector_type(2)));
typedef float f32x2 __attribute__((ext_vector_type(2)));
typedef float f32x4 __attribute__((ext_vector_type(4)));
typedef unsigned u32x2 __attribute__((ext_vector_type(2)));
typedef unsigned u32x4 __attribute__((ext_vector_type(4)));
typedef __amdgpu_buffer_rsrc_t rsrc_t;
#define LAS __attribute__((address_space(3)))
#define GAS __attribute__((address_space(1)))
#define DEVI __device__ __forceinline__

constexpr int SEQ = 2048, DM = 640, FF = 1728, FH = 864;

constexpr unsigned WS_EPOCH = 0x0;
constexpr unsigned WS_TMO   = 0x100;
constexpr unsigned WS_FLAG0 = 0x1A00000;
constexpr unsigned WS_FLAGB = 0x1A08000;
constexpr unsigned WS_FLAG1 = 0x1A10000;
constexpr unsigned WS_FLAG2 = 0x1A18000;
constexpr unsigned WS_FLAG4 = 0x1A20000;
constexpr unsigned WS_FLAG3 = 0x1A28000;
constexpr unsigned WS_MODP  = 0x10000;
constexpr unsigned WS_UV    = 0x20000;
constexpr unsigned WS_ST1   = 0x40000;
constexpr unsigned WS_ST2   = 0x80000;
constexpr unsigned WS_XNT   = 0x100000;
constexpr unsigned WS_PQ    = 0x400000;
constexpr unsigned WS_X1C   = 0xA00000;
constexpr unsigned WS_Z2T   = 0xD00000;
constexpr unsigned WS_U     = 0x1100000;
constexpr unsigned WS_WINR  = 0x1500000;
constexpr unsigned WS_WOUTR = 0x1800000;
constexpr unsigned WS_STAMP = 0x2400000;
constexpr unsigned WS_USED  = 0x2600000;

constexpr int LDS_BYTES = 163840;
constexpr int L_MISC = 159744;
constexpr int L_CNT  = L_MISC;
constexpr int L_TAB  = L_MISC + 128;
constexpr int L_UV   = L_MISC + 1728;
constexpr int L_SINK = L_MISC + 3584;
constexpr int L_TABE = 153600;
constexpr int L_P0T  = 98304;
constexpr int L_RT   = 118784;
constexpr int P0T_RS = 1296;

#define T0N 1
#define F0DELAY 10
#define PDELAY 8
constexpr unsigned SPIN_MAX = 1u << 17;

struct Params {
  const u16 *x, *wsre, *wsim, *wdre, *wdim, *win, *wsp, *bsp, *wout, *c, *wmod;
  u16* out;
  char* ws;
};

DEVI rsrc_t mkrs(const void* p, unsigned bytes) { return __builtin_amdgcn_make_buffer_rsrc((void*)p, 0, (int)bytes, 0x00020000); }
template <int AUX> DEVI u32x4 ld16(rsrc_t r, unsigned v, unsigned s) { return __builtin_amdgcn_raw_buffer_load_b128(r, (int)v, (int)s, AUX); }
template <int AUX> DEVI u32x2 ld8(rsrc_t r, unsigned v, unsigned s) { return __builtin_amdgcn_raw_buffer_load_b64(r, (int)v, (int)s, AUX); }
template <int AUX> DEVI unsigned ld4(rsrc_t r, unsigned v, unsigned s) { return __builtin_amdgcn_raw_buffer_load_b32(r, (int)v, (int)s, AUX); }
template <int AUX> DEVI unsigned ld2(rsrc_t r, unsigned v, unsigned s) { return (unsigned)__builtin_amdgcn_raw_buffer_load_b16(r, (int)v, (int)s, AUX) & 0xffffu; }
template <int AUX> DEVI void st16(u32x4 d, rsrc_t r, unsigned v, unsigned s) { __builtin_amdgcn_raw_buffer_store_b128(d, r, (int)v, (int)s, AUX); }
template <int AUX> DEVI void st8(u32x2 d, rsrc_t r, unsigned v, unsigned s) { __builtin_amdgcn_raw_buffer_store_b64(d, r, (int)v, (int)s, AUX); }
template <int AUX> DEVI void dma16(rsrc_t r, LAS char* dst, unsigned v, unsigned s) { __builtin_amdgcn_raw_ptr_buffer_load_lds(r, (LAS void*)dst, 16, (int)v, (int)s, 0, AUX); }
constexpr int SC1 = 16;
constexpr unsigned OOB = 0x7ffffff0u;

template <int N> DEVI void wait_vm() { asm volatile("s_waitcnt vmcnt(%0)" ::"n"(N > 63 ? 63 : N) : "memory"); }
DEVI void wait_lgkm0() { __builtin_amdgcn_s_waitcnt(0xC07F); }
DEVI void BAR() { asm volatile("s_barrier" ::: "memory"); }
DEVI void CFENCE() { asm volatile("" ::: "memory"); }
#define SCHED() __builtin_amdgcn_sched_barrier(0)

DEVI float bflo(unsigned v) { return __uint_as_float(v << 16); }
DEVI float bfhi(unsigned v) { return __uint_as_float(v & 0xffff0000u); }
DEVI unsigned pk2(float lo, float hi) { f32x2 f = {lo, hi}; return __builtin_bit_cast(unsigned, __builtin_convertvector(f, bf16x2)); }
DEVI float rbf(float v) { return bflo(pk2(v, 0.f)); }
DEVI f32x4 mfma(bf16x8 a, bf16x8 b, f32x4 c) { return __builtin_amdgcn_mfma_f32_16x16x32_bf16(a, b, c, 0, 0, 0); }
DEVI bf16x8 asfrag(u32x4 v) { return __builtin_bit_cast(bf16x8, v); }
DEVI bf16x8 ldsfrag(LAS char* p) { return *(LAS bf16x8*)p; }

template <int CTRL> DEVI float dpp_add(float v) { return v + __int_as_float(__builtin_amdgcn_update_dpp(0, __float_as_int(v), CTRL, 0xf, 0xf, false)); }
DEVI float row16_sum(float v) { v = dpp_add<0x128>(v); v = dpp_add<0x124>(v); v = dpp_add<0x122>(v); return dpp_add<0x121>(v); }
DEVI f32x2 both16(float v) { auto r = __builtin_amdgcn_permlane16_swap(__float_as_uint(v), __float_as_uint(v), false, false); return (f32x2){__uint_as_float(r[0]), __uint_as_float(r[1])}; }
DEVI f32x2 both32(float v) { auto r = __builtin_amdgcn_permlane32_swap(__float_as_uint(v), __float_as_uint(v), false, false); return (f32x2){__uint_as_float(r[0]), __uint_as_float(r[1])}; }
DEVI float xq_sum(float v) { f32x2 a = both16(v); v = a.x + a.y; a = both32(v); return a.x + a.y; }
DEVI float rsq(float v) { return __builtin_amdgcn_rsqf(v); }
DEVI float wave_sum(float v) { return xq_sum(row16_sum(v)); }

DEVI float gelu_tanh(float x) {
  constexpr float k1 = -2.0f * 0.7978845608028654f * 1.4426950408889634f, k2 = k1 * 0.044715f;
  return x * __builtin_amdgcn_rcpf(1.0f + __builtin_amdgcn_exp2f(x * __builtin_fmaf(x * x, k2, k1)));
}
DEVI f32x2 ln_stats(float s, float q, float inv_n) {
  const float m = s * inv_n, v = __builtin_fmaf(q, inv_n, -m * m);
  return (f32x2){m, __builtin_amdgcn_rsqf((v > 0.f ? v : 0.f) + 1e-5f)};
}

DEVI u32x4 pair_swap(u32x2 a, u32x2 b) {
  auto s0 = __builtin_amdgcn_permlane16_swap(a.x, b.x, false, false);
  auto s1 = __builtin_amdgcn_permlane16_swap(a.y, b.y, false, false);
  return (u32x4){s0[0], s1[0], s0[1], s1[1]};
}

DEVI void note_timeout(char* ws, unsigned code, int lane) {
  if (lane == 0) __hip_atomic_store((GAS unsigned*)(ws + WS_TMO), code, __ATOMIC_RELAXED, __HIP_MEMORY_SCOPE_AGENT);
}
DEVI void poll_flags(char* ws, unsigned off, int n, u64 T, int lane) {
  for (unsigned spins = 0;; ++spins) {
    u64 f = T;
    if (lane < n) f = __hip_atomic_load((GAS u64*)(ws + off + lane * 128), __ATOMIC_RELAXED, __HIP_MEMORY_SCOPE_AGENT);
    if (__all(f == T)) break;
    if (spins > SPIN_MAX) { note_timeout(ws, 0x100u | ((off >> 15) & 0xff), lane); break; }
    __builtin_amdgcn_s_sleep(1);
  }
  CFENCE();
}
DEVI void poll_flags256(char* ws, unsigned off, u64 T, int lane) {
  for (unsigned spins = 0;; ++spins) {
    bool ok = true;
#pragma unroll
    for (int i = 0; i < 4; ++i) ok &= __hip_atomic_load((GAS u64*)(ws + off + (i * 8 + (lane >> 3)) * 128 + (lane & 7) * 8), __ATOMIC_RELAXED, __HIP_MEMORY_SCOPE_AGENT) == T;
    if (__all(ok)) break;
    if (spins > SPIN_MAX) { note_timeout(ws, 0x200u | ((off >> 15) & 0xff), lane); break; }
    __builtin_amdgcn_s_sleep(4);
  }
  CFENCE();
}
DEVI void set_flag(char* ws, unsigned off, u64 T, int lane) {
  if (lane == 0) __hip_atomic_store((GAS u64*)(ws + off), T, __ATOMIC_RELAXED, __HIP_MEMORY_SCOPE_AGENT);
}
DEVI void lds_signal(LAS char* lds, int idx, int lane) {
  CFENCE();
  if (lane == 0) __hip_atomic_store((LAS unsigned*)(lds + L_CNT) + idx, 1u, __ATOMIC_RELAXED, __HIP_MEMORY_SCOPE_WORKGROUP);
}
DEVI void lds_wait(LAS char* lds, int idx) {
  for (unsigned spins = 0; spins < (SPIN_MAX << 6); ++spins) {
    if (__hip_atomic_load((LAS unsigned*)(lds + L_CNT) + idx, __ATOMIC_RELAXED, __HIP_MEMORY_SCOPE_WORKGROUP) != 0u) break;
    __builtin_amdgcn_s_sleep(1);
  }
  CFENCE();
}
DEVI void lds_wait_n(LAS char* lds, int idx, unsigned n) {
  for (unsigned spins = 0; spins < (SPIN_MAX << 6); ++spins) {
    if (__hip_atomic_load((LAS unsigned*)(lds + L_CNT) + idx, __ATOMIC_RELAXED, __HIP_MEMORY_SCOPE_WORKGROUP) >= n) break;
    __builtin_amdgcn_s_sleep(1);
  }
  CFENCE();
}
DEVI void group_wait(LAS char* lds, int idx, char* ws, unsigned off, int n, u64 T, bool poller, int lane) {
  if (poller) { __builtin_amdgcn_s_sleep(PDELAY); poll_flags(ws, off, n, T, lane); lds_signal(lds, idx, lane); } else lds_wait(lds, idx);
}
DEVI bool arrive_last(LAS char* lds, int idx, unsigned n, int lane) {
  unsigned old = 0;
  if (lane == 0) old = __hip_atomic_fetch_add((LAS unsigned*)(lds + L_CNT) + idx, 1u, __ATOMIC_RELAXED, __HIP_MEMORY_SCOPE_WORKGROUP);
  return (unsigned)__builtin_amdgcn_readfirstlane(old) == n - 1;
}
DEVI void publish(LAS char* lds, int cidx, unsigned nwaves, char* ws, unsigned flag_off, u64 T, int lane) {
  wait_vm<0>();
  if (arrive_last(lds, cidx, nwaves, lane)) set_flag(ws, flag_off, T, lane);
}

#define LANE_IDS() int tz_ = threadIdx.x; asm volatile("" : "+v"(tz_)); const int lane = tz_ & 63, l15 = lane & 15, q = lane >> 4; (void)l15; (void)q

#define LANE_IDS2() int tz2_ = threadIdx.x; asm volatile("" : "+v"(tz2_)); const int laneb = tz2_ & 63, l15b = laneb & 15, qb = laneb >> 4; (void)l15b; (void)qb

#define STAMP(k) do { } while (0)
#define STAMPC(k) do { } while (0)

template <int NT, int RING, int P0, int CU> constexpr int issued(int i) {
  int n = P0 < NT ? P0 : NT;
  for (int k = 0; k <= i; ++k) { int m = n + CU; if (m > k + RING) m = k + RING; if (m > NT) m = NT; n = m; }
  return n;
}
constexpr int cap63(int n, int pc) { return n > 63 - pc ? 63 - pc : n; }
template <int I, int IEND, int NT, int RING, int PC, int P0, int CU, int XI, class Issue> DEVI void loader_steps(Issue& issue) {
  if constexpr (I < IEND) {
    constexpr int before = issued<NT, RING, P0, CU>(I - 1), after = issued<NT, RING, P0, CU>(I);
    wait_vm<cap63((before - (I + 2)) * PC, (after - before) * PC)>(); BAR();
    if constexpr (after > before) issue(before, before % RING);
    if constexpr (after > before + 1) issue(before + 1, (before + 1) % RING);
    if constexpr (after > before + 2) issue(before + 2, (before + 2) % RING);
    static_assert(after <= before + 3, "at most three tiles per step");
    if constexpr (I == XI) BAR();
    loader_steps<I + 1, IEND, NT, RING, PC, P0, CU, XI>(issue);
  }
}
template <int NT, int RING, int P0, int CU> constexpr int steady_from() {
  for (int i = 0; i < NT; ++i) if (issued<NT, RING, P0, CU>(i - 1) == i - 1 + RING) return i;
  return NT;
}
template <int T0, int T1, int RING, class Issue> DEVI void loader_fill(Issue& issue) {
#pragma unroll 1
  for (int t = T0; t < T1; ++t) issue(t, t);
}
struct NoHook { DEVI void operator()(int) const {} };
template <int NT, int RING, int PC, int P0, int CU, int XI, class Issue, class Hook = NoHook> DEVI void loader_run(Issue& issue, Hook hook = Hook()) {
  constexpr int P = P0 < NT ? P0 : NT;
  wait_vm<cap63((P - 1) * PC, 0)>(); BAR();
  constexpr int S0 = steady_from<NT, RING, P0, CU>(), S1 = NT - RING + 1;
  if constexpr (XI < 0 && S0 + 2 < S1) {
    loader_steps<0, S0, NT, RING, PC, P0, CU, XI>(issue);
    int slot = (S0 - 1 + RING) % RING;
#pragma unroll 1
    for (int i = S0; i < S1; ++i) {
      wait_vm<cap63((RING - 3) * PC, PC)>(); BAR();
      issue(i - 1 + RING, slot);
      hook(i);
      slot = (slot + 1 == RING) ? 0 : slot + 1;
    }
    loader_steps<S1, NT - 1, NT, RING, PC, P0, CU, XI>(issue);
  } else {
    loader_steps<0, NT - 1, NT, RING, PC, P0, CU, XI>(issue);
  }
}
template <int NBAR> DEVI void idle_phase() {
#pragma unroll 1
  for (int i = 0; i < NBAR; ++i) BAR();
}

template <int I, int NMF, int NRD, int PER> struct Interleave {
  static DEVI void run() {
    if constexpr (I < NMF) {
      __builtin_amdgcn_sched_group_barrier(0x8, 1, 0);
      constexpr int left = NRD - I * PER, now = left < PER ? left : PER;
      if constexpr (now > 0) __builtin_amdgcn_sched_group_barrier(0x100, now, 0);
      Interleave<I + 1, NMF, NRD, PER>::run();
    }
  }
};
template <int NMF, int NRD, int PER> DEVI void interleave() { Interleave<0, NMF, NRD, PER>::run(); }

#define AW 4
#define PF2 12
#define PF3 7
#define PF4 8
#define PF5 12
constexpr int P1_SA = 6, P1_DB = 5, P1_DOFF = 98304, P1_X = 32768;
constexpr int P2_RING = 15, P2_X = 51200;
constexpr int P3_RING = 8, P3_X = 114688;
constexpr int P4_SA = 9, P4_DB = 5, P4_DOFF = 73728, P4_X = 40960;
constexpr int L_X1 = 145408;
constexpr int P5_RING = 14, P5_X = 0;

DEVI f32x4 sel4(int c, f32x4 a, f32x4 b) { asm volatile("" : "+v"(a), "+v"(b)); return c ? a : b; }
DEVI float sel1(int c, float a, float b) { asm volatile("" : "+v"(a), "+v"(b)); return c ? a : b; }
DEVI void lds_put(LAS char* p, f32x4 v) { *(LAS f32x4*)p = v; }
DEVI f32x4 lds_get(LAS char* p) { return *(LAS f32x4*)p; }
DEVI u64 epoch_tag(u64 e) {
  return (((u64)(unsigned)__builtin_amdgcn_readfirstlane((unsigned)(e >> 32)) << 32) | (unsigned)__builtin_amdgcn_readfirstlane((unsigned)e)) + 1;
}
DEVI void chan(float& n, float& m, float& M2, float no, float mo, float Mo) {
  const float nt = n + no, d = mo - m, r = __builtin_amdgcn_rcpf(nt);
  M2 = M2 + Mo + d * d * (n * no * r);
  m = m + d * (no * r);
  n = nt;
}

__global__ __launch_bounds__(768, 3) void adaLN_cond__spectral_fnet__mixer_gmlp__prompt_kernel(Params p) {
  extern __shared__ __attribute__((aligned(16))) char smem_[];
  LAS char* lds = (LAS char*)smem_;
  const int tid = threadIdx.x, wid = __builtin_amdgcn_readfirstlane(tid >> 6);
  const int b = blockIdx.x, xg = b & 7, i8 = b >> 3;
  const int g = xg * 4 + (i8 >> 3), j = i8 & 7;
  char* const ws = p.ws;
  const int rot = xg * 4;

  { LANE_IDS(); STAMP(0); STAMPC(30); }
  const u64 Ein = __hip_atomic_load((GAS u64*)(ws + WS_EPOCH), __ATOMIC_RELAXED, __HIP_MEMORY_SCOPE_AGENT);
  if (tid < 32) *((LAS unsigned*)(lds + L_CNT) + tid) = 0u;

  if (wid < 8) {
    const int w = wid, m = w & 3, kh = w >> 2;
    __builtin_amdgcn_s_setprio(2);
    {
      LANE_IDS();
      const rsrc_t rx = mkrs(p.x, SEQ * DM * 2);
      u32x4 v[2];
#pragma unroll
      for (int h = 0; h < 2; ++h) v[h] = ld16<0>(rx, lane < 40 ? (unsigned)((lane + 40 * h) * 16) : OOB, (unsigned)(64 * g + 8 * j + w) * (DM * 2));
      wait_lgkm0();
      BAR();
      float f[16];
#pragma unroll
      for (int h = 0; h < 2; ++h)
#pragma unroll
        for (int i = 0; i < 4; ++i) { f[h * 8 + 2 * i] = bflo(v[h][i]); f[h * 8 + 2 * i + 1] = bfhi(v[h][i]); }
      float s = 0.f, sq = 0.f;
#pragma unroll
      for (int i = 0; i < 16; ++i) { s += f[i]; sq = __builtin_fmaf(f[i], f[i], sq); }
      const f32x2 ls = ln_stats(wave_sum(s), wave_sum(sq), 1.0f / DM);
      const float rstd = ls.y, c0 = -ls.x * ls.y;
      if (lane < 40) {
#pragma unroll
        for (int h = 0; h < 2; ++h) {
          u32x4 o;
#pragma unroll
          for (int i = 0; i < 4; ++i) o[i] = pk2(__builtin_fmaf(f[h * 8 + 2 * i], rstd, c0), __builtin_fmaf(f[h * 8 + 2 * i + 1], rstd, c0));
          *(LAS u32x4*)(lds + L_P0T + w * P0T_RS + (lane + 40 * h) * 16) = o;
        }
      }
      wait_lgkm0(); CFENCE();
      if (lane == 0) __hip_atomic_fetch_add((LAS unsigned*)(lds + L_CNT) + 0, 1u, __ATOMIC_RELAXED, __HIP_MEMORY_SCOPE_WORKGROUP);
      for (unsigned spins = 0; spins < SPIN_MAX; ++spins) {
        if (__hip_atomic_load((LAS unsigned*)(lds + L_CNT) + 0, __ATOMIC_RELAXED, __HIP_MEMORY_SCOPE_WORKGROUP) >= 8u) break;
      }
      CFENCE();
      const rsrc_t rxn = mkrs(ws + WS_XNT, 8 * 32 * 10240);
#pragma unroll
      for (int it = 0; it < 2; ++it) {
        const int pc = it == 0 ? q : 4, nf = 5 * w + pc;
        const int qq = l15 >> 2, pp = l15 & 3;
        LAS char* a0 = lds + L_P0T + qq * P0T_RS + (16 * nf + 4 * pp) * 2;
        const u32x2 lo = __builtin_bit_cast(u32x2, __builtin_amdgcn_ds_read_tr16_b64_v4i16((LAS s16x4*)a0));
        const u32x2 hi = __builtin_bit_cast(u32x2, __builtin_amdgcn_ds_read_tr16_b64_v4i16((LAS s16x4*)(a0 + 4 * P0T_RS)));
        const unsigned off = (unsigned)(((w * 32 + g) * 10 + (j >> 2) * 5 + pc) * 1024 + ((j & 3) * 16 + l15) * 16);
        st16<SC1>((u32x4){lo.x, lo.y, hi.x, hi.y}, rxn, (it == 0 || q == 0) ? off : OOB, 0);
      }
    }
    { LANE_IDS(); if (lane == 0) __hip_atomic_fetch_add((LAS unsigned*)(lds + L_CNT) + 13, 1u, __ATOMIC_RELAXED, __HIP_MEMORY_SCOPE_WORKGROUP); }
    const u64 T = epoch_tag(Ein);
    { LANE_IDS(); wait_vm<0>(); if (arrive_last(lds, 1, 8, lane)) {
      if (lane < 8) __hip_atomic_store((GAS u64*)(ws + WS_FLAG0 + lane * 4096 + g * 128 + j * 8), T, __ATOMIC_RELAXED, __HIP_MEMORY_SCOPE_AGENT);
      lds_signal(lds, 14, lane); } STAMP(1); }

    unsigned z1p[8];

    {
      LANE_IDS();
      f32x4 acc[2][5];
#pragma unroll
      for (int a = 0; a < 2; ++a)
#pragma unroll
        for (int n = 0; n < 5; ++n) acc[a][n] = (f32x4){0.f, 0.f, 0.f, 0.f};
      f32x4 rs[2] = {{0.f, 0.f, 0.f, 0.f}, {0.f, 0.f, 0.f, 0.f}};
      u32x4 ones4 = {0x3f803f80u, 0x3f803f80u, 0x3f803f80u, 0x3f803f80u}; asm volatile("" : "+v"(ones4));
      const bf16x8 ones = asfrag(ones4);
      const unsigned aoff = (m * 16 + l15) * 128 + (((kh * 4 + q) ^ (l15 >> 1)) << 4);
      const unsigned boff = P1_DOFF + kh * 5120 + lane * 16;
      bf16x8 XA[2], XB[5], YA[2], YB[5];
      int sa = 0, sb = 0;
      auto rd = [&](bf16x8(&A)[2], bf16x8(&B)[5]) {
        LAS char* pa = lds + sa * 16384 + aoff;
        LAS char* pb = lds + sb * 10240 + boff;
        A[0] = ldsfrag(pa); A[1] = ldsfrag(pa + 8192);
#pragma unroll
        for (int n = 0; n < 5; ++n) B[n] = ldsfrag(pb + n * 1024);
        sa = (sa + 1 == P1_SA) ? 0 : sa + 1; sb = (sb + 1 == P1_DB) ? 0 : sb + 1;
      };
      auto mm = [&](const bf16x8(&A)[2], const bf16x8(&B)[5]) {
#pragma unroll
        for (int a = 0; a < 2; ++a) {
#pragma unroll
          for (int n = 0; n < 5; ++n) acc[a][n] = mfma(B[n], A[a], acc[a][n]);
          rs[a] = mfma(ones, A[a], rs[a]);
        }
      };
      auto pairs = [&](int n) {
#pragma unroll 1
        for (int i = 0; i < n; ++i) {
          BAR(); rd(YA, YB); mm(XA, XB); interleave<12, 7, 1>(); SCHED();
          BAR(); rd(XA, XB); mm(YA, YB); interleave<12, 7, 1>(); SCHED();
        }
      };
      BAR();
      STAMP(2); STAMPC(28);
      rd(XA, XB);
      pairs(6);
      u64 fb[4];
      if (w == 0) {
#pragma unroll
        for (int i = 0; i < 4; ++i) fb[i] = __hip_atomic_load((GAS u64*)(ws + WS_FLAGB + (i * 8 + (lane >> 3)) * 128 + (lane & 7) * 8), __ATOMIC_RELAXED, __HIP_MEMORY_SCOPE_AGENT);
      }
      pairs(3);
      f32x4 mp[2][4];
      const int vs0 = lane < 20 ? 1 : lane < 40 ? 0 : 2, vs1 = lane < 20 ? 4 : 5;
      const int vl = lane < 20 ? lane : lane < 40 ? lane - 20 : lane - 40;
      if (w == 0) {
        if (!__all(fb[0] == T && fb[1] == T && fb[2] == T && fb[3] == T)) poll_flags256(ws, WS_FLAGB, T, lane);
        CFENCE();
        const rsrc_t rm = mkrs(ws + WS_MODP, 4 * 3840 * 4);
#pragma unroll
        for (int k = 0; k < 4; ++k) {
          mp[0][k] = __builtin_bit_cast(f32x4, ld16<SC1>(rm, lane < 60 ? (unsigned)((vs0 * DM + 80 * j + vl * 4) * 4) : OOB, k * 3840 * 4));
          mp[1][k] = __builtin_bit_cast(f32x4, ld16<SC1>(rm, lane < 40 ? (unsigned)((vs1 * DM + 80 * j + vl * 4) * 4) : OOB, k * 3840 * 4));
        }
      }
      pairs(3);
      if (w == 0) {
        f32x4 s0 = (mp[0][0] + mp[0][1]) + (mp[0][2] + mp[0][3]), s1 = (mp[1][0] + mp[1][1]) + (mp[1][2] + mp[1][3]);
#pragma unroll
        for (int i = 0; i < 4; ++i) {
          s0[i] = rbf(s0[i]); if (lane < 20) s0[i] = rbf(1.0f + s0[i]);
          s1[i] = rbf(s1[i]); if (lane < 20) s1[i] = rbf(1.0f + s1[i]);
        }
        if (lane < 60) lds_put(lds + L_TAB + ((lane < 20 ? 0 : lane < 40 ? 1 : 2) * 80 + vl * 4) * 4, s0);
        if (lane < 40) lds_put(lds + L_TAB + ((lane < 20 ? 3 : 4) * 80 + vl * 4) * 4, s1);
      }
      pairs(3);
      BAR(); rd(YA, YB); mm(XA, XB); interleave<12, 7, 1>(); SCHED();
      mm(YA, YB);
      STAMP(3); STAMPC(29);
      {
        LAS char* xo = lds + P1_X + w * 6144 + lane * 16;
        LAS char* xi = lds + P1_X + (w ^ 4) * 6144 + lane * 16;
#pragma unroll
        for (int n = 0; n < 5; ++n) lds_put(xo + n * 1024, sel4(kh, acc[0][n], acc[1][n]));
        *(LAS float*)(lds + P1_X + w * 6144 + 5120 + lane * 4) = sel1(kh, rs[0][0], rs[1][0]);
        wait_lgkm0();
        BAR();
        const float rsum = sel1(kh, rs[1][0], rs[0][0]) + *(LAS float*)(lds + P1_X + (w ^ 4) * 6144 + 5120 + lane * 4);
        u32x2 pkd[5];
#pragma unroll
        for (int n = 0; n < 5; ++n) {
          const f32x4 a = sel4(kh, acc[1][n], acc[0][n]) + lds_get(xi + n * 1024);
          const f32x4 c1 = lds_get(lds + L_TAB + (0 * 80 + 16 * n + 4 * q) * 4), sa4 = lds_get(lds + L_TAB + (1 * 80 + 16 * n + 4 * q) * 4);
          f32x4 v = a * c1 + rsum * sa4;
          if (kh) v = -v;
          pkd[n] = (u32x2){pk2(v[0], v[1]), pk2(v[2], v[3])};
        }
        const rsrc_t rpq = mkrs(ws + WS_PQ, 32 * 40 * 4 * 1024);
        auto pq_off = [&](int nf) { const int c = 10 * j + 2 * nf + (q >> 1); return (unsigned)(((g * 40 + kh * 20 + (c >> 2)) * 4 + m) * 1024 + ((c & 3) * 16 + l15) * 16); };
        const int odd = q & 1;
        st16<SC1>(pair_swap(pkd[0], pkd[1]), rpq, pq_off(odd), 0);
        st16<SC1>(pair_swap(pkd[2], pkd[3]), rpq, pq_off(2 + odd), 0);
        st8<SC1>(pkd[4], rpq, pq_off(4) + odd * 8, 0);
      }
      BAR();
      publish(lds, 3, 8, ws, WS_FLAG1 + (g * 8 + j) * 128, T, lane);
      STAMP(4);
    }

    {
      LANE_IDS();
      constexpr int NT = 20;
      const int nb = kh ? 3 : 0;
      group_wait(lds, 8, ws, WS_FLAG1 + g * 1024, 8, T, w == 0, lane);
      STAMP(5);
      const rsrc_t ra = mkrs(ws + WS_PQ + (size_t)g * 40 * 4 * 1024, 40 * 4 * 1024);
      u32x4 A[NT];
#pragma unroll
      for (int t = 0; t < AW; ++t) A[t] = ld16<SC1>(ra, lane * 16, ((2 * t + kh) * 4 + m) * 1024);
      f32x4 acc[5];
#pragma unroll
      for (int n = 0; n < 5; ++n) acc[n] = (f32x4){0.f, 0.f, 0.f, 0.f};
      const unsigned boff = l15 * 128 + (((kh * 4 + q) ^ (l15 >> 1)) << 4);
      bf16x8 B[2][5];
      auto rd = [&](bf16x8(&Bv)[5], int slot) {
#pragma unroll
        for (int n = 0; n < 5; ++n) Bv[n] = ldsfrag(lds + slot * 10240 + boff + n * 2048);
      };
      BAR();
      STAMP(6);
      rd(B[0], 0);
#pragma unroll
      for (int t = 0; t < NT; ++t) {
        if (t + 1 < NT) { BAR(); rd(B[(t + 1) & 1], (t + 1) % P2_RING); }
        if (t + AW < NT) A[t + AW] = ld16<SC1>(ra, lane * 16, ((2 * (t + AW) + kh) * 4 + m) * 1024);
#pragma unroll
        for (int n = 0; n < 5; ++n) acc[n] = mfma(B[t & 1][n], asfrag(A[t]), acc[n]);
        interleave<5, 5, 1>(); SCHED();
      }
      STAMP(7);
      LANE_IDS2();
      const rsrc_t rx = mkrs(p.x, SEQ * DM * 2);
      u32x2 xin[3];
#pragma unroll
      for (int n = 0; n < 3; ++n) xin[n] = ld8<0>(rx, (kh && n == 2) ? OOB : (unsigned)((64 * g + 16 * m + l15b) * (DM * 2) + (80 * j + 16 * (nb + n) + 4 * qb) * 2), 0);
      LAS char* xo = lds + P2_X + w * 3072 + lane * 16;
      LAS char* xi = lds + P2_X + (w ^ 4) * 3072 + lane * 16;
      if (kh) { lds_put(xo, acc[0]); lds_put(xo + 1024, acc[1]); lds_put(xo + 2048, acc[2]); }
      else { lds_put(xo, acc[3]); lds_put(xo + 1024, acc[4]); }
      wait_lgkm0();
      BAR();
      float s = 0.f, sq = 0.f;
      u32x2 cp[3]; unsigned x1p[6];
      auto fin = [&](int n, f32x4 own) {
        const f32x4 y = own + lds_get(xi + n * 1024);
        const int col = 16 * (nb + n) + 4 * q;
        const f32x4 ga = lds_get(lds + L_TAB + (2 * 80 + col) * 4), cb1 = lds_get(lds + L_TAB + (3 * 80 + col) * 4);
        const f32x4 xv = {bflo(xin[n].x), bfhi(xin[n].x), bflo(xin[n].y), bfhi(xin[n].y)};
        const f32x4 x1 = xv + ga * y, c = x1 * cb1;
        x1p[2 * n] = pk2(x1[0], x1[1]); x1p[2 * n + 1] = pk2(x1[2], x1[3]);
        cp[n] = (u32x2){pk2(c[0], c[1]), pk2(c[2], c[3])};
        s += (x1[0] + x1[1]) + (x1[2] + x1[3]);
        sq = __builtin_fmaf(x1[0], x1[0], __builtin_fmaf(x1[1], x1[1], __builtin_fmaf(x1[2], x1[2], __builtin_fmaf(x1[3], x1[3], sq))));
      };
      if (kh) { fin(0, acc[3]); fin(1, acc[4]); cp[2] = (u32x2){0u, 0u}; x1p[4] = 0u; x1p[5] = 0u; }
      else { fin(0, acc[0]); fin(1, acc[1]); fin(2, acc[2]); }
      s = xq_sum(s); sq = xq_sum(sq);
      const rsrc_t rc = mkrs(ws + WS_X1C, 32 * 20 * 4 * 1024);
      auto c_off = [&](int nf) { const int c = 10 * j + 2 * nf + (q >> 1); return (unsigned)(((g * 20 + (c >> 2)) * 4 + m) * 1024 + ((c & 3) * 16 + l15) * 16); };
      const int odd = q & 1;
      st16<SC1>(pair_swap(cp[0], cp[1]), rc, c_off(nb + odd), 0);
      st8<SC1>(cp[2], rc, kh ? OOB : c_off(2) + odd * 8, 0);
      const rsrc_t rs1 = mkrs(ws + WS_ST1, SEQ * 16 * 8);
      st8<SC1>((u32x2){__float_as_uint(s), __float_as_uint(sq)}, rs1, q == 0 ? (unsigned)(((64 * g + 16 * m + l15) * 16 + 2 * j + kh) * 8) : OOB, 0);
#pragma unroll
      for (int n = 0; n < 3; ++n) *(LAS u32x2*)(lds + L_X1 + (w * 3 + n) * 512 + lane * 8) = (u32x2){x1p[2 * n], x1p[2 * n + 1]};
      wait_lgkm0();
      BAR();
      publish(lds, 4, 8, ws, WS_FLAG2 + (g * 8 + j) * 128, T, lane);
      STAMP(8);
    }

    {
      LANE_IDS();
      constexpr int NT = 10;
      const int nb = kh ? 4 : 0, nn = kh ? 3 : 4;
      {
        const rsrc_t ruv = mkrs(ws + WS_UV, 2 * 896 * 16);
        const int col = w * 28 + lane, part = col >= 112 ? 1 : 0, cc = col - 112 * part;
        const unsigned off = lane < 28 ? (unsigned)((part * 896 + 112 * j + cc) * 16) : OOB;
        u32x4 gv;
        for (unsigned spins = 0;; ++spins) {
          gv = ld16<SC1>(ruv, off, 0);
          const bool ok = lane >= 28 || (gv.z == (unsigned)T && gv.w == (unsigned)(T >> 32));
          if (__all(ok)) break;
          if (spins > SPIN_MAX) { note_timeout(ws, 0x300, lane); break; }
          __builtin_amdgcn_s_sleep(1); CFENCE();
        }
        if (lane < 28) { *(LAS unsigned*)(lds + L_UV + col * 4) = gv.x; *(LAS unsigned*)(lds + L_UV + (224 + col) * 4) = gv.y; }
      }
      group_wait(lds, 9, ws, WS_FLAG2 + g * 1024, 8, T, w == 0, lane);
      STAMP(9);
      const rsrc_t ra = mkrs(ws + WS_X1C + (size_t)g * 20 * 4 * 1024, 20 * 4 * 1024);
      u32x4 A[NT];
#pragma unroll
      for (int t = 0; t < AW; ++t) A[t] = ld16<SC1>(ra, lane * 16, ((2 * t + kh) * 4 + m) * 1024);
      const rsrc_t rs1 = mkrs(ws + WS_ST1, SEQ * 16 * 8);
      const unsigned s1o = (unsigned)(((64 * g + 16 * m + l15) * 16 + 4 * q) * 8);
      const u32x4 st0 = ld16<SC1>(rs1, s1o, 0), st1 = ld16<SC1>(rs1, s1o + 16, 0);
      f32x4 acc[7];
      bf16x8 B[2][7];
      auto rd = [&](bf16x8(&Bv)[7], int slot) {
#pragma unroll
        for (int n = 0; n < 7; ++n) Bv[n] = ldsfrag(lds + slot * 14336 + kh * 7168 + n * 1024 + lane * 16);
      };
      wait_lgkm0();
      BAR();
      STAMP(10);
      rd(B[0], 0); wait_lgkm0();
      float mean1, rstd1;
      {
        const float sm = xq_sum((__uint_as_float(st0.x) + __uint_as_float(st0.z)) + (__uint_as_float(st1.x) + __uint_as_float(st1.z)));
        const float sq = xq_sum((__uint_as_float(st0.y) + __uint_as_float(st0.w)) + (__uint_as_float(st1.y) + __uint_as_float(st1.w)));
        const f32x2 ls = ln_stats(sm, sq, 1.0f / DM);
        mean1 = ls.x; rstd1 = ls.y;
      }
      LAS char* xo = lds + P3_X + (kh ? 12288 + m * 4096 : m * 3072) + lane * 16;
      LAS char* xi = lds + P3_X + (kh ? m * 3072 : 12288 + m * 4096) + lane * 16;
#pragma unroll
      for (int n = 0; n < 7; ++n) acc[n] = (f32x4){0.f, 0.f, 0.f, 0.f};
#pragma unroll
      for (int t = 0; t < NT; ++t) {
        BAR(); if (t + 1 < NT) rd(B[(t + 1) & 1], (t + 1) % P3_RING);
        if (t + AW < NT) A[t + AW] = ld16<SC1>(ra, lane * 16, ((2 * (t + AW) + kh) * 4 + m) * 1024);
#pragma unroll
        for (int n = 0; n < 7; ++n) acc[n] = mfma(asfrag(A[t]), B[t & 1][n], acc[n]);
        interleave<7, 7, 1>(); SCHED();
      }
      STAMP(25);
      if (kh) {
#pragma unroll
        for (int n = 0; n < 4; ++n) lds_put(xo + n * 1024, acc[n]);
      } else {
#pragma unroll
        for (int n = 0; n < 3; ++n) lds_put(xo + n * 1024, acc[4 + n]);
      }
      wait_lgkm0();
      BAR();
      u32x2 z2p[4];
      {
        float rr[4], c1[4];
#pragma unroll
        for (int r = 0; r < 4; ++r) { rr[r] = __shfl(rstd1, 4 * q + r); c1[r] = __shfl(-rstd1 * mean1, 4 * q + r); }
        float zs[4] = {0.f, 0.f, 0.f, 0.f}, zq[4] = {0.f, 0.f, 0.f, 0.f};
        auto fin = [&](int n, f32x4 own) {
          const f32x4 a = own + lds_get(xi + n * 1024);
          const int col = 16 * (nb + n) + l15;
          const float u = *(LAS float*)(lds + L_UV + col * 4), v = *(LAS float*)(lds + L_UV + (224 + col) * 4);
          float z[4];
#pragma unroll
          for (int r = 0; r < 4; ++r) {
            z[r] = gelu_tanh(__builtin_fmaf(rr[r], a[r], __builtin_fmaf(c1[r], u, v)));
            zs[r] += z[r]; zq[r] = __builtin_fmaf(z[r], z[r], zq[r]);
          }
          z2p[n] = (u32x2){pk2(z[0], z[1]), pk2(z[2], z[3])};
        };
        if (kh) { fin(0, acc[4]); fin(1, acc[5]); fin(2, acc[6]); z2p[3] = (u32x2){0u, 0u}; }
        else { fin(0, acc[0]); fin(1, acc[1]); fin(2, acc[2]); fin(3, acc[3]); }
#pragma unroll
        for (int r = 0; r < 4; ++r) { zs[r] = row16_sum(zs[r]); zq[r] = row16_sum(zq[r]); }
        const float ms = l15 == 0 ? zs[0] : l15 == 1 ? zs[1] : l15 == 2 ? zs[2] : zs[3];
        const float Ms = l15 == 0 ? zq[0] : l15 == 1 ? zq[1] : l15 == 2 ? zq[2] : zq[3];
        const rsrc_t rs2 = mkrs(ws + WS_ST2, SEQ * 16 * 16);
        st16<SC1>((u32x4){__float_as_uint(ms), __float_as_uint(Ms), (unsigned)T, (unsigned)(T >> 32)}, rs2,
                  l15 < 4 ? (unsigned)(((64 * g + 16 * m + 4 * q + l15) * 16 + 2 * j + kh) * 16) : OOB, 0);
      }
      STAMP(11);
      rd(B[0], NT % P3_RING); wait_lgkm0();
#pragma unroll
      for (int n = 0; n < 7; ++n) acc[n] = (f32x4){0.f, 0.f, 0.f, 0.f};
#pragma unroll
      for (int t = 0; t < NT; ++t) {
        if (t + 1 < NT) { BAR(); rd(B[(t + 1) & 1], (NT + t + 1) % P3_RING); }
#pragma unroll
        for (int n = 0; n < 7; ++n) acc[n] = mfma(B[t & 1][n], asfrag(A[t]), acc[n]);
        interleave<7, 7, 1>(); SCHED();
      }
      STAMP(12);
      const rsrc_t rs2 = mkrs(ws + WS_ST2, SEQ * 16 * 16);
      const unsigned soff = (unsigned)(((64 * g + 16 * m + l15) * 16 + 4 * q) * 16);
      u32x4 gs[4];
#pragma unroll
      for (int i = 0; i < 4; ++i) gs[i] = ld16<SC1>(rs2, soff + 16 * i, 0);
      if (kh) {
#pragma unroll
        for (int n = 0; n < 4; ++n) lds_put(xo + n * 1024, acc[n]);
      } else {
#pragma unroll
        for (int n = 0; n < 3; ++n) lds_put(xo + n * 1024, acc[4 + n]);
      }
      wait_lgkm0();
      BAR();
      {
        const float c1 = -rstd1 * mean1;
        auto fin = [&](int n, f32x4 own) {
          const f32x4 a = own + lds_get(xi + n * 1024);
          const int col = 112 + 16 * (nb + n) + 4 * q;
          const f32x4 u = lds_get(lds + L_UV + col * 4), v = lds_get(lds + L_UV + (224 + col) * 4);
          const f32x4 pre = a * rstd1 + (u * c1 + v);
          z1p[2 * n] = pk2(gelu_tanh(pre[0]), gelu_tanh(pre[1])); z1p[2 * n + 1] = pk2(gelu_tanh(pre[2]), gelu_tanh(pre[3]));
        };
        if (kh) { fin(0, acc[4]); fin(1, acc[5]); fin(2, acc[6]); z1p[6] = 0u; z1p[7] = 0u; }
        else { fin(0, acc[0]); fin(1, acc[1]); fin(2, acc[2]); fin(3, acc[3]); }
      }
#pragma unroll
      for (int i = 0; i < 8; ++i) asm volatile("" : "+v"(z1p[i]));
      STAMP(26);
      for (unsigned spins = 0;; ++spins) {
        bool ok = true;
#pragma unroll
        for (int i = 0; i < 4; ++i) ok &= gs[i].z == (unsigned)T && gs[i].w == (unsigned)(T >> 32);
        if (__all(ok)) break;
        if (spins > SPIN_MAX) { note_timeout(ws, 0x400, lane); break; }
        __builtin_amdgcn_s_sleep(1); CFENCE();
#pragma unroll
        for (int i = 0; i < 4; ++i) gs[i] = ld16<SC1>(rs2, soff + 16 * i, 0);
      }
      {
        const float sm = xq_sum((__uint_as_float(gs[0].x) + __uint_as_float(gs[1].x)) + (__uint_as_float(gs[2].x) + __uint_as_float(gs[3].x)));
        const float sq = xq_sum((__uint_as_float(gs[0].y) + __uint_as_float(gs[1].y)) + (__uint_as_float(gs[2].y) + __uint_as_float(gs[3].y)));
        const f32x2 ls = ln_stats(sm, sq, 1.0f / FH);
        float rr[4], c2[4];
#pragma unroll
        for (int r = 0; r < 4; ++r) { rr[r] = __shfl(ls.y, 4 * q + r); c2[r] = __shfl(-ls.x * ls.y, 4 * q + r); }
        u32x2 zn[4];
#pragma unroll
        for (int n = 0; n < 4; ++n)
          zn[n] = (u32x2){pk2(__builtin_fmaf(bflo(z2p[n].x), rr[0], c2[0]), __builtin_fmaf(bfhi(z2p[n].x), rr[1], c2[1])),
                          pk2(__builtin_fmaf(bflo(z2p[n].y), rr[2], c2[2]), __builtin_fmaf(bfhi(z2p[n].y), rr[3], c2[3]))};
        const rsrc_t rz2 = mkrs(ws + WS_Z2T, 8 * 32 * 14336);
        auto z_off = [&](int nf) { return (unsigned)(((j * 32 + g) * 14 + (m >> 1) * 7 + nf) * 1024 + ((2 * (m & 1) + (q >> 1)) * 16 + l15) * 16); };
        const int odd = q & 1;
        st16<SC1>(pair_swap(zn[0], zn[1]), rz2, z_off(nb + odd), 0);
        if (kh) st8<SC1>(zn[2], rz2, z_off(6) + odd * 8, 0);
        else st16<SC1>(pair_swap(zn[2], zn[3]), rz2, z_off(2 + odd), 0);
      }
      STAMP(27);
      BAR();
      publish(lds, 5, 8, ws, WS_FLAG3 + (j * 32 + g) * 128, T, lane);
      STAMP(13);
    }

    {
      LANE_IDS();
      const int mh = m >> 1, k4 = m & 1, nh = kh;
      f32x4 acc[2][4];
#pragma unroll
      for (int a = 0; a < 2; ++a)
#pragma unroll
        for (int n = 0; n < 4; ++n) acc[a][n] = (f32x4){0.f, 0.f, 0.f, 0.f};
      const unsigned aoff = (mh * 32 + l15) * 128 + (((k4 * 4 + q) ^ (l15 >> 1)) << 4);
      const unsigned boff = P4_DOFF + k4 * 7168 + nh * 4096 + lane * 16;
      const rsrc_t rb = mkrs(p.bsp, SEQ * 2);
      const float bs = bflo(ld2<0>(rb, (64 * g + 16 * m + l15) * 2, 0));
      bf16x8 XA[2], XB[4], YA[2], YB[4];
      int sa = 0, sb = 0;
      if (nh == 0) {
        auto rd = [&](bf16x8(&A)[2], bf16x8(&B)[4]) {
          LAS char* pa = lds + sa * 8192 + aoff;
          LAS char* pb = lds + sb * 14336 + boff;
          A[0] = ldsfrag(pa); A[1] = ldsfrag(pa + 2048);
#pragma unroll
          for (int n = 0; n < 4; ++n) B[n] = ldsfrag(pb + n * 1024);
          sa = (sa + 1 == P4_SA) ? 0 : sa + 1; sb = (sb + 1 == P4_DB) ? 0 : sb + 1;
        };
        auto mm = [&](const bf16x8(&A)[2], const bf16x8(&B)[4]) {
#pragma unroll
          for (int a = 0; a < 2; ++a)
#pragma unroll
            for (int n = 0; n < 4; ++n) acc[a][n] = mfma(B[n], A[a], acc[a][n]);
        };
        BAR();
        STAMP(14);
        rd(XA, XB);
#pragma unroll 1
        for (int i = 0; i < 15; ++i) {
          BAR(); rd(YA, YB); mm(XA, XB); interleave<8, 6, 1>(); SCHED();
          BAR(); rd(XA, XB); mm(YA, YB); interleave<8, 6, 1>(); SCHED();
        }
        BAR(); rd(YA, YB); mm(XA, XB); interleave<8, 6, 1>(); SCHED();
        mm(YA, YB);
      } else {
        auto rd = [&](bf16x8(&A)[2], bf16x8(&B)[4]) {
          LAS char* pa = lds + sa * 8192 + aoff;
          LAS char* pb = lds + sb * 14336 + boff;
          A[0] = ldsfrag(pa); A[1] = ldsfrag(pa + 2048);
#pragma unroll
          for (int n = 0; n < 3; ++n) B[n] = ldsfrag(pb + n * 1024);
          sa = (sa + 1 == P4_SA) ? 0 : sa + 1; sb = (sb + 1 == P4_DB) ? 0 : sb + 1;
        };
        auto mm = [&](const bf16x8(&A)[2], const bf16x8(&B)[4]) {
#pragma unroll
          for (int a = 0; a < 2; ++a)
#pragma unroll
            for (int n = 0; n < 3; ++n) acc[a][n] = mfma(B[n], A[a], acc[a][n]);
        };
        BAR();
        STAMP(14);
        rd(XA, XB);
#pragma unroll 1
        for (int i = 0; i < 15; ++i) {
          BAR(); rd(YA, YB); mm(XA, XB); interleave<6, 5, 1>(); SCHED();
          BAR(); rd(XA, XB); mm(YA, YB); interleave<6, 5, 1>(); SCHED();
        }
        BAR(); rd(YA, YB); mm(XA, XB); interleave<6, 5, 1>(); SCHED();
        mm(YA, YB);
      }
      STAMP(15);
      LAS char* xo = lds + P4_X + w * 4096 + lane * 16;
      LAS char* xi = lds + P4_X + (w ^ 1) * 4096 + lane * 16;
#pragma unroll
      for (int n = 0; n < 4; ++n) lds_put(xo + n * 1024, sel4(k4, acc[0][n], acc[1][n]));
      wait_lgkm0();
      BAR();
      u32x2 up[4];
#pragma unroll
      for (int n = 0; n < 4; ++n) {
        const f32x4 gv = sel4(k4, acc[1][n], acc[0][n]) + lds_get(xi + n * 1024) + bs;
        up[n] = (u32x2){pk2(bflo(z1p[2 * n]) * gv[0], bfhi(z1p[2 * n]) * gv[1]), pk2(bflo(z1p[2 * n + 1]) * gv[2], bfhi(z1p[2 * n + 1]) * gv[3])};
      }
      const rsrc_t ru = mkrs(ws + WS_U, 32 * 28 * 4 * 1024);
      auto u_off = [&](int nf) { const int c = 14 * j + 2 * nf + (q >> 1); return (unsigned)(((g * 28 + (c >> 2)) * 4 + m) * 1024 + ((c & 3) * 16 + l15) * 16); };
      const int odd = q & 1;
      st16<SC1>(pair_swap(up[0], up[1]), ru, u_off(4 * nh + odd), 0);
      if (nh) st8<SC1>(up[2], ru, u_off(6) + odd * 8, 0);
      else st16<SC1>(pair_swap(up[2], up[3]), ru, u_off(2 + odd), 0);
      STAMP(24);
      BAR();
      publish(lds, 6, 8, ws, WS_FLAG4 + (g * 8 + j) * 128, T, lane);
      if (b == 0 && w == 0 && lane == 0) __hip_atomic_store((GAS u64*)(ws + WS_EPOCH), T, __ATOMIC_RELAXED, __HIP_MEMORY_SCOPE_AGENT);
      STAMP(16);
    }

    {
      LANE_IDS();
      constexpr int NT = 14;
      const int nb = kh ? 3 : 0;
      group_wait(lds, 10, ws, WS_FLAG4 + g * 1024, 8, T, w == 0, lane);
      STAMP(17);
      const rsrc_t ra = mkrs(ws + WS_U + (size_t)g * 28 * 4 * 1024, 28 * 4 * 1024);
      u32x4 A[NT];
#pragma unroll
      for (int t = 0; t < AW; ++t) A[t] = ld16<SC1>(ra, lane * 16, ((2 * t + kh) * 4 + m) * 1024);
      f32x4 acc[5];
#pragma unroll
      for (int n = 0; n < 5; ++n) acc[n] = (f32x4){0.f, 0.f, 0.f, 0.f};
      bf16x8 B[2][5];
      auto rd = [&](bf16x8(&Bv)[5], int slot) {
#pragma unroll
        for (int n = 0; n < 5; ++n) Bv[n] = ldsfrag(lds + slot * 10240 + kh * 5120 + n * 1024 + lane * 16);
      };
      BAR();
      STAMP(18);
      rd(B[0], 0);
#pragma unroll
      for (int t = 0; t < NT; ++t) {
        if (t + 1 < NT) { BAR(); rd(B[(t + 1) & 1], t + 1); }
        if (t + AW < NT) A[t + AW] = ld16<SC1>(ra, lane * 16, ((2 * (t + AW) + kh) * 4 + m) * 1024);
#pragma unroll
        for (int n = 0; n < 5; ++n) acc[n] = mfma(B[t & 1][n], asfrag(A[t]), acc[n]);
        interleave<5, 5, 1>(); SCHED();
      }
      STAMP(19);
      LAS char* xo = lds + P5_X + w * 3072 + lane * 16;
      LAS char* xi = lds + P5_X + (w ^ 4) * 3072 + lane * 16;
      if (kh) { lds_put(xo, acc[0]); lds_put(xo + 1024, acc[1]); lds_put(xo + 2048, acc[2]); }
      else { lds_put(xo, acc[3]); lds_put(xo + 1024, acc[4]); }
      wait_lgkm0();
      BAR();
      u32x2 op[3];
#pragma unroll
      for (int n = 0; n < 3; ++n) {
        const u32x2 x1v = *(LAS u32x2*)(lds + L_X1 + (w * 3 + n) * 512 + lane * 8);
        const unsigned x1p[6] = {x1v.x, x1v.y, x1v.x, x1v.y, x1v.x, x1v.y};
        const f32x4 o = sel4(kh, acc[n == 2 ? 4 : 3 + n], acc[n]) + lds_get(xi + n * 1024);
        const int col = (kh && n == 2) ? 0 : 16 * (nb + n) + 4 * q;
        const f32x4 gb = lds_get(lds + L_TAB + (4 * 80 + col) * 4);
        op[n] = (u32x2){pk2(__builtin_fmaf(gb[0], o[0], bflo(x1p[2 * n])), __builtin_fmaf(gb[1], o[1], bfhi(x1p[2 * n]))),
                        pk2(__builtin_fmaf(gb[2], o[2], bflo(x1p[2 * n + 1])), __builtin_fmaf(gb[3], o[3], bfhi(x1p[2 * n + 1])))};
      }
      const rsrc_t ro = mkrs(p.out, SEQ * DM * 2);
      const unsigned rowb = (unsigned)((64 * g + 16 * m + l15) * (DM * 2) + 80 * j * 2);
      const int odd = q & 1;
      st16<SC1>(pair_swap(op[0], op[1]), ro, rowb + (16 * (nb + odd) + 8 * (q >> 1)) * 2, 0);
      st8<SC1>(op[2], ro, kh ? OOB : rowb + (32 + 4 * q) * 2, 0);
      STAMP(20); STAMPC(31);
    }
  } else if (wid < 10) {
    LANE_IDS();
    const int h = wid - 8;
    const unsigned sw0 = (unsigned)(((lane & 7) ^ (lane >> 4)) * 16), sw1 = sw0 ^ 64;
    wait_lgkm0();
    BAR();
    {
      const rsrc_t r = mkrs((h ? p.wsim : p.wsre) + (size_t)g * 64 * SEQ, 64 * SEQ * 2);
      const unsigned v0 = (lane >> 3) * 4096 + sw0, v1 = (lane >> 3) * 4096 + sw1;
      auto issue = [&](int t, int slot) {
        const unsigned k = (unsigned)((t + rot) & 31) * 128;
#pragma unroll
        for (int pc = 0; pc < 8; ++pc) dma16<0>(r, lds + slot * 16384 + (h * 8 + pc) * 1024, (pc & 1) ? v1 : v0, k + pc * 32768);
      };
      auto touch = [&](int it) {
        __builtin_amdgcn_raw_ptr_buffer_load_lds(r, (LAS void*)(lds + L_SINK), 4, (int)((j * 8 + (lane >> 3)) * 4096 + ((it * 8 + (lane & 7) + rot) & 31) * 128), 0, 0, 0);
      };
      lds_wait(lds, 15); lds_wait(lds, 16);
      if (h == 1) {
        const int cg = b % 60, kq = b / 60;
        const rsrc_t rw = mkrs(p.wmod, b < 240 ? DM * 3840 * 2 : 0), rc = mkrs(p.c, DM * 2);
        u32x4 wv[20]; unsigned cv[20];
#pragma unroll
        for (int it = 0; it < 20; ++it) {
          const int row = (kq & 3) * 160 + it * 8 + (lane >> 3);
          wv[it] = ld16<0>(rw, (unsigned)(row * 7680 + cg * 128 + (lane & 7) * 16), 0);
          cv[it] = ld2<0>(rc, row * 2, 0);
        }
        touch(0); if (T0N > 1) touch(1);
        float a8[8] = {0.f, 0.f, 0.f, 0.f, 0.f, 0.f, 0.f, 0.f};
#pragma unroll
        for (int it = 0; it < 20; ++it) {
          const float cf = bflo(cv[it]);
          const float sl = rbf(cf * __builtin_amdgcn_rcpf(1.0f + __builtin_amdgcn_exp2f(-1.4426950408889634f * cf)));
#pragma unroll
          for (int i = 0; i < 4; ++i) { a8[2 * i] += sl * bflo(wv[it][i]); a8[2 * i + 1] += sl * bfhi(wv[it][i]); }
        }
#pragma unroll
        for (int i = 0; i < 8; ++i) { a8[i] += __shfl_xor(a8[i], 8); a8[i] = xq_sum(a8[i]); }
        const rsrc_t rm = mkrs(ws + WS_MODP, 4 * 3840 * 4);
        const unsigned mo = (unsigned)((kq * 3840 + cg * 64 + (lane & 7) * 8) * 4);
        const bool on = lane < 8 && b < 240;
        st16<SC1>((u32x4){__float_as_uint(a8[0]), __float_as_uint(a8[1]), __float_as_uint(a8[2]), __float_as_uint(a8[3])}, rm, on ? mo : OOB, 0);
        st16<SC1>((u32x4){__float_as_uint(a8[4]), __float_as_uint(a8[5]), __float_as_uint(a8[6]), __float_as_uint(a8[7])}, rm, on ? mo + 16 : OOB, 0);
      } else {
        touch(0); if (T0N > 1) touch(1);
      }
      lds_wait(lds, 11);
      loader_fill<0, 2, P1_SA>(issue);
      if (T0N < 2) touch(1);
      STAMP(1);
      if (h == 1) {
        if (T0N < 2) wait_vm<9>(); else wait_vm<8>();
        if (arrive_last(lds, 7, 3, lane)) set_flag(ws, WS_FLAGB + g * 128 + j * 8, epoch_tag(Ein), lane);
      }
      loader_run<32, P1_SA, 8, 2, 2, -1>(issue, [&](int i) { if (i == 4) touch(2); if (i == 12) touch(3); });
      BAR(); BAR();
    }
    {
      const unsigned v0 = (lane >> 3) * 1280 + (h ? sw1 : sw0), v1 = (lane >> 3) * 1280 + (h ? sw0 : sw1);
      auto issue = [&](int t, int slot) {
        const int im = t >= 10;
        const rsrc_t r = mkrs((im ? p.wdim : p.wdre) + (size_t)j * 80 * DM, 80 * DM * 2);
        const unsigned k = (unsigned)(t - 10 * im) * 128 + h * 5 * 8 * 1280;
#pragma unroll
        for (int pc = 0; pc < 5; ++pc) dma16<0>(r, lds + slot * 10240 + (h * 5 + pc) * 1024, (pc & 1) ? v1 : v0, k + pc * 8 * 1280);
      };
      loader_fill<0, PF2, P2_RING>(issue);
      STAMP(2);
      loader_run<20, P2_RING, 5, PF2, 2, -1>(issue);
      BAR(); BAR();
    }
    {
      const rsrc_t r = mkrs(ws + WS_WINR + (size_t)j * 20 * 14336, 20 * 14336);
      auto issue = [&](int t, int slot) {
#pragma unroll
        for (int pc = 0; pc < 7; ++pc) dma16<SC1>(r, lds + slot * 14336 + (h * 7 + pc) * 1024, lane * 16, (unsigned)(t * 14336 + (h * 7 + pc) * 1024));
      };
      loader_fill<0, PF3, P3_RING>(issue);
      STAMP(3);
      loader_run<20, P3_RING, 7, PF3, 2, 9>(issue);
      BAR(); BAR();
    }
    {
      const rsrc_t r = mkrs(p.wsp + (size_t)g * 64 * SEQ, 64 * SEQ * 2);
      const unsigned v0 = (lane >> 3) * 4096 + sw0, v1 = (lane >> 3) * 4096 + sw1;
      auto issue = [&](int t, int slot) {
        const unsigned k = (unsigned)((t + rot) & 31) * 128 + h * 4 * 32768;
#pragma unroll
        for (int pc = 0; pc < 4; ++pc) dma16<0>(r, lds + slot * 8192 + (h * 4 + pc) * 1024, (pc & 1) ? v1 : v0, k + pc * 32768);
      };
      loader_fill<0, PF4, P4_SA>(issue);
      STAMP(4);
      loader_run<32, P4_SA, 4, PF4, 2, -1>(issue);
      BAR(); BAR();
    }
    {
      const rsrc_t r = mkrs(ws + WS_WOUTR + (size_t)j * 14 * 10240, 14 * 10240);
      auto issue = [&](int t, int slot) {
#pragma unroll
        for (int pc = 0; pc < 5; ++pc) dma16<SC1>(r, lds + slot * 10240 + (h * 5 + pc) * 1024, lane * 16, (unsigned)(t * 10240 + (h * 5 + pc) * 1024));
      };
      loader_fill<0, PF5, P5_RING>(issue);
      STAMP(5);
      loader_run<14, P5_RING, 5, PF5, 2, -1>(issue);
      BAR();
    }
  } else {
    LANE_IDS();
    const int h = wid - 10;
    wait_lgkm0();
    BAR();
    {
      LAS char* sc = lds + L_RT + h * 10240;
      const int u = b * 2 + h;
      const int qq = l15 >> 2, pp = l15 & 3;
      if (u < 320) {
        const int jp = u / 20, kf = u - 20 * jp, jj = jp >> 1, part = jp & 1;
        const rsrc_t r = mkrs(p.win, DM * FF * 2);
#pragma unroll
        for (int pc = 0; pc < 7; ++pc) {
          const int idx = pc * 64 + lane, row = idx / 14, ch = idx - 14 * row;
          const bool ok = 112 * jj + 8 * ch < FH;
          dma16<0>(r, sc + pc * 1024, ok ? (unsigned)((32 * kf + row) * (FF * 2) + (part ? 0 : FH * 2) + 224 * jj + ch * 16) : OOB, 0);
        }
      } else if (u < 432) {
        const int uu = u - 320, jj = uu / 14, kp = uu - 14 * jj;
        const rsrc_t r = mkrs(p.wout, FH * DM * 2);
#pragma unroll
        for (int pc = 0; pc < 10; ++pc) {
          const int idx = pc * 64 + lane, row = idx / 10, ch = idx - 10 * row;
          dma16<0>(r, sc + pc * 1024, (unsigned)((64 * kp + row) * (DM * 2) + 160 * jj + ch * 16), 0);
        }
      }
      lds_signal(lds, 15 + h, lane);
      wait_vm<0>();
      CFENCE();
      if (u < 320) {
        const int jp = u / 20, kf = u - 20 * jp, jj = jp >> 1, part = jp & 1;
        const rsrc_t ro = mkrs(ws + WS_WINR, 8 * 20 * 14336);
#pragma unroll
        for (int nf = 0; nf < 7; ++nf) {
          LAS char* a0 = sc + (8 * q + qq) * 224 + (16 * nf + 4 * pp) * 2;
          const u32x2 lo = __builtin_bit_cast(u32x2, __builtin_amdgcn_ds_read_tr16_b64_v4i16((LAS s16x4*)a0));
          const u32x2 hi = __builtin_bit_cast(u32x2, __builtin_amdgcn_ds_read_tr16_b64_v4i16((LAS s16x4*)(a0 + 4 * 224)));
          st16<SC1>((u32x4){lo.x, lo.y, hi.x, hi.y}, ro, (unsigned)((((jj * 2 + part) * 20 + kf) * 7 + nf) * 1024 + lane * 16), 0);
        }
      } else if (u < 432) {
        const int uu = u - 320, jj = uu / 14, kp = uu - 14 * jj;
        const rsrc_t ro = mkrs(ws + WS_WOUTR, 8 * 14 * 10240);
#pragma unroll
        for (int f = 0; f < 10; ++f) {
          const int kk = f / 5, nf = f - 5 * kk;
          LAS char* a0 = sc + (32 * kk + 8 * q + qq) * 160 + (16 * nf + 4 * pp) * 2;
          const u32x2 lo = __builtin_bit_cast(u32x2, __builtin_amdgcn_ds_read_tr16_b64_v4i16((LAS s16x4*)a0));
          const u32x2 hi = __builtin_bit_cast(u32x2, __builtin_amdgcn_ds_read_tr16_b64_v4i16((LAS s16x4*)(a0 + 4 * 160)));
          st16<SC1>((u32x4){lo.x, lo.y, hi.x, hi.y}, ro, (unsigned)(((jj * 14 + kp) * 10 + f) * 1024 + lane * 16), 0);
        }
      }
      wait_lgkm0(); CFENCE();
    }
    const u64 T = epoch_tag(Ein);
    STAMP(1);
    {
      if (h == 0) {
        lds_wait(lds, 14);
        __builtin_amdgcn_s_sleep(F0DELAY);
        poll_flags256(ws, WS_FLAG0 + xg * 4096, T, lane);
        lds_signal(lds, 12, lane);
      } else {
        lds_wait(lds, 12);
      }
      STAMP(2);
      const rsrc_t r = mkrs(ws + WS_XNT + (size_t)j * 32 * 10240, 32 * 10240);
      auto issue = [&](int t, int slot) {
        const unsigned k = (unsigned)((t + rot) & 31) * 10240;
#pragma unroll
        for (int pc = 0; pc < 5; ++pc) dma16<SC1>(r, lds + P1_DOFF + slot * 10240 + (h * 5 + pc) * 1024, lane * 16, k + (h * 5 + pc) * 1024);
      };
      loader_fill<0, 1, P1_DB>(issue);
      if (h == 0) lds_signal(lds, 11, lane);
      loader_fill<1, 2, P1_DB>(issue);
      wait_vm<5>();
      if (arrive_last(lds, 7, 3, lane)) set_flag(ws, WS_FLAGB + g * 128 + j * 8, T, lane);
      loader_run<32, P1_DB, 5, 2, 2, -1>(issue, [&](int i) {
        if (i == 20 && h == 1) {
          const int bi = g * 8 + j;
          const rsrc_t r3 = mkrs(p.wdre, DM * DM * 2), r4 = mkrs(p.wdim, DM * DM * 2);
          __builtin_amdgcn_raw_ptr_buffer_load_lds(r3, (LAS void*)(lds + L_SINK), 4, lane < 25 ? (int)((bi * 25 + lane) * 128) : (int)OOB, 0, 0, 0);
          __builtin_amdgcn_raw_ptr_buffer_load_lds(r4, (LAS void*)(lds + L_SINK), 4, lane < 25 ? (int)((bi * 25 + lane) * 128) : (int)OOB, 0, 0, 0);
        }
      });
      BAR(); BAR();
    }
    if (h == 0 && b < 112) {
      const int jj = b / 14, pn = b - 14 * jj, part = pn / 7, nf = pn - 7 * part;
      const rsrc_t rw = mkrs(ws + WS_WINR, 8 * 20 * 14336), rm = mkrs(ws + WS_MODP, 4 * 3840 * 4);
      u32x4 wf[20];
#pragma unroll
      for (int k = 0; k < 20; ++k) wf[k] = ld16<SC1>(rw, lane * 16, (unsigned)((((jj * 2 + part) * 20 + k) * 7 + nf) * 1024));
#pragma unroll
      for (int it = 0; it < 5; ++it) {
        const int c4 = (it * 64 + lane) * 4, isS = c4 >= DM, cc = c4 - DM * isS;
        const unsigned o = (unsigned)(((isS ? 3 : 4) * DM + cc) * 4);
        const f32x4 m0 = __builtin_bit_cast(f32x4, ld16<SC1>(rm, o, 0)), m1 = __builtin_bit_cast(f32x4, ld16<SC1>(rm, o, 3840 * 4)),
                    m2 = __builtin_bit_cast(f32x4, ld16<SC1>(rm, o, 2 * 3840 * 4)), m3 = __builtin_bit_cast(f32x4, ld16<SC1>(rm, o, 3 * 3840 * 4));
        f32x4 s = (m0 + m1) + (m2 + m3);
#pragma unroll
        for (int i = 0; i < 4; ++i) { s[i] = rbf(s[i]); if (!isS) s[i] = 1.0f + s[i]; }
        *(LAS u32x2*)(lds + L_TABE + (isS * DM + cc) * 2) = (u32x2){pk2(s[0], s[1]), pk2(s[2], s[3])};
      }
      wait_lgkm0(); CFENCE();
      f32x4 acc = {0.f, 0.f, 0.f, 0.f};
#pragma unroll
      for (int k = 0; k < 20; ++k) {
        u32x4 e = *(LAS u32x4*)(lds + L_TABE + ((l15 & 1) * DM + 32 * k + 8 * q) * 2);
        if (l15 >= 2) e = (u32x4){0u, 0u, 0u, 0u};
        acc = mfma(asfrag(e), asfrag(wf[k]), acc);
      }
      const rsrc_t ruv = mkrs(ws + WS_UV, 2 * 896 * 16);
      st16<SC1>((u32x4){__float_as_uint(acc[0]), __float_as_uint(acc[1]), (unsigned)T, (unsigned)(T >> 32)}, ruv,
                q == 0 ? (unsigned)((part * 896 + 112 * jj + 16 * nf + l15) * 16) : OOB, 0);
    }
    STAMP(3);
    idle_phase<22>();
    idle_phase<12>();
    if (h == 1) {
      const rsrc_t r1 = mkrs(p.wsp + (size_t)(64 * g + 8 * j) * SEQ, 8 * SEQ * 2);
#pragma unroll
      for (int it = 0; it < 4; ++it)
        __builtin_amdgcn_raw_ptr_buffer_load_lds(r1, (LAS void*)(lds + L_SINK), 4, (int)((lane >> 3) * 4096 + ((it * 8 + (lane & 7) + rot) & 31) * 128), 0, 0, 0);
    }
    idle_phase<11>();
    {
      group_wait(lds, 17, ws, WS_FLAG3 + j * 4096, 32, T, h == 0, lane);
      STAMP(4);
      const rsrc_t r = mkrs(ws + WS_Z2T + (size_t)j * 32 * 14336, 32 * 14336);
      auto issue = [&](int t, int slot) {
        const unsigned k = (unsigned)((t + rot) & 31) * 14336;
#pragma unroll
        for (int pc = 0; pc < 7; ++pc) dma16<SC1>(r, lds + P4_DOFF + slot * 14336 + (h * 7 + pc) * 1024, lane * 16, k + (h * 7 + pc) * 1024);
      };
      loader_fill<0, 2, P4_DB>(issue);
      loader_run<32, P4_DB, 7, 2, 2, -1>(issue);
      BAR(); BAR();
    }
    idle_phase<15>();
  }
}

extern "C" void kernel_launch(void* const* d_in, const int* in_sizes, int n_in, void* d_out, int out_size, void* d_ws, size_t ws_size,
                              hipStream_t stream) {
  Params p;
  p.x = (const u16*)d_in[0]; p.wsre = (const u16*)d_in[1]; p.wsim = (const u16*)d_in[2]; p.wdre = (const u16*)d_in[3]; p.wdim = (const u16*)d_in[4];
  p.win = (const u16*)d_in[6]; p.wsp = (const u16*)d_in[7]; p.bsp = (const u16*)d_in[8]; p.wout = (const u16*)d_in[9];
  p.c = (const u16*)d_in[10]; p.wmod = (const u16*)d_in[11];
  p.out = (u16*)d_out; p.ws = (char*)d_ws + ((ws_size - WS_USED) & ~(size_t)0x1fffff);
  (void)hipFuncSetAttribute((const void*)adaLN_cond__spectral_fnet__mixer_gmlp__prompt_kernel, hipFuncAttributeMaxDynamicSharedMemorySize, LDS_BYTES);
  adaLN_cond__spectral_fnet__mixer_gmlp__prompt_kernel<<<256, 768, LDS_BYTES, stream>>>(p);
}
```

```cpp
#define STAMPS
#include <hip/hip_runtime.h>
#include <hip/hip_bf16.h>

typedef unsigned short u16;
typedef unsigned long long u64;
typedef short bf16x8 __attribute__((ext_vector_type(8)));
typedef short s16x4 __attribute__((ext_vector_type(4)));
typedef __bf16 bf16x2 __attribute__((ext_vector_type(2)));
typedef float f32x2 __attribute__((ext_vector_type(2)));
typedef float f32x4 __attribute__((ext_vector_type(4)));
typedef unsigned u32x2 __attribute__((ext_vector_type(2)));
typedef unsigned u32x4 __attribute__((ext_vector_type(4)));
typedef __amdgpu_buffer_rsrc_t rsrc_t;
#define LAS __attribute__((address_space(3)))
#define GAS __attribute__((address_space(1)))
#define DEVI __device__ __forceinline__

constexpr int SEQ = 2048, DM = 640, FF = 1728, FH = 864;

constexpr unsigned WS_EPOCH = 0x0;
constexpr unsigned WS_TMO   = 0x100;
constexpr unsigned WS_FLAG0 = 0x1A00000;
constexpr unsigned WS_FLAGB = 0x1A08000;
constexpr unsigned WS_FLAG1 = 0x1A10000;
constexpr unsigned WS_FLAG2 = 0x1A18000;
constexpr unsigned WS_FLAG4 = 0x1A20000;
constexpr unsigned WS_FLAG3 = 0x1A28000;
constexpr unsigned WS_MODP  = 0x10000;
constexpr unsigned WS_UV    = 0x20000;
constexpr unsigned WS_ST1   = 0x40000;
constexpr unsigned WS_ST2   = 0x80000;
constexpr unsigned WS_XNT   = 0x100000;
constexpr unsigned WS_PQ    = 0x400000;
constexpr unsigned WS_X1C   = 0xA00000;
constexpr unsigned WS_Z2T   = 0xD00000;
constexpr unsigned WS_U     = 0x1100000;
constexpr unsigned WS_WINR  = 0x1500000;
constexpr unsigned WS_WOUTR = 0x1800000;
constexpr unsigned WS_STAMP = 0x2400000;
constexpr unsigned WS_USED  = 0x2600000;

constexpr int LDS_BYTES = 163840;
constexpr int L_MISC = 159744;
constexpr int L_CNT  = L_MISC;
constexpr int L_TAB  = L_MISC + 128;
constexpr int L_UV   = L_MISC + 1728;
constexpr int L_SINK = L_MISC + 3584;
constexpr int L_TABE = 153600;
constexpr int L_P0T  = 98304;
constexpr int L_RT   = 118784;
constexpr int P0T_RS = 1296;

#define T0N 1
#define F0DELAY 10
#define PDELAY 8
constexpr unsigned SPIN_MAX = 1u << 17;

struct Params {
  const u16 *x, *wsre, *wsim, *wdre, *wdim, *win, *wsp, *bsp, *wout, *c, *wmod;
  u16* out;
  char* ws;
};

DEVI rsrc_t mkrs(const void* p, unsigned bytes) { return __builtin_amdgcn_make_buffer_rsrc((void*)p, 0, (int)bytes, 0x00020000); }
template <int AUX> DEVI u32x4 ld16(rsrc_t r, unsigned v, unsigned s) { return __builtin_amdgcn_raw_buffer_load_b128(r, (int)v, (int)s, AUX); }
template <int AUX> DEVI u32x2 ld8(rsrc_t r, unsigned v, unsigned s) { return __builtin_amdgcn_raw_buffer_load_b64(r, (int)v, (int)s, AUX); }
template <int AUX> DEVI unsigned ld4(rsrc_t r, unsigned v, unsigned s) { return __builtin_amdgcn_raw_buffer_load_b32(r, (int)v, (int)s, AUX); }
template <int AUX> DEVI unsigned ld2(rsrc_t r, unsigned v, unsigned s) { return (unsigned)__builtin_amdgcn_raw_buffer_load_b16(r, (int)v, (int)s, AUX) & 0xffffu; }
template <int AUX> DEVI void st16(u32x4 d, rsrc_t r, unsigned v, unsigned s) { __builtin_amdgcn_raw_buffer_store_b128(d, r, (int)v, (int)s, AUX); }
template <int AUX> DEVI void st8(u32x2 d, rsrc_t r, unsigned v, unsigned s) { __builtin_amdgcn_raw_buffer_store_b64(d, r, (int)v, (int)s, AUX); }
template <int AUX> DEVI void dma16(rsrc_t r, LAS char* dst, unsigned v, unsigned s) { __builtin_amdgcn_raw_ptr_buffer_load_lds(r, (LAS void*)dst, 16, (int)v, (int)s, 0, AUX); }
constexpr int SC1 = 16;
constexpr unsigned OOB = 0x7ffffff0u;

template <int N> DEVI void wait_vm() { asm volatile("s_waitcnt vmcnt(%0)" ::"n"(N > 63 ? 63 : N) : "memory"); }
DEVI void wait_lgkm0() { __builtin_amdgcn_s_waitcnt(0xC07F); }
DEVI void BAR() { asm volatile("s_barrier" ::: "memory"); }
DEVI void CFENCE() { asm volatile("" ::: "memory"); }
#define SCHED() __builtin_amdgcn_sched_barrier(0)

DEVI float bflo(unsigned v) { return __uint_as_float(v << 16); }
DEVI float bfhi(unsigned v) { return __uint_as_float(v & 0xffff0000u); }
DEVI unsigned pk2(float lo, float hi) { f32x2 f = {lo, hi}; return __builtin_bit_cast(unsigned, __builtin_convertvector(f, bf16x2)); }
DEVI float rbf(float v) { return bflo(pk2(v, 0.f)); }
DEVI f32x4 mfma(bf16x8 a, bf16x8 b, f32x4 c) { return __builtin_amdgcn_mfma_f32_16x16x32_bf16(a, b, c, 0, 0, 0); }
DEVI bf16x8 asfrag(u32x4 v) { return __builtin_bit_cast(bf16x8, v); }
DEVI bf16x8 ldsfrag(LAS char* p) { return *(LAS bf16x8*)p; }

template <int CTRL> DEVI float dpp_add(float v) { return v + __int_as_float(__builtin_amdgcn_update_dpp(0, __float_as_int(v), CTRL, 0xf, 0xf, false)); }
DEVI float row16_sum(float v) { v = dpp_add<0x128>(v); v = dpp_add<0x124>(v); v = dpp_add<0x122>(v); return dpp_add<0x121>(v); }
DEVI f32x2 both16(float v) { auto r = __builtin_amdgcn_permlane16_swap(__float_as_uint(v), __float_as_uint(v), false, false); return (f32x2){__uint_as_float(r[0]), __uint_as_float(r[1])}; }
DEVI f32x2 both32(float v) { auto r = __builtin_amdgcn_permlane32_swap(__float_as_uint(v), __float_as_uint(v), false, false); return (f32x2){__uint_as_float(r[0]), __uint_as_float(r[1])}; }
DEVI float xq_sum(float v) { f32x2 a = both16(v); v = a.x + a.y; a = both32(v); return a.x + a.y; }
DEVI float rsq(float v) { return __builtin_amdgcn_rsqf(v); }
DEVI float wave_sum(float v) { return xq_sum(row16_sum(v)); }

DEVI float gelu_tanh(float x) {
  constexpr float k1 = -2.0f * 0.7978845608028654f * 1.4426950408889634f, k2 = k1 * 0.044715f;
  return x * __builtin_amdgcn_rcpf(1.0f + __builtin_amdgcn_exp2f(x * __builtin_fmaf(x * x, k2, k1)));
}
DEVI f32x2 ln_stats(float s, float q, float inv_n) {
  const float m = s * inv_n, v = __builtin_fmaf(q, inv_n, -m * m);
  return (f32x2){m, __builtin_amdgcn_rsqf((v > 0.f ? v : 0.f) + 1e-5f)};
}

DEVI u32x4 pair_swap(u32x2 a, u32x2 b) {
  auto s0 = __builtin_amdgcn_permlane16_swap(a.x, b.x, false, false);
  auto s1 = __builtin_amdgcn_permlane16_swap(a.y, b.y, false, false);
  return (u32x4){s0[0], s1[0], s0[1], s1[1]};
}

DEVI void note_timeout(char* ws, unsigned code, int lane) {
  if (lane == 0) __hip_atomic_store((GAS unsigned*)(ws + WS_TMO), code, __ATOMIC_RELAXED, __HIP_MEMORY_SCOPE_AGENT);
}
DEVI void poll_flags(char* ws, unsigned off, int n, u64 T, int lane) {
  for (unsigned spins = 0;; ++spins) {
    u64 f = T;
    if (lane < n) f = __hip_atomic_load((GAS u64*)(ws + off + lane * 128), __ATOMIC_RELAXED, __HIP_MEMORY_SCOPE_AGENT);
    if (__all(f == T)) break;
    if (spins > SPIN_MAX) { note_timeout(ws, 0x100u | ((off >> 15) & 0xff), lane); break; }
    __builtin_amdgcn_s_sleep(1);
  }
  CFENCE();
}
DEVI void poll_flags256(char* ws, unsigned off, u64 T, int lane) {
  for (unsigned spins = 0;; ++spins) {
    bool ok = true;
#pragma unroll
    for (int i = 0; i < 4; ++i) ok &= __hip_atomic_load((GAS u64*)(ws + off + (i * 8 + (lane >> 3)) * 128 + (lane & 7) * 8), __ATOMIC_RELAXED, __HIP_MEMORY_SCOPE_AGENT) == T;
    if (__all(ok)) break;
    if (spins > SPIN_MAX) { note_timeout(ws, 0x200u | ((off >> 15) & 0xff), lane); break; }
    __builtin_amdgcn_s_sleep(4);
  }
  CFENCE();
}
DEVI void set_flag(char* ws, unsigned off, u64 T, int lane) {
  if (lane == 0) __hip_atomic_store((GAS u64*)(ws + off), T, __ATOMIC_RELAXED, __HIP_MEMORY_SCOPE_AGENT);
}
DEVI void lds_signal(LAS char* lds, int idx, int lane) {
  CFENCE();
  if (lane == 0) __hip_atomic_store((LAS unsigned*)(lds + L_CNT) + idx, 1u, __ATOMIC_RELAXED, __HIP_MEMORY_SCOPE_WORKGROUP);
}
DEVI void lds_wait(LAS char* lds, int idx) {
  for (unsigned spins = 0; spins < (SPIN_MAX << 6); ++spins) {
    if (__hip_atomic_load((LAS unsigned*)(lds + L_CNT) + idx, __ATOMIC_RELAXED, __HIP_MEMORY_SCOPE_WORKGROUP) != 0u) break;
    __builtin_amdgcn_s_sleep(1);
  }
  CFENCE();
}
DEVI void lds_wait_n(LAS char* lds, int idx, unsigned n) {
  for (unsigned spins = 0; spins < (SPIN_MAX << 6); ++spins) {
    if (__hip_atomic_load((LAS unsigned*)(lds + L_CNT) + idx, __ATOMIC_RELAXED, __HIP_MEMORY_SCOPE_WORKGROUP) >= n) break;
    __builtin_amdgcn_s_sleep(1);
  }
  CFENCE();
}
DEVI void group_wait(LAS char* lds, int idx, char* ws, unsigned off, int n, u64 T, bool poller, int lane) {
  if (poller) { __builtin_amdgcn_s_sleep(PDELAY); poll_flags(ws, off, n, T, lane); lds_signal(lds, idx, lane); } else lds_wait(lds, idx);
}
DEVI bool arrive_last(LAS char* lds, int idx, unsigned n, int lane) {
  unsigned old = 0;
  if (lane == 0) old = __hip_atomic_fetch_add((LAS unsigned*)(lds + L_CNT) + idx, 1u, __ATOMIC_RELAXED, __HIP_MEMORY_SCOPE_WORKGROUP);
  return (unsigned)__builtin_amdgcn_readfirstlane(old) == n - 1;
}
DEVI void publish(LAS char* lds, int cidx, unsigned nwaves, char* ws, unsigned flag_off, u64 T, int lane) {
  wait_vm<0>();
  if (arrive_last(lds, cidx, nwaves, lane)) set_flag(ws, flag_off, T, lane);
}

#define LANE_IDS() int tz_ = threadIdx.x; asm volatile("" : "+v"(tz_)); const int lane = tz_ & 63, l15 = lane & 15, q = lane >> 4; (void)l15; (void)q

#define LANE_IDS2() int tz2_ = threadIdx.x; asm volatile("" : "+v"(tz2_)); const int laneb = tz2_ & 63, l15b = laneb & 15, qb = laneb >> 4; (void)l15b; (void)qb

#define STAMP(k) do { if (((k) == 0 || ((k) == 2 && wid == 0)) && lane == 0) *(GAS u64*)(p.ws + WS_STAMP + ((size_t)b * 16 + wid) * 256 + (k) * 8) = __builtin_amdgcn_s_memrealtime(); } while (0)
#define STAMPC(k) do { } while (0)

template <int NT, int RING, int P0, int CU> constexpr int issued(int i) {
  int n = P0 < NT ? P0 : NT;
  for (int k = 0; k <= i; ++k) { int m = n + CU; if (m > k + RING) m = k + RING; if (m > NT) m = NT; n = m; }
  return n;
}
constexpr int cap63(int n, int pc) { return n > 63 - pc ? 63 - pc : n; }
template <int I, int IEND, int NT, int RING, int PC, int P0, int CU, int XI, class Issue> DEVI void loader_steps(Issue& issue) {
  if constexpr (I < IEND) {
    constexpr int before = issued<NT, RING, P0, CU>(I - 1), after = issued<NT, RING, P0, CU>(I);
    wait_vm<cap63((before - (I + 2)) * PC, (after - before) * PC)>(); BAR();
    if constexpr (after > before) issue(before, before % RING);
    if constexpr (after > before + 1) issue(before + 1, (before + 1) % RING);
    if constexpr (after > before + 2) issue(before + 2, (before + 2) % RING);
    static_assert(after <= before + 3, "at most three tiles per step");
    if constexpr (I == XI) BAR();
    loader_steps<I + 1, IEND, NT, RING, PC, P0, CU, XI>(issue);
  }
}
template <int NT, int RING, int P0, int CU> constexpr int steady_from() {
  for (int i = 0; i < NT; ++i) if (issued<NT, RING, P0, CU>(i - 1) == i - 1 + RING) return i;
  return NT;
}
template <int T0, int T1, int RING, class Issue> DEVI void loader_fill(Issue& issue) {
#pragma unroll 1
  for (int t = T0; t < T1; ++t) issue(t, t);
}
struct NoHook { DEVI void operator()(int) const {} };
template <int NT, int RING, int PC, int P0, int CU, int XI, class Issue, class Hook = NoHook> DEVI void loader_run(Issue& issue, Hook hook = Hook()) {
  constexpr int P = P0 < NT ? P0 : NT;
  wait_vm<cap63((P - 1) * PC, 0)>(); BAR();
  constexpr int S0 = steady_from<NT, RING, P0, CU>(), S1 = NT - RING + 1;
  if constexpr (XI < 0 && S0 + 2 < S1) {
    loader_steps<0, S0, NT, RING, PC, P0, CU, XI>(issue);
    int slot = (S0 - 1 + RING) % RING;
#pragma unroll 1
    for (int i = S0; i < S1; ++i) {
      wait_vm<cap63((RING - 3) * PC, PC)>(); BAR();
      issue(i - 1 + RING, slot);
      hook(i);
      slot = (slot + 1 == RING) ? 0 : slot + 1;
    }
    loader_steps<S1, NT - 1, NT, RING, PC, P0, CU, XI>(issue);
  } else {
    loader_steps<0, NT - 1, NT, RING, PC, P0, CU, XI>(issue);
  }
}
template <int NBAR> DEVI void idle_phase() {
#pragma unroll 1
  for (int i = 0; i < NBAR; ++i) BAR();
}

template <int I, int NMF, int NRD, int PER> struct Interleave {
  static DEVI void run() {
    if constexpr (I < NMF) {
      __builtin_amdgcn_sched_group_barrier(0x8, 1, 0);
      constexpr int left = NRD - I * PER, now = left < PER ? left : PER;
      if constexpr (now > 0) __builtin_amdgcn_sched_group_barrier(0x100, now, 0);
      Interleave<I + 1, NMF, NRD, PER>::run();
    }
  }
};
template <int NMF, int NRD, int PER> DEVI void interleave() { Interleave<0, NMF, NRD, PER>::run(); }

#define AW 4
#define PF2 12
#define PF3 7
#define PF4 8
#define PF5 12
constexpr int P1_SA = 6, P1_DB = 5, P1_DOFF = 98304, P1_X = 32768;
constexpr int P2_RING = 15, P2_X = 51200;
constexpr int P3_RING = 8, P3_X = 114688;
constexpr int P4_SA = 9, P4_DB = 5, P4_DOFF = 73728, P4_X = 40960;
constexpr int L_X1 = 145408;
constexpr int P5_RING = 14, P5_X = 0;

DEVI f32x4 sel4(int c, f32x4 a, f32x4 b) { asm volatile("" : "+v"(a), "+v"(b)); return c ? a : b; }
DEVI float sel1(int c, float a, float b) { asm volatile("" : "+v"(a), "+v"(b)); return c ? a : b; }
DEVI void lds_put(LAS char* p, f32x4 v) { *(LAS f32x4*)p = v; }
DEVI f32x4 lds_get(LAS char* p) { return *(LAS f32x4*)p; }
DEVI u64 epoch_tag(u64 e) {
  return (((u64)(unsigned)__builtin_amdgcn_readfirstlane((unsigned)(e >> 32)) << 32) | (unsigned)__builtin_amdgcn_readfirstlane((unsigned)e)) + 1;
}
DEVI void chan(float& n, float& m, float& M2, float no, float mo, float Mo) {
  const float nt = n + no, d = mo - m, r = __builtin_amdgcn_rcpf(nt);
  M2 = M2 + Mo + d * d * (n * no * r);
  m = m + d * (no * r);
  n = nt;
}

__global__ __launch_bounds__(768, 3) void adaLN_cond__spectral_fnet__mixer_gmlp__prompt_kernel(Params p) {
  extern __shared__ __attribute__((aligned(16))) char smem_[];
  LAS char* lds = (LAS char*)smem_;
  const int tid = threadIdx.x, wid = __builtin_amdgcn_readfirstlane(tid >> 6);
  const int b = blockIdx.x, xg = b & 7, i8 = b >> 3;
  const int g = xg * 4 + (i8 >> 3), j = i8 & 7;
  char* const ws = p.ws;
  const int rot = xg * 4;

  { LANE_IDS(); STAMP(0); STAMPC(30); }
  const u64 Ein = __hip_atomic_load((GAS u64*)(ws + WS_EPOCH), __ATOMIC_RELAXED, __HIP_MEMORY_SCOPE_AGENT);
  if (tid < 32) *((LAS unsigned*)(lds + L_CNT) + tid) = 0u;

  if (wid < 8) {
    const int w = wid, m = w & 3, kh = w >> 2;
    __builtin_amdgcn_s_setprio(2);
    {
      LANE_IDS();
      const rsrc_t rx = mkrs(p.x, SEQ * DM * 2);
      u32x4 v[2];
#pragma unroll
      for (int h = 0; h < 2; ++h) v[h] = ld16<0>(rx, lane < 40 ? (unsigned)((lane + 40 * h) * 16) : OOB, (unsigned)(64 * g + 8 * j + w) * (DM * 2));
      wait_lgkm0();
      BAR();
      float f[16];
#pragma unroll
      for (int h = 0; h < 2; ++h)
#pragma unroll
        for (int i = 0; i < 4; ++i) { f[h * 8 + 2 * i] = bflo(v[h][i]); f[h * 8 + 2 * i + 1] = bfhi(v[h][i]); }
      float s = 0.f, sq = 0.f;
#pragma unroll
      for (int i = 0; i < 16; ++i) { s += f[i]; sq = __builtin_fmaf(f[i], f[i], sq); }
      const f32x2 ls = ln_stats(wave_sum(s), wave_sum(sq), 1.0f / DM);
      const float rstd = ls.y, c0 = -ls.x * ls.y;
      if (lane < 40) {
#pragma unroll
        for (int h = 0; h < 2; ++h) {
          u32x4 o;
#pragma unroll
          for (int i = 0; i < 4; ++i) o[i] = pk2(__builtin_fmaf(f[h * 8 + 2 * i], rstd, c0), __builtin_fmaf(f[h * 8 + 2 * i + 1], rstd, c0));
          *(LAS u32x4*)(lds + L_P0T + w * P0T_RS + (lane + 40 * h) * 16) = o;
        }
      }
      wait_lgkm0(); CFENCE();
      if (lane == 0) __hip_atomic_fetch_add((LAS unsigned*)(lds + L_CNT) + 0, 1u, __ATOMIC_RELAXED, __HIP_MEMORY_SCOPE_WORKGROUP);
      for (unsigned spins = 0; spins < SPIN_MAX; ++spins) {
        if (__hip_atomic_load((LAS unsigned*)(lds + L_CNT) + 0, __ATOMIC_RELAXED, __HIP_MEMORY_SCOPE_WORKGROUP) >= 8u) break;
      }
      CFENCE();
      const rsrc_t rxn = mkrs(ws + WS_XNT, 8 * 32 * 10240);
#pragma unroll
      for (int it = 0; it < 2; ++it) {
        const int pc = it == 0 ? q : 4, nf = 5 * w + pc;
        const int qq = l15 >> 2, pp = l15 & 3;
        LAS char* a0 = lds + L_P0T + qq * P0T_RS + (16 * nf + 4 * pp) * 2;
        const u32x2 lo = __builtin_bit_cast(u32x2, __builtin_amdgcn_ds_read_tr16_b64_v4i16((LAS s16x4*)a0));
        const u32x2 hi = __builtin_bit_cast(u32x2, __builtin_amdgcn_ds_read_tr16_b64_v4i16((LAS s16x4*)(a0 + 4 * P0T_RS)));
        const unsigned off = (unsigned)(((w * 32 + g) * 10 + (j >> 2) * 5 + pc) * 1024 + ((j & 3) * 16 + l15) * 16);
        st16<SC1>((u32x4){lo.x, lo.y, hi.x, hi.y}, rxn, (it == 0 || q == 0) ? off : OOB, 0);
      }
    }
    { LANE_IDS(); if (lane == 0) __hip_atomic_fetch_add((LAS unsigned*)(lds + L_CNT) + 13, 1u, __ATOMIC_RELAXED, __HIP_MEMORY_SCOPE_WORKGROUP); }
    const u64 T = epoch_tag(Ein);
    { LANE_IDS(); wait_vm<0>(); if (arrive_last(lds, 1, 8, lane)) {
      if (lane < 8) __hip_atomic_store((GAS u64*)(ws + WS_FLAG0 + lane * 4096 + g * 128 + j * 8), T, __ATOMIC_RELAXED, __HIP_MEMORY_SCOPE_AGENT);
      lds_signal(lds, 14, lane); } STAMP(1); }

    unsigned z1p[8];

    {
      LANE_IDS();
      f32x4 acc[2][5];
#pragma unroll
      for (int a = 0; a < 2; ++a)
#pragma unroll
        for (int n = 0; n < 5; ++n) acc[a][n] = (f32x4){0.f, 0.f, 0.f, 0.f};
      f32x4 rs[2] = {{0.f, 0.f, 0.f, 0.f}, {0.f, 0.f, 0.f, 0.f}};
      u32x4 ones4 = {0x3f803f80u, 0x3f803f80u, 0x3f803f80u, 0x3f803f80u}; asm volatile("" : "+v"(ones4));
      const bf16x8 ones = asfrag(ones4);
      const unsigned aoff = (m * 16 + l15) * 128 + (((kh * 4 + q) ^ (l15 >> 1)) << 4);
      const unsigned boff = P1_DOFF + kh * 5120 + lane * 16;
      bf16x8 XA[2], XB[5], YA[2], YB[5];
      int sa = 0, sb = 0;
      auto rd = [&](bf16x8(&A)[2], bf16x8(&B)[5]) {
        LAS char* pa = lds + sa * 16384 + aoff;
        LAS char* pb = lds + sb * 10240 + boff;
        A[0] = ldsfrag(pa); A[1] = ldsfrag(pa + 8192);
#pragma unroll
        for (int n = 0; n < 5; ++n) B[n] = ldsfrag(pb + n * 1024);
        sa = (sa + 1 == P1_SA) ? 0 : sa + 1; sb = (sb + 1 == P1_DB) ? 0 : sb + 1;
      };
      auto mm = [&](const bf16x8(&A)[2], const bf16x8(&B)[5]) {
#pragma unroll
        for (int a = 0; a < 2; ++a) {
#pragma unroll
          for (int n = 0; n < 5; ++n) acc[a][n] = mfma(B[n], A[a], acc[a][n]);
          rs[a] = mfma(ones, A[a], rs[a]);
        }
      };
      auto pairs = [&](int n) {
#pragma unroll 1
        for (int i = 0; i < n; ++i) {
          BAR(); rd(YA, YB); mm(XA, XB); interleave<12, 7, 1>(); SCHED();
          BAR(); rd(XA, XB); mm(YA, YB); interleave<12, 7, 1>(); SCHED();
        }
      };
      BAR();
      STAMP(2); STAMPC(28);
      rd(XA, XB);
      pairs(6);
      u64 fb[4];
      if (w == 0) {
#pragma unroll
        for (int i = 0; i < 4; ++i) fb[i] = __hip_atomic_load((GAS u64*)(ws + WS_FLAGB + (i * 8 + (lane >> 3)) * 128 + (lane & 7) * 8), __ATOMIC_RELAXED, __HIP_MEMORY_SCOPE_AGENT);
      }
      pairs(3);
      f32x4 mp[2][4];
      const int vs0 = lane < 20 ? 1 : lane < 40 ? 0 : 2, vs1 = lane < 20 ? 4 : 5;
      const int vl = lane < 20 ? lane : lane < 40 ? lane - 20 : lane - 40;
      if (w == 0) {
        if (!__all(fb[0] == T && fb[1] == T && fb[2] == T && fb[3] == T)) poll_flags256(ws, WS_FLAGB, T, lane);
        CFENCE();
        const rsrc_t rm = mkrs(ws + WS_MODP, 4 * 3840 * 4);
#pragma unroll
        for (int k = 0; k < 4; ++k) {
          mp[0][k] = __builtin_bit_cast(f32x4, ld16<SC1>(rm, lane < 60 ? (unsigned)((vs0 * DM + 80 * j + vl * 4) * 4) : OOB, k * 3840 * 4));
          mp[1][k] = __builtin_bit_cast(f32x4, ld16<SC1>(rm, lane < 40 ? (unsigned)((vs1 * DM + 80 * j + vl * 4) * 4) : OOB, k * 3840 * 4));
        }
      }
      pairs(3);
      if (w == 0) {
        f32x4 s0 = (mp[0][0] + mp[0][1]) + (mp[0][2] + mp[0][3]), s1 = (mp[1][0] + mp[1][1]) + (mp[1][2] + mp[1][3]);
#pragma unroll
        for (int i = 0; i < 4; ++i) {
          s0[i] = rbf(s0[i]); if (lane < 20) s0[i] = rbf(1.0f + s0[i]);
          s1[i] = rbf(s1[i]); if (lane < 20) s1[i] = rbf(1.0f + s1[i]);
        }
        if (lane < 60) lds_put(lds + L_TAB + ((lane < 20 ? 0 : lane < 40 ? 1 : 2) * 80 + vl * 4) * 4, s0);
        if (lane < 40) lds_put(lds + L_TAB + ((lane < 20 ? 3 : 4) * 80 + vl * 4) * 4, s1);
      }
      pairs(3);
      BAR(); rd(YA, YB); mm(XA, XB); interleave<12, 7, 1>(); SCHED();
      mm(YA, YB);
      STAMP(3); STAMPC(29);
      {
        LAS char* xo = lds + P1_X + w * 6144 + lane * 16;
        LAS char* xi = lds + P1_X + (w ^ 4) * 6144 + lane * 16;
#pragma unroll
        for (int n = 0; n < 5; ++n) lds_put(xo + n * 1024, sel4(kh, acc[0][n], acc[1][n]));
        *(LAS float*)(lds + P1_X + w * 6144 + 5120 + lane * 4) = sel1(kh, rs[0][0], rs[1][0]);
        wait_lgkm0();
        BAR();
        const float rsum = sel1(kh, rs[1][0], rs[0][0]) + *(LAS float*)(lds + P1_X + (w ^ 4) * 6144 + 5120 + lane * 4);
        u32x2 pkd[5];
#pragma unroll
        for (int n = 0; n < 5; ++n) {
          const f32x4 a = sel4(kh, acc[1][n], acc[0][n]) + lds_get(xi + n * 1024);
          const f32x4 c1 = lds_get(lds + L_TAB + (0 * 80 + 16 * n + 4 * q) * 4), sa4 = lds_get(lds + L_TAB + (1 * 80 + 16 * n + 4 * q) * 4);
          f32x4 v = a * c1 + rsum * sa4;
          if (kh) v = -v;
          pkd[n] = (u32x2){pk2(v[0], v[1]), pk2(v[2], v[3])};
        }
        const rsrc_t rpq = mkrs(ws + WS_PQ, 32 * 40 * 4 * 1024);
        auto pq_off = [&](int nf) { const int c = 10 * j + 2 * nf + (q >> 1); return (unsigned)(((g * 40 + kh * 20 + (c >> 2)) * 4 + m) * 1024 + ((c & 3) * 16 + l15) * 16); };
        const int odd = q & 1;
        st16<SC1>(pair_swap(pkd[0], pkd[1]), rpq, pq_off(odd), 0);
        st16<SC1>(pair_swap(pkd[2], pkd[3]), rpq, pq_off(2 + odd), 0);
        st8<SC1>(pkd[4], rpq, pq_off(4) + odd * 8, 0);
      }
      BAR();
      publish(lds, 3, 8, ws, WS_FLAG1 + (g * 8 + j) * 128, T, lane);
      STAMP(4);
    }

    {
      LANE_IDS();
      constexpr int NT = 20;
      const int nb = kh ? 3 : 0;
      group_wait(lds, 8, ws, WS_FLAG1 + g * 1024, 8, T, w == 0, lane);
      STAMP(5);
      const rsrc_t ra = mkrs(ws + WS_PQ + (size_t)g * 40 * 4 * 1024, 40 * 4 * 1024);
      u32x4 A[NT];
#pragma unroll
      for (int t = 0; t < AW; ++t) A[t] = ld16<SC1>(ra, lane * 16, ((2 * t + kh) * 4 + m) * 1024);
      f32x4 acc[5];
#pragma unroll
      for (int n = 0; n < 5; ++n) acc[n] = (f32x4){0.f, 0.f, 0.f, 0.f};
      const unsigned boff = l15 * 128 + (((kh * 4 + q) ^ (l15 >> 1)) << 4);
      bf16x8 B[2][5];
      auto rd = [&](bf16x8(&Bv)[5], int slot) {
#pragma unroll
        for (int n = 0; n < 5; ++n) Bv[n] = ldsfrag(lds + slot * 10240 + boff + n * 2048);
      };
      BAR();
      STAMP(6);
      rd(B[0], 0);
#pragma unroll
      for (int t = 0; t < NT; ++t) {
        if (t + 1 < NT) { BAR(); rd(B[(t + 1) & 1], (t + 1) % P2_RING); }
        if (t + AW < NT) A[t + AW] = ld16<SC1>(ra, lane * 16, ((2 * (t + AW) + kh) * 4 + m) * 1024);
#pragma unroll
        for (int n = 0; n < 5; ++n) acc[n] = mfma(B[t & 1][n], asfrag(A[t]), acc[n]);
        interleave<5, 5, 1>(); SCHED();
      }
      STAMP(7);
      LANE_IDS2();
      const rsrc_t rx = mkrs(p.x, SEQ * DM * 2);
      u32x2 xin[3];
#pragma unroll
      for (int n = 0; n < 3; ++n) xin[n] = ld8<0>(rx, (kh && n == 2) ? OOB : (unsigned)((64 * g + 16 * m + l15b) * (DM * 2) + (80 * j + 16 * (nb + n) + 4 * qb) * 2), 0);
      LAS char* xo = lds + P2_X + w * 3072 + lane * 16;
      LAS char* xi = lds + P2_X + (w ^ 4) * 3072 + lane * 16;
      if (kh) { lds_put(xo, acc[0]); lds_put(xo + 1024, acc[1]); lds_put(xo + 2048, acc[2]); }
      else { lds_put(xo, acc[3]); lds_put(xo + 1024, acc[4]); }
      wait_lgkm0();
      BAR();
      float s = 0.f, sq = 0.f;
      u32x2 cp[3]; unsigned x1p[6];
      auto fin = [&](int n, f32x4 own) {
        const f32x4 y = own + lds_get(xi + n * 1024);
        const int col = 16 * (nb + n) + 4 * q;
        const f32x4 ga = lds_get(lds + L_TAB + (2 * 80 + col) * 4), cb1 = lds_get(lds + L_TAB + (3 * 80 + col) * 4);
        const f32x4 xv = {bflo(xin[n].x), bfhi(xin[n].x), bflo(xin[n].y), bfhi(xin[n].y)};
        const f32x4 x1 = xv + ga * y, c = x1 * cb1;
        x1p[2 * n] = pk2(x1[0], x1[1]); x1p[2 * n + 1] = pk2(x1[2], x1[3]);
        cp[n] = (u32x2){pk2(c[0], c[1]), pk2(c[2], c[3])};
        s += (x1[0] + x1[1]) + (x1[2] + x1[3]);
        sq = __builtin_fmaf(x1[0], x1[0], __builtin_fmaf(x1[1], x1[1], __builtin_fmaf(x1[2], x1[2], __builtin_fmaf(x1[3], x1[3], sq))));
      };
      if (kh) { fin(0, acc[3]); fin(1, acc[4]); cp[2] = (u32x2){0u, 0u}; x1p[4] = 0u; x1p[5] = 0u; }
      else { fin(0, acc[0]); fin(1, acc[1]); fin(2, acc[2]); }
      s = xq_sum(s); sq = xq_sum(sq);
      const rsrc_t rc = mkrs(ws + WS_X1C, 32 * 20 * 4 * 1024);
      auto c_off = [&](int nf) { const int c = 10 * j + 2 * nf + (q >> 1); return (unsigned)(((g * 20 + (c >> 2)) * 4 + m) * 1024 + ((c & 3) * 16 + l15) * 16); };
      const int odd = q & 1;
      st16<SC1>(pair_swap(cp[0], cp[1]), rc, c_off(nb + odd), 0);
      st8<SC1>(cp[2], rc, kh ? OOB : c_off(2) + odd * 8, 0);
      const rsrc_t rs1 = mkrs(ws + WS_ST1, SEQ * 16 * 8);
      st8<SC1>((u32x2){__float_as_uint(s), __float_as_uint(sq)}, rs1, q == 0 ? (unsigned)(((64 * g + 16 * m + l15) * 16 + 2 * j + kh) * 8) : OOB, 0);
#pragma unroll
      for (int n = 0; n < 3; ++n) *(LAS u32x2*)(lds + L_X1 + (w * 3 + n) * 512 + lane * 8) = (u32x2){x1p[2 * n], x1p[2 * n + 1]};
      wait_lgkm0();
      BAR();
      publish(lds, 4, 8, ws, WS_FLAG2 + (g * 8 + j) * 128, T, lane);
      STAMP(8);
    }

    {
      LANE_IDS();
      constexpr int NT = 10;
      const int nb = kh ? 4 : 0, nn = kh ? 3 : 4;
      {
        const rsrc_t ruv = mkrs(ws + WS_UV, 2 * 896 * 16);
        const int col = w * 28 + lane, part = col >= 112 ? 1 : 0, cc = col - 112 * part;
        const unsigned off = lane < 28 ? (unsigned)((part * 896 + 112 * j + cc) * 16) : OOB;
        u32x4 gv;
        for (unsigned spins = 0;; ++spins) {
          gv = ld16<SC1>(ruv, off, 0);
          const bool ok = lane >= 28 || (gv.z == (unsigned)T && gv.w == (unsigned)(T >> 32));
          if (__all(ok)) break;
          if (spins > SPIN_MAX) { note_timeout(ws, 0x300, lane); break; }
          __builtin_amdgcn_s_sleep(1); CFENCE();
        }
        if (lane < 28) { *(LAS unsigned*)(lds + L_UV + col * 4) = gv.x; *(LAS unsigned*)(lds + L_UV + (224 + col) * 4) = gv.y; }
      }
      group_wait(lds, 9, ws, WS_FLAG2 + g * 1024, 8, T, w == 0, lane);
      STAMP(9);
      const rsrc_t ra = mkrs(ws + WS_X1C + (size_t)g * 20 * 4 * 1024, 20 * 4 * 1024);
      u32x4 A[NT];
#pragma unroll
      for (int t = 0; t < AW; ++t) A[t] = ld16<SC1>(ra, lane * 16, ((2 * t + kh) * 4 + m) * 1024);
      const rsrc_t rs1 = mkrs(ws + WS_ST1, SEQ * 16 * 8);
      const unsigned s1o = (unsigned)(((64 * g + 16 * m + l15) * 16 + 4 * q) * 8);
      const u32x4 st0 = ld16<SC1>(rs1, s1o, 0), st1 = ld16<SC1>(rs1, s1o + 16, 0);
      f32x4 acc[7];
      bf16x8 B[2][7];
      auto rd = [&](bf16x8(&Bv)[7], int slot) {
#pragma unroll
        for (int n = 0; n < 7; ++n) Bv[n] = ldsfrag(lds + slot * 14336 + kh * 7168 + n * 1024 + lane * 16);
      };
      wait_lgkm0();
      BAR();
      STAMP(10);
      rd(B[0], 0); wait_lgkm0();
      float mean1, rstd1;
      {
        const float sm = xq_sum((__uint_as_float(st0.x) + __uint_as_float(st0.z)) + (__uint_as_float(st1.x) + __uint_as_float(st1.z)));
        const float sq = xq_sum((__uint_as_float(st0.y) + __uint_as_float(st0.w)) + (__uint_as_float(st1.y) + __uint_as_float(st1.w)));
        const f32x2 ls = ln_stats(sm, sq, 1.0f / DM);
        mean1 = ls.x; rstd1 = ls.y;
      }
      LAS char* xo = lds + P3_X + (kh ? 12288 + m * 4096 : m * 3072) + lane * 16;
      LAS char* xi = lds + P3_X + (kh ? m * 3072 : 12288 + m * 4096) + lane * 16;
#pragma unroll
      for (int n = 0; n < 7; ++n) acc[n] = (f32x4){0.f, 0.f, 0.f, 0.f};
#pragma unroll
      for (int t = 0; t < NT; ++t) {
        BAR(); if (t + 1 < NT) rd(B[(t + 1) & 1], (t + 1) % P3_RING);
        if (t + AW < NT) A[t + AW] = ld16<SC1>(ra, lane * 16, ((2 * (t + AW) + kh) * 4 + m) * 1024);
#pragma unroll
        for (int n = 0; n < 7; ++n) acc[n] = mfma(asfrag(A[t]), B[t & 1][n], acc[n]);
        interleave<7, 7, 1>(); SCHED();
      }
      STAMP(25);
      if (kh) {
#pragma unroll
        for (int n = 0; n < 4; ++n) lds_put(xo + n * 1024, acc[n]);
      } else {
#pragma unroll
        for (int n = 0; n < 3; ++n) lds_put(xo + n * 1024, acc[4 + n]);
      }
      wait_lgkm0();
      BAR();
      u32x2 z2p[4];
      {
        float rr[4], c1[4];
#pragma unroll
        for (int r = 0; r < 4; ++r) { rr[r] = __shfl(rstd1, 4 * q + r); c1[r] = __shfl(-rstd1 * mean1, 4 * q + r); }
        float zs[4] = {0.f, 0.f, 0.f, 0.f}, zq[4] = {0.f, 0.f, 0.f, 0.f};
        auto fin = [&](int n, f32x4 own) {
          const f32x4 a = own + lds_get(xi + n * 1024);
          const int col = 16 * (nb + n) + l15;
          const float u = *(LAS float*)(lds + L_UV + col * 4), v = *(LAS float*)(lds + L_UV + (224 + col) * 4);
          float z[4];
#pragma unroll
          for (int r = 0; r < 4; ++r) {
            z[r] = gelu_tanh(__builtin_fmaf(rr[r], a[r], __builtin_fmaf(c1[r], u, v)));
            zs[r] += z[r]; zq[r] = __builtin_fmaf(z[r], z[r], zq[r]);
          }
          z2p[n] = (u32x2){pk2(z[0], z[1]), pk2(z[2], z[3])};
        };
        if (kh) { fin(0, acc[4]); fin(1, acc[5]); fin(2, acc[6]); z2p[3] = (u32x2){0u, 0u}; }
        else { fin(0, acc[0]); fin(1, acc[1]); fin(2, acc[2]); fin(3, acc[3]); }
#pragma unroll
        for (int r = 0; r < 4; ++r) { zs[r] = row16_sum(zs[r]); zq[r] = row16_sum(zq[r]); }
        const float ms = l15 == 0 ? zs[0] : l15 == 1 ? zs[1] : l15 == 2 ? zs[2] : zs[3];
        const float Ms = l15 == 0 ? zq[0] : l15 == 1 ? zq[1] : l15 == 2 ? zq[2] : zq[3];
        const rsrc_t rs2 = mkrs(ws + WS_ST2, SEQ * 16 * 16);
        st16<SC1>((u32x4){__float_as_uint(ms), __float_as_uint(Ms), (unsigned)T, (unsigned)(T >> 32)}, rs2,
                  l15 < 4 ? (unsigned)(((64 * g + 16 * m + 4 * q + l15) * 16 + 2 * j + kh) * 16) : OOB, 0);
      }
      STAMP(11);
      rd(B[0], NT % P3_RING); wait_lgkm0();
#pragma unroll
      for (int n = 0; n < 7; ++n) acc[n] = (f32x4){0.f, 0.f, 0.f, 0.f};
#pragma unroll
      for (int t = 0; t < NT; ++t) {
        if (t + 1 < NT) { BAR(); rd(B[(t + 1) & 1], (NT + t + 1) % P3_RING); }
#pragma unroll
        for (int n = 0; n < 7; ++n) acc[n] = mfma(B[t & 1][n], asfrag(A[t]), acc[n]);
        interleave<7, 7, 1>(); SCHED();
      }
      STAMP(12);
      const rsrc_t rs2 = mkrs(ws + WS_ST2, SEQ * 16 * 16);
      const unsigned soff = (unsigned)(((64 * g + 16 * m + l15) * 16 + 4 * q) * 16);
      u32x4 gs[4];
#pragma unroll
      for (int i = 0; i < 4; ++i) gs[i] = ld16<SC1>(rs2, soff + 16 * i, 0);
      if (kh) {
#pragma unroll
        for (int n = 0; n < 4; ++n) lds_put(xo + n * 1024, acc[n]);
      } else {
#pragma unroll
        for (int n = 0; n < 3; ++n) lds_put(xo + n * 1024, acc[4 + n]);
      }
      wait_lgkm0();
      BAR();
      {
        const float c1 = -rstd1 * mean1;
        auto fin = [&](int n, f32x4 own) {
          const f32x4 a = own + lds_get(xi + n * 1024);
          const int col = 112 + 16 * (nb + n) + 4 * q;
          const f32x4 u = lds_get(lds + L_UV + col * 4), v = lds_get(lds + L_UV + (224 + col) * 4);
          const f32x4 pre = a * rstd1 + (u * c1 + v);
          z1p[2 * n] = pk2(gelu_tanh(pre[0]), gelu_tanh(pre[1])); z1p[2 * n + 1] = pk2(gelu_tanh(pre[2]), gelu_tanh(pre[3]));
        };
        if (kh) { fin(0, acc[4]); fin(1, acc[5]); fin(2, acc[6]); z1p[6] = 0u; z1p[7] = 0u; }
        else { fin(0, acc[0]); fin(1, acc[1]); fin(2, acc[2]); fin(3, acc[3]); }
      }
#pragma unroll
      for (int i = 0; i < 8; ++i) asm volatile("" : "+v"(z1p[i]));
      STAMP(26);
      for (unsigned spins = 0;; ++spins) {
        bool ok = true;
#pragma unroll
        for (int i = 0; i < 4; ++i) ok &= gs[i].z == (unsigned)T && gs[i].w == (unsigned)(T >> 32);
        if (__all(ok)) break;
        if (spins > SPIN_MAX) { note_timeout(ws, 0x400, lane); break; }
        __builtin_amdgcn_s_sleep(1); CFENCE();
#pragma unroll
        for (int i = 0; i < 4; ++i) gs[i] = ld16<SC1>(rs2, soff + 16 * i, 0);
      }
      {
        const float sm = xq_sum((__uint_as_float(gs[0].x) + __uint_as_float(gs[1].x)) + (__uint_as_float(gs[2].x) + __uint_as_float(gs[3].x)));
        const float sq = xq_sum((__uint_as_float(gs[0].y) + __uint_as_float(gs[1].y)) + (__uint_as_float(gs[2].y) + __uint_as_float(gs[3].y)));
        const f32x2 ls = ln_stats(sm, sq, 1.0f / FH);
        float rr[4], c2[4];
#pragma unroll
        for (int r = 0; r < 4; ++r) { rr[r] = __shfl(ls.y, 4 * q + r); c2[r] = __shfl(-ls.x * ls.y, 4 * q + r); }
        u32x2 zn[4];
#pragma unroll
        for (int n = 0; n < 4; ++n)
          zn[n] = (u32x2){pk2(__builtin_fmaf(bflo(z2p[n].x), rr[0], c2[0]), __builtin_fmaf(bfhi(z2p[n].x), rr[1], c2[1])),
                          pk2(__builtin_fmaf(bflo(z2p[n].y), rr[2], c2[2]), __builtin_fmaf(bfhi(z2p[n].y), rr[3], c2[3]))};
        const rsrc_t rz2 = mkrs(ws + WS_Z2T, 8 * 32 * 14336);
        auto z_off = [&](int nf) { return (unsigned)(((j * 32 + g) * 14 + (m >> 1) * 7 + nf) * 1024 + ((2 * (m & 1) + (q >> 1)) * 16 + l15) * 16); };
        const int odd = q & 1;
        st16<SC1>(pair_swap(zn[0], zn[1]), rz2, z_off(nb + odd), 0);
        if (kh) st8<SC1>(zn[2], rz2, z_off(6) + odd * 8, 0);
        else st16<SC1>(pair_swap(zn[2], zn[3]), rz2, z_off(2 + odd), 0);
      }
      STAMP(27);
      BAR();
      publish(lds, 5, 8, ws, WS_FLAG3 + (j * 32 + g) * 128, T, lane);
      STAMP(13);
    }

    {
      LANE_IDS();
      const int mh = m >> 1, k4 = m & 1, nh = kh;
      f32x4 acc[2][4];
#pragma unroll
      for (int a = 0; a < 2; ++a)
#pragma unroll
        for (int n = 0; n < 4; ++n) acc[a][n] = (f32x4){0.f, 0.f, 0.f, 0.f};
      const unsigned aoff = (mh * 32 + l15) * 128 + (((k4 * 4 + q) ^ (l15 >> 1)) << 4);
      const unsigned boff = P4_DOFF + k4 * 7168 + nh * 4096 + lane * 16;
      const rsrc_t rb = mkrs(p.bsp, SEQ * 2);
      const float bs = bflo(ld2<0>(rb, (64 * g + 16 * m + l15) * 2, 0));
      bf16x8 XA[2], XB[4], YA[2], YB[4];
      int sa = 0, sb = 0;
      if (nh == 0) {
        auto rd = [&](bf16x8(&A)[2], bf16x8(&B)[4]) {
          LAS char* pa = lds + sa * 8192 + aoff;
          LAS char* pb = lds + sb * 14336 + boff;
          A[0] = ldsfrag(pa); A[1] = ldsfrag(pa + 2048);
#pragma unroll
          for (int n = 0; n < 4; ++n) B[n] = ldsfrag(pb + n * 1024);
          sa = (sa + 1 == P4_SA) ? 0 : sa + 1; sb = (sb + 1 == P4_DB) ? 0 : sb + 1;
        };
        auto mm = [&](const bf16x8(&A)[2], const bf16x8(&B)[4]) {
#pragma unroll
          for (int a = 0; a < 2; ++a)
#pragma unroll
            for (int n = 0; n < 4; ++n) acc[a][n] = mfma(B[n], A[a], acc[a][n]);
        };
        BAR();
        STAMP(14);
        rd(XA, XB);
#pragma unroll 1
        for (int i = 0; i < 15; ++i) {
          BAR(); rd(YA, YB); mm(XA, XB); interleave<8, 6, 1>(); SCHED();
          BAR(); rd(XA, XB); mm(YA, YB); interleave<8, 6, 1>(); SCHED();
        }
        BAR(); rd(YA, YB); mm(XA, XB); interleave<8, 6, 1>(); SCHED();
        mm(YA, YB);
      } else {
        auto rd = [&](bf16x8(&A)[2], bf16x8(&B)[4]) {
          LAS char* pa = lds + sa * 8192 + aoff;
          LAS char* pb = lds + sb * 14336 + boff;
          A[0] = ldsfrag(pa); A[1] = ldsfrag(pa + 2048);
#pragma unroll
          for (int n = 0; n < 3; ++n) B[n] = ldsfrag(pb + n * 1024);
          sa = (sa + 1 == P4_SA) ? 0 : sa + 1; sb = (sb + 1 == P4_DB) ? 0 : sb + 1;
        };
        auto mm = [&](const bf16x8(&A)[2], const bf16x8(&B)[4]) {
#pragma unroll
          for (int a = 0; a < 2; ++a)
#pragma unroll
            for (int n = 0; n < 3; ++n) acc[a][n] = mfma(B[n], A[a], acc[a][n]);
        };
        BAR();
        STAMP(14);
        rd(XA, XB);
#pragma unroll 1
        for (int i = 0; i < 15; ++i) {
          BAR(); rd(YA, YB); mm(XA, XB); interleave<6, 5, 1>(); SCHED();
          BAR(); rd(XA, XB); mm(YA, YB); interleave<6, 5, 1>(); SCHED();
        }
        BAR(); rd(YA, YB); mm(XA, XB); interleave<6, 5, 1>(); SCHED();
        mm(YA, YB);
      }
      STAMP(15);
      LAS char* xo = lds + P4_X + w * 4096 + lane * 16;
      LAS char* xi = lds + P4_X + (w ^ 1) * 4096 + lane * 16;
#pragma unroll
      for (int n = 0; n < 4; ++n) lds_put(xo + n * 1024, sel4(k4, acc[0][n], acc[1][n]));
      wait_lgkm0();
      BAR();
      u32x2 up[4];
#pragma unroll
      for (int n = 0; n < 4; ++n) {
        const f32x4 gv = sel4(k4, acc[1][n], acc[0][n]) + lds_get(xi + n * 1024) + bs;
        up[n] = (u32x2){pk2(bflo(z1p[2 * n]) * gv[0], bfhi(z1p[2 * n]) * gv[1]), pk2(bflo(z1p[2 * n + 1]) * gv[2], bfhi(z1p[2 * n + 1]) * gv[3])};
      }
      const rsrc_t ru = mkrs(ws + WS_U, 32 * 28 * 4 * 1024);
      auto u_off = [&](int nf) { const int c = 14 * j + 2 * nf + (q >> 1); return (unsigned)(((g * 28 + (c >> 2)) * 4 + m) * 1024 + ((c & 3) * 16 + l15) * 16); };
      const int odd = q & 1;
      st16<SC1>(pair_swap(up[0], up[1]), ru, u_off(4 * nh + odd), 0);
      if (nh) st8<SC1>(up[2], ru, u_off(6) + odd * 8, 0);
      else st16<SC1>(pair_swap(up[2], up[3]), ru, u_off(2 + odd), 0);
      STAMP(24);
      BAR();
      publish(lds, 6, 8, ws, WS_FLAG4 + (g * 8 + j) * 128, T, lane);
      if (b == 0 && w == 0 && lane == 0) __hip_atomic_store((GAS u64*)(ws + WS_EPOCH), T, __ATOMIC_RELAXED, __HIP_MEMORY_SCOPE_AGENT);
      STAMP(16);
    }

    {
      LANE_IDS();
      constexpr int NT = 14;
      const int nb = kh ? 3 : 0;
      group_wait(lds, 10, ws, WS_FLAG4 + g * 1024, 8, T, w == 0, lane);
      STAMP(17);
      const rsrc_t ra = mkrs(ws + WS_U + (size_t)g * 28 * 4 * 1024, 28 * 4 * 1024);
      u32x4 A[NT];
#pragma unroll
      for (int t = 0; t < AW; ++t) A[t] = ld16<SC1>(ra, lane * 16, ((2 * t + kh) * 4 + m) * 1024);
      f32x4 acc[5];
#pragma unroll
      for (int n = 0; n < 5; ++n) acc[n] = (f32x4){0.f, 0.f, 0.f, 0.f};
      bf16x8 B[2][5];
      auto rd = [&](bf16x8(&Bv)[5], int slot) {
#pragma unroll
        for (int n = 0; n < 5; ++n) Bv[n] = ldsfrag(lds + slot * 10240 + kh * 5120 + n * 1024 + lane * 16);
      };
      BAR();
      STAMP(18);
      rd(B[0], 0);
#pragma unroll
      for (int t = 0; t < NT; ++t) {
        if (t + 1 < NT) { BAR(); rd(B[(t + 1) & 1], t + 1); }
        if (t + AW < NT) A[t + AW] = ld16<SC1>(ra, lane * 16, ((2 * (t + AW) + kh) * 4 + m) * 1024);
#pragma unroll
        for (int n = 0; n < 5; ++n) acc[n] = mfma(B[t & 1][n], asfrag(A[t]), acc[n]);
        interleave<5, 5, 1>(); SCHED();
      }
      STAMP(19);
      LAS char* xo = lds + P5_X + w * 3072 + lane * 16;
      LAS char* xi = lds + P5_X + (w ^ 4) * 3072 + lane * 16;
      if (kh) { lds_put(xo, acc[0]); lds_put(xo + 1024, acc[1]); lds_put(xo + 2048, acc[2]); }
      else { lds_put(xo, acc[3]); lds_put(xo + 1024, acc[4]); }
      wait_lgkm0();
      BAR();
      u32x2 op[3];
#pragma unroll
      for (int n = 0; n < 3; ++n) {
        const u32x2 x1v = *(LAS u32x2*)(lds + L_X1 + (w * 3 + n) * 512 + lane * 8);
        const unsigned x1p[6] = {x1v.x, x1v.y, x1v.x, x1v.y, x1v.x, x1v.y};
        const f32x4 o = sel4(kh, acc[n == 2 ? 4 : 3 + n], acc[n]) + lds_get(xi + n * 1024);
        const int col = (kh && n == 2) ? 0 : 16 * (nb + n) + 4 * q;
        const f32x4 gb = lds_get(lds + L_TAB + (4 * 80 + col) * 4);
        op[n] = (u32x2){pk2(__builtin_fmaf(gb[0], o[0], bflo(x1p[2 * n])), __builtin_fmaf(gb[1], o[1], bfhi(x1p[2 * n]))),
                        pk2(__builtin_fmaf(gb[2], o[2], bflo(x1p[2 * n + 1])), __builtin_fmaf(gb[3], o[3], bfhi(x1p[2 * n + 1])))};
      }
      const rsrc_t ro = mkrs(p.out, SEQ * DM * 2);
      const unsigned rowb = (unsigned)((64 * g + 16 * m + l15) * (DM * 2) + 80 * j * 2);
      const int odd = q & 1;
      st16<SC1>(pair_swap(op[0], op[1]), ro, rowb + (16 * (nb + odd) + 8 * (q >> 1)) * 2, 0);
      st8<SC1>(op[2], ro, kh ? OOB : rowb + (32 + 4 * q) * 2, 0);
      STAMP(20); STAMPC(31);
    }
  } else if (wid < 10) {
    LANE_IDS();
    const int h = wid - 8;
    const unsigned sw0 = (unsigned)(((lane & 7) ^ (lane >> 4)) * 16), sw1 = sw0 ^ 64;
    wait_lgkm0();
    BAR();
    {
      const rsrc_t r = mkrs((h ? p.wsim : p.wsre) + (size_t)g * 64 * SEQ, 64 * SEQ * 2);
      const unsigned v0 = (lane >> 3) * 4096 + sw0, v1 = (lane >> 3) * 4096 + sw1;
      auto issue = [&](int t, int slot) {
        const unsigned k = (unsigned)((t + rot) & 31) * 128;
#pragma unroll
        for (int pc = 0; pc < 8; ++pc) dma16<0>(r, lds + slot * 16384 + (h * 8 + pc) * 1024, (pc & 1) ? v1 : v0, k + pc * 32768);
      };
      auto touch = [&](int it) {
        __builtin_amdgcn_raw_ptr_buffer_load_lds(r, (LAS void*)(lds + L_SINK), 4, (int)((j * 8 + (lane >> 3)) * 4096 + ((it * 8 + (lane & 7) + rot) & 31) * 128), 0, 0, 0);
      };
      lds_wait(lds, 15); lds_wait(lds, 16);
      if (h == 1) {
        const int cg = b % 60, kq = b / 60;
        const rsrc_t rw = mkrs(p.wmod, b < 240 ? DM * 3840 * 2 : 0), rc = mkrs(p.c, DM * 2);
        u32x4 wv[20]; unsigned cv[20];
#pragma unroll
        for (int it = 0; it < 20; ++it) {
          const int row = (kq & 3) * 160 + it * 8 + (lane >> 3);
          wv[it] = ld16<0>(rw, (unsigned)(row * 7680 + cg * 128 + (lane & 7) * 16), 0);
          cv[it] = ld2<0>(rc, row * 2, 0);
        }
        touch(0); if (T0N > 1) touch(1);
        float a8[8] = {0.f, 0.f, 0.f, 0.f, 0.f, 0.f, 0.f, 0.f};
#pragma unroll
        for (int it = 0; it < 20; ++it) {
          const float cf = bflo(cv[it]);
          const float sl = rbf(cf * __builtin_amdgcn_rcpf(1.0f + __builtin_amdgcn_exp2f(-1.4426950408889634f * cf)));
#pragma unroll
          for (int i = 0; i < 4; ++i) { a8[2 * i] += sl * bflo(wv[it][i]); a8[2 * i + 1] += sl * bfhi(wv[it][i]); }
        }
#pragma unroll
        for (int i = 0; i < 8; ++i) { a8[i] += __shfl_xor(a8[i], 8); a8[i] = xq_sum(a8[i]); }
        const rsrc_t rm = mkrs(ws + WS_MODP, 4 * 3840 * 4);
        const unsigned mo = (unsigned)((kq * 3840 + cg * 64 + (lane & 7) * 8) * 4);
        const bool on = lane < 8 && b < 240;
        st16<SC1>((u32x4){__float_as_uint(a8[0]), __float_as_uint(a8[1]), __float_as_uint(a8[2]), __float_as_uint(a8[3])}, rm, on ? mo : OOB, 0);
        st16<SC1>((u32x4){__float_as_uint(a8[4]), __float_as_uint(a8[5]), __float_as_uint(a8[6]), __float_as_uint(a8[7])}, rm, on ? mo + 16 : OOB, 0);
      } else {
        touch(0); if (T0N > 1) touch(1);
      }
      lds_wait(lds, 11);
      loader_fill<0, 2, P1_SA>(issue);
      if (T0N < 2) touch(1);
      STAMP(1);
      if (h == 1) {
        if (T0N < 2) wait_vm<9>(); else wait_vm<8>();
        if (arrive_last(lds, 7, 3, lane)) set_flag(ws, WS_FLAGB + g * 128 + j * 8, epoch_tag(Ein), lane);
      }
      loader_run<32, P1_SA, 8, 2, 2, -1>(issue, [&](int i) { if (i == 4) touch(2); if (i == 12) touch(3); });
      BAR(); BAR();
    }
    {
      const unsigned v0 = (lane >> 3) * 1280 + (h ? sw1 : sw0), v1 = (lane >> 3) * 1280 + (h ? sw0 : sw1);
      auto issue = [&](int t, int slot) {
        const int im = t >= 10;
        const rsrc_t r = mkrs((im ? p.wdim : p.wdre) + (size_t)j * 80 * DM, 80 * DM * 2);
        const unsigned k = (unsigned)(t - 10 * im) * 128 + h * 5 * 8 * 1280;
#pragma unroll
        for (int pc = 0; pc < 5; ++pc) dma16<0>(r, lds + slot * 10240 + (h * 5 + pc) * 1024, (pc & 1) ? v1 : v0, k + pc * 8 * 1280);
      };
      loader_fill<0, PF2, P2_RING>(issue);
      STAMP(2);
      loader_run<20, P2_RING, 5, PF2, 2, -1>(issue);
      BAR(); BAR();
    }
    {
      const rsrc_t r = mkrs(ws + WS_WINR + (size_t)j * 20 * 14336, 20 * 14336);
      auto issue = [&](int t, int slot) {
#pragma unroll
        for (int pc = 0; pc < 7; ++pc) dma16<SC1>(r, lds + slot * 14336 + (h * 7 + pc) * 1024, lane * 16, (unsigned)(t * 14336 + (h * 7 + pc) * 1024));
      };
      loader_fill<0, PF3, P3_RING>(issue);
      STAMP(3);
      loader_run<20, P3_RING, 7, PF3, 2, 9>(issue);
      BAR(); BAR();
    }
    {
      const rsrc_t r = mkrs(p.wsp + (size_t)g * 64 * SEQ, 64 * SEQ * 2);
      const unsigned v0 = (lane >> 3) * 4096 + sw0, v1 = (lane >> 3) * 4096 + sw1;
      auto issue = [&](int t, int slot) {
        const unsigned k = (unsigned)((t + rot) & 31) * 128 + h * 4 * 32768;
#pragma unroll
        for (int pc = 0; pc < 4; ++pc) dma16<0>(r, lds + slot * 8192 + (h * 4 + pc) * 1024, (pc & 1) ? v1 : v0, k + pc * 32768);
      };
      loader_fill<0, PF4, P4_SA>(issue);
      STAMP(4);
      loader_run<32, P4_SA, 4, PF4, 2, -1>(issue);
      BAR(); BAR();
    }
    {
      const rsrc_t r = mkrs(ws + WS_WOUTR + (size_t)j * 14 * 10240, 14 * 10240);
      auto issue = [&](int t, int slot) {
#pragma unroll
        for (int pc = 0; pc < 5; ++pc) dma16<SC1>(r, lds + slot * 10240 + (h * 5 + pc) * 1024, lane * 16, (unsigned)(t * 10240 + (h * 5 + pc) * 1024));
      };
      loader_fill<0, PF5, P5_RING>(issue);
      STAMP(5);
      loader_run<14, P5_RING, 5, PF5, 2, -1>(issue);
      BAR();
    }
  } else {
    LANE_IDS();
    const int h = wid - 10;
    wait_lgkm0();
    BAR();
    {
      LAS char* sc = lds + L_RT + h * 10240;
      const int u = b * 2 + h;
      const int qq = l15 >> 2, pp = l15 & 3;
      if (u < 320) {
        const int jp = u / 20, kf = u - 20 * jp, jj = jp >> 1, part = jp & 1;
        const rsrc_t r = mkrs(p.win, DM * FF * 2);
#pragma unroll
        for (int pc = 0; pc < 7; ++pc) {
          const int idx = pc * 64 + lane, row = idx / 14, ch = idx - 14 * row;
          const bool ok = 112 * jj + 8 * ch < FH;
          dma16<0>(r, sc + pc * 1024, ok ? (unsigned)((32 * kf + row) * (FF * 2) + (part ? 0 : FH * 2) + 224 * jj + ch * 16) : OOB, 0);
        }
      } else if (u < 432) {
        const int uu = u - 320, jj = uu / 14, kp = uu - 14 * jj;
        const rsrc_t r = mkrs(p.wout, FH * DM * 2);
#pragma unroll
        for (int pc = 0; pc < 10; ++pc) {
          const int idx = pc * 64 + lane, row = idx / 10, ch = idx - 10 * row;
          dma16<0>(r, sc + pc * 1024, (unsigned)((64 * kp + row) * (DM * 2) + 160 * jj + ch * 16), 0);
        }
      }
      lds_signal(lds, 15 + h, lane);
      wait_vm<0>();
      CFENCE();
      if (u < 320) {
        const int jp = u / 20, kf = u - 20 * jp, jj = jp >> 1, part = jp & 1;
        const rsrc_t ro = mkrs(ws + WS_WINR, 8 * 20 * 14336);
#pragma unroll
        for (int nf = 0; nf < 7; ++nf) {
          LAS char* a0 = sc + (8 * q + qq) * 224 + (16 * nf + 4 * pp) * 2;
          const u32x2 lo = __builtin_bit_cast(u32x2, __builtin_amdgcn_ds_read_tr16_b64_v4i16((LAS s16x4*)a0));
          const u32x2 hi = __builtin_bit_cast(u32x2, __builtin_amdgcn_ds_read_tr16_b64_v4i16((LAS s16x4*)(a0 + 4 * 224)));
          st16<SC1>((u32x4){lo.x, lo.y, hi.x, hi.y}, ro, (unsigned)((((jj * 2 + part) * 20 + kf) * 7 + nf) * 1024 + lane * 16), 0);
        }
      } else if (u < 432) {
        const int uu = u - 320, jj = uu / 14, kp = uu - 14 * jj;
        const rsrc_t ro = mkrs(ws + WS_WOUTR, 8 * 14 * 10240);
#pragma unroll
        for (int f = 0; f < 10; ++f) {
          const int kk = f / 5, nf = f - 5 * kk;
          LAS char* a0 = sc + (32 * kk + 8 * q + qq) * 160 + (16 * nf + 4 * pp) * 2;
          const u32x2 lo = __builtin_bit_cast(u32x2, __builtin_amdgcn_ds_read_tr16_b64_v4i16((LAS s16x4*)a0));
          const u32x2 hi = __builtin_bit_cast(u32x2, __builtin_amdgcn_ds_read_tr16_b64_v4i16((LAS s16x4*)(a0 + 4 * 160)));
          st16<SC1>((u32x4){lo.x, lo.y, hi.x, hi.y}, ro, (unsigned)(((jj * 14 + kp) * 10 + f) * 1024 + lane * 16), 0);
        }
      }
      wait_lgkm0(); CFENCE();
    }
    const u64 T = epoch_tag(Ein);
    STAMP(1);
    {
      if (h == 0) {
        lds_wait(lds, 14);
        __builtin_amdgcn_s_sleep(F0DELAY);
        poll_flags256(ws, WS_FLAG0 + xg * 4096, T, lane);
        lds_signal(lds, 12, lane);
      } else {
        lds_wait(lds, 12);
      }
      STAMP(2);
      const rsrc_t r = mkrs(ws + WS_XNT + (size_t)j * 32 * 10240, 32 * 10240);
      auto issue = [&](int t, int slot) {
        const unsigned k = (unsigned)((t + rot) & 31) * 10240;
#pragma unroll
        for (int pc = 0; pc < 5; ++pc) dma16<SC1>(r, lds + P1_DOFF + slot * 10240 + (h * 5 + pc) * 1024, lane * 16, k + (h * 5 + pc) * 1024);
      };
      loader_fill<0, 1, P1_DB>(issue);
      if (h == 0) lds_signal(lds, 11, lane);
      loader_fill<1, 2, P1_DB>(issue);
      wait_vm<5>();
      if (arrive_last(lds, 7, 3, lane)) set_flag(ws, WS_FLAGB + g * 128 + j * 8, T, lane);
      loader_run<32, P1_DB, 5, 2, 2, -1>(issue, [&](int i) {
        if (i == 20 && h == 1) {
          const int bi = g * 8 + j;
          const rsrc_t r3 = mkrs(p.wdre, DM * DM * 2), r4 = mkrs(p.wdim, DM * DM * 2);
          __builtin_amdgcn_raw_ptr_buffer_load_lds(r3, (LAS void*)(lds + L_SINK), 4, lane < 25 ? (int)((bi * 25 + lane) * 128) : (int)OOB, 0, 0, 0);
          __builtin_amdgcn_raw_ptr_buffer_load_lds(r4, (LAS void*)(lds + L_SINK), 4, lane < 25 ? (int)((bi * 25 + lane) * 128) : (int)OOB, 0, 0, 0);
        }
      });
      BAR(); BAR();
    }
    if (h == 0 && b < 112) {
      const int jj = b / 14, pn = b - 14 * jj, part = pn / 7, nf = pn - 7 * part;
      const rsrc_t rw = mkrs(ws + WS_WINR, 8 * 20 * 14336), rm = mkrs(ws + WS_MODP, 4 * 3840 * 4);
      u32x4 wf[20];
#pragma unroll
      for (int k = 0; k < 20; ++k) wf[k] = ld16<SC1>(rw, lane * 16, (unsigned)((((jj * 2 + part) * 20 + k) * 7 + nf) * 1024));
#pragma unroll
      for (int it = 0; it < 5; ++it) {
        const int c4 = (it * 64 + lane) * 4, isS = c4 >= DM, cc = c4 - DM * isS;
        const unsigned o = (unsigned)(((isS ? 3 : 4) * DM + cc) * 4);
        const f32x4 m0 = __builtin_bit_cast(f32x4, ld16<SC1>(rm, o, 0)), m1 = __builtin_bit_cast(f32x4, ld16<SC1>(rm, o, 3840 * 4)),
                    m2 = __builtin_bit_cast(f32x4, ld16<SC1>(rm, o, 2 * 3840 * 4)), m3 = __builtin_bit_cast(f32x4, ld16<SC1>(rm, o, 3 * 3840 * 4));
        f32x4 s = (m0 + m1) + (m2 + m3);
#pragma unroll
        for (int i = 0; i < 4; ++i) { s[i] = rbf(s[i]); if (!isS) s[i] = 1.0f + s[i]; }
        *(LAS u32x2*)(lds + L_TABE + (isS * DM + cc) * 2) = (u32x2){pk2(s[0], s[1]), pk2(s[2], s[3])};
      }
      wait_lgkm0(); CFENCE();
      f32x4 acc = {0.f, 0.f, 0.f, 0.f};
#pragma unroll
      for (int k = 0; k < 20; ++k) {
        u32x4 e = *(LAS u32x4*)(lds + L_TABE + ((l15 & 1) * DM + 32 * k + 8 * q) * 2);
        if (l15 >= 2) e = (u32x4){0u, 0u, 0u, 0u};
        acc = mfma(asfrag(e), asfrag(wf[k]), acc);
      }
      const rsrc_t ruv = mkrs(ws + WS_UV, 2 * 896 * 16);
      st16<SC1>((u32x4){__float_as_uint(acc[0]), __float_as_uint(acc[1]), (unsigned)T, (unsigned)(T >> 32)}, ruv,
                q == 0 ? (unsigned)((part * 896 + 112 * jj + 16 * nf + l15) * 16) : OOB, 0);
    }
    STAMP(3);
    idle_phase<22>();
    idle_phase<12>();
    if (h == 1) {
      const rsrc_t r1 = mkrs(p.wsp + (size_t)(64 * g + 8 * j) * SEQ, 8 * SEQ * 2);
#pragma unroll
      for (int it = 0; it < 4; ++it)
        __builtin_amdgcn_raw_ptr_buffer_load_lds(r1, (LAS void*)(lds + L_SINK), 4, (int)((lane >> 3) * 4096 + ((it * 8 + (lane & 7) + rot) & 31) * 128), 0, 0, 0);
    }
    idle_phase<11>();
    {
      group_wait(lds, 17, ws, WS_FLAG3 + j * 4096, 32, T, h == 0, lane);
      STAMP(4);
      const rsrc_t r = mkrs(ws + WS_Z2T + (size_t)j * 32 * 14336, 32 * 14336);
      auto issue = [&](int t, int slot) {
        const unsigned k = (unsigned)((t + rot) & 31) * 14336;
#pragma unroll
        for (int pc = 0; pc < 7; ++pc) dma16<SC1>(r, lds + P4_DOFF + slot * 14336 + (h * 7 + pc) * 1024, lane * 16, k + (h * 7 + pc) * 1024);
      };
      loader_fill<0, 2, P4_DB>(issue);
      loader_run<32, P4_DB, 7, 2, 2, -1>(issue);
      BAR(); BAR();
    }
    idle_phase<15>();
  }
}

__global__ void k_report(const char* ws, int wv, int k) {
  __shared__ unsigned long long mn[256], mx[256];
  int t = threadIdx.x;
  mn[t] = *(const unsigned long long*)(ws + WS_STAMP + ((size_t)t * 16 + 0) * 256);
  mx[t] = *(const unsigned long long*)(ws + WS_STAMP + ((size_t)t * 16 + wv) * 256 + k * 8);
  __syncthreads();
  if (t == 0) { unsigned long long a = ~0ull, b = 0; for (int i = 0; i < 256; ++i) { a = mn[i] < a ? mn[i] : a; b = mx[i] > b ? mx[i] : b; }
    unsigned long long span = b - a; if (span > 20000) span = 20000;
    unsigned long long t0 = __builtin_amdgcn_s_memrealtime(); while (__builtin_amdgcn_s_memrealtime() - t0 < span * 10) __builtin_amdgcn_s_sleep(4); }
}
extern "C" void kernel_launch(void* const* d_in, const int* in_sizes, int n_in, void* d_out, int out_size, void* d_ws, size_t ws_size,
                              hipStream_t stream) {
  Params p;
  p.x = (const u16*)d_in[0]; p.wsre = (const u16*)d_in[1]; p.wsim = (const u16*)d_in[2]; p.wdre = (const u16*)d_in[3]; p.wdim = (const u16*)d_in[4];
  p.win = (const u16*)d_in[6]; p.wsp = (const u16*)d_in[7]; p.bsp = (const u16*)d_in[8]; p.wout = (const u16*)d_in[9];
  p.c = (const u16*)d_in[10]; p.wmod = (const u16*)d_in[11];
  p.out = (u16*)d_out; p.ws = (char*)d_ws + ((ws_size - WS_USED) & ~(size_t)0x1fffff);
  (void)hipFuncSetAttribute((const void*)adaLN_cond__spectral_fnet__mixer_gmlp__prompt_kernel, hipFuncAttributeMaxDynamicSharedMemorySize, LDS_BYTES);
  adaLN_cond__spectral_fnet__mixer_gmlp__prompt_kernel<<<256, 768, LDS_BYTES, stream>>>(p);
  k_report<<<1, 256, 0, stream>>>((const char*)p.ws, 0, 2);
}
```

```cpp
#include <hip/hip_runtime.h>
#include <hip/hip_bf16.h>

typedef unsigned short u16;
typedef unsigned long long u64;
typedef short bf16x8 __attribute__((ext_vector_type(8)));
typedef short s16x4 __attribute__((ext_vector_type(4)));
typedef __bf16 bf16x2 __attribute__((ext_vector_type(2)));
typedef float f32x2 __attribute__((ext_vector_type(2)));
typedef float f32x4 __attribute__((ext_vector_type(4)));
typedef unsigned u32x2 __attribute__((ext_vector_type(2)));
typedef unsigned u32x4 __attribute__((ext_vector_type(4)));
typedef __amdgpu_buffer_rsrc_t rsrc_t;
#define LAS __attribute__((address_space(3)))
#define GAS __attribute__((address_space(1)))
#define DEVI __device__ __forceinline__

constexpr int SEQ = 2048, DM = 640, FF = 1728, FH = 864;

constexpr unsigned WS_EPOCH = 0x0;
constexpr unsigned WS_TMO   = 0x100;
constexpr unsigned WS_FLAG0 = 0x1A00000;
constexpr unsigned WS_FLAGB = 0x1A08000;
constexpr unsigned WS_FLAG1 = 0x1A10000;
constexpr unsigned WS_FLAG2 = 0x1A18000;
constexpr unsigned WS_FLAG4 = 0x1A20000;
constexpr unsigned WS_FLAG3 = 0x1A28000;
constexpr unsigned WS_MODP  = 0x10000;
constexpr unsigned WS_UV    = 0x20000;
constexpr unsigned WS_ST1   = 0x40000;
constexpr unsigned WS_ST2   = 0x80000;
constexpr unsigned WS_XNT   = 0x100000;
constexpr unsigned WS_PQ    = 0x400000;
constexpr unsigned WS_X1C   = 0xA00000;
constexpr unsigned WS_Z2T   = 0xD00000;
constexpr unsigned WS_U     = 0x1100000;
constexpr unsigned WS_WINR  = 0x1500000;
constexpr unsigned WS_WOUTR = 0x1800000;
constexpr unsigned WS_STAMP = 0x2400000;
constexpr unsigned WS_USED  = 0x2600000;

constexpr int LDS_BYTES = 163840;
constexpr int L_MISC = 159744;
constexpr int L_CNT  = L_MISC;
constexpr int L_TAB  = L_MISC + 128;
constexpr int L_UV   = L_MISC + 1728;
constexpr int L_SINK = L_MISC + 3584;
constexpr int L_TABE = 153600;
constexpr int L_P0T  = 98304;
constexpr int L_RT   = 118784;
constexpr int P0T_RS = 1296;

#define TH1 0
#define TH2 5
#define TH3 12
#define ASIG 14
#define T0N 1
#define F0DELAY 38
#define PDELAY 4
constexpr unsigned SPIN_MAX = 1u << 17;

struct Params {
  const u16 *x, *wsre, *wsim, *wdre, *wdim, *win, *wsp, *bsp, *wout, *c, *wmod;
  u16* out;
  char* ws;
};

DEVI rsrc_t mkrs(const void* p, unsigned bytes) { return __builtin_amdgcn_make_buffer_rsrc((void*)p, 0, (int)bytes, 0x00020000); }
template <int AUX> DEVI u32x4 ld16(rsrc_t r, unsigned v, unsigned s) { return __builtin_amdgcn_raw_buffer_load_b128(r, (int)v, (int)s, AUX); }
template <int AUX> DEVI u32x2 ld8(rsrc_t r, unsigned v, unsigned s) { return __builtin_amdgcn_raw_buffer_load_b64(r, (int)v, (int)s, AUX); }
template <int AUX> DEVI unsigned ld4(rsrc_t r, unsigned v, unsigned s) { return __builtin_amdgcn_raw_buffer_load_b32(r, (int)v, (int)s, AUX); }
template <int AUX> DEVI unsigned ld2(rsrc_t r, unsigned v, unsigned s) { return (unsigned)__builtin_amdgcn_raw_buffer_load_b16(r, (int)v, (int)s, AUX) & 0xffffu; }
template <int AUX> DEVI void st16(u32x4 d, rsrc_t r, unsigned v, unsigned s) { __builtin_amdgcn_raw_buffer_store_b128(d, r, (int)v, (int)s, AUX); }
template <int AUX> DEVI void st8(u32x2 d, rsrc_t r, unsigned v, unsigned s) { __builtin_amdgcn_raw_buffer_store_b64(d, r, (int)v, (int)s, AUX); }
template <int AUX> DEVI void dma16(rsrc_t r, LAS char* dst, unsigned v, unsigned s) { __builtin_amdgcn_raw_ptr_buffer_load_lds(r, (LAS void*)dst, 16, (int)v, (int)s, 0, AUX); }
constexpr int SC1 = 16;
constexpr unsigned OOB = 0x7ffffff0u;

template <int N> DEVI void wait_vm() { asm volatile("s_waitcnt vmcnt(%0)" ::"n"(N > 63 ? 63 : N) : "memory"); }
DEVI void wait_lgkm0() { __builtin_amdgcn_s_waitcnt(0xC07F); }
DEVI void BAR() { asm volatile("s_barrier" ::: "memory"); }
DEVI void CFENCE() { asm volatile("" ::: "memory"); }
#define SCHED() __builtin_amdgcn_sched_barrier(0)

DEVI float bflo(unsigned v) { return __uint_as_float(v << 16); }
DEVI float bfhi(unsigned v) { return __uint_as_float(v & 0xffff0000u); }
DEVI unsigned pk2(float lo, float hi) { f32x2 f = {lo, hi}; return __builtin_bit_cast(unsigned, __builtin_convertvector(f, bf16x2)); }
DEVI float rbf(float v) { return bflo(pk2(v, 0.f)); }
DEVI f32x4 mfma(bf16x8 a, bf16x8 b, f32x4 c) { return __builtin_amdgcn_mfma_f32_16x16x32_bf16(a, b, c, 0, 0, 0); }
DEVI bf16x8 asfrag(u32x4 v) { return __builtin_bit_cast(bf16x8, v); }
DEVI bf16x8 ldsfrag(LAS char* p) { return *(LAS bf16x8*)p; }

template <int CTRL> DEVI float dpp_add(float v) { return v + __int_as_float(__builtin_amdgcn_update_dpp(0, __float_as_int(v), CTRL, 0xf, 0xf, false)); }
DEVI float row16_sum(float v) { v = dpp_add<0x128>(v); v = dpp_add<0x124>(v); v = dpp_add<0x122>(v); return dpp_add<0x121>(v); }
DEVI f32x2 both16(float v) { auto r = __builtin_amdgcn_permlane16_swap(__float_as_uint(v), __float_as_uint(v), false, false); return (f32x2){__uint_as_float(r[0]), __uint_as_float(r[1])}; }
DEVI f32x2 both32(float v) { auto r = __builtin_amdgcn_permlane32_swap(__float_as_uint(v), __float_as_uint(v), false, false); return (f32x2){__uint_as_float(r[0]), __uint_as_float(r[1])}; }
DEVI float xq_sum(float v) { f32x2 a = both16(v); v = a.x + a.y; a = both32(v); return a.x + a.y; }
DEVI float rsq(float v) { return __builtin_amdgcn_rsqf(v); }
DEVI float wave_sum(float v) { return xq_sum(row16_sum(v)); }

DEVI float gelu_tanh(float x) {
  constexpr float k1 = -2.0f * 0.7978845608028654f * 1.4426950408889634f, k2 = k1 * 0.044715f;
  return x * __builtin_amdgcn_rcpf(1.0f + __builtin_amdgcn_exp2f(x * __builtin_fmaf(x * x, k2, k1)));
}
DEVI f32x2 ln_stats(float s, float q, float inv_n) {
  const float m = s * inv_n, v = __builtin_fmaf(q, inv_n, -m * m);
  return (f32x2){m, __builtin_amdgcn_rsqf((v > 0.f ? v : 0.f) + 1e-5f)};
}

DEVI u32x4 pair_swap(u32x2 a, u32x2 b) {
  auto s0 = __builtin_amdgcn_permlane16_swap(a.x, b.x, false, false);
  auto s1 = __builtin_amdgcn_permlane16_swap(a.y, b.y, false, false);
  return (u32x4){s0[0], s1[0], s0[1], s1[1]};
}

DEVI void note_timeout(char* ws, unsigned code, int lane) {
  if (lane == 0) __hip_atomic_store((GAS unsigned*)(ws + WS_TMO), code, __ATOMIC_RELAXED, __HIP_MEMORY_SCOPE_AGENT);
}
DEVI void poll_flags(char* ws, unsigned off, int n, u64 T, int lane) {
  for (unsigned spins = 0;; ++spins) {
    u64 f = T;
    if (lane < n) f = __hip_atomic_load((GAS u64*)(ws + off + lane * 128), __ATOMIC_RELAXED, __HIP_MEMORY_SCOPE_AGENT);
    if (__all(f == T)) break;
    if (spins > SPIN_MAX) { note_timeout(ws, 0x100u | ((off >> 15) & 0xff), lane); break; }
    __builtin_amdgcn_s_sleep(1);
  }
  CFENCE();
}
DEVI void poll_flags256(char* ws, unsigned off, u64 T, int lane) {
  for (unsigned spins = 0;; ++spins) {
    bool ok = true;
#pragma unroll
    for (int i = 0; i < 4; ++i) ok &= __hip_atomic_load((GAS u64*)(ws + off + (i * 8 + (lane >> 3)) * 128 + (lane & 7) * 8), __ATOMIC_RELAXED, __HIP_MEMORY_SCOPE_AGENT) == T;
    if (__all(ok)) break;
    if (spins > SPIN_MAX) { note_timeout(ws, 0x200u | ((off >> 15) & 0xff), lane); break; }
    __builtin_amdgcn_s_sleep(4);
  }
  CFENCE();
}
DEVI void set_flag(char* ws, unsigned off, u64 T, int lane) {
  if (lane == 0) __hip_atomic_store((GAS u64*)(ws + off), T, __ATOMIC_RELAXED, __HIP_MEMORY_SCOPE_AGENT);
}
DEVI void lds_signal(LAS char* lds, int idx, int lane) {
  CFENCE();
  if (lane == 0) __hip_atomic_store((LAS unsigned*)(lds + L_CNT) + idx, 1u, __ATOMIC_RELAXED, __HIP_MEMORY_SCOPE_WORKGROUP);
}
DEVI void lds_wait(LAS char* lds, int idx) {
  for (unsigned spins = 0; spins < (SPIN_MAX << 6); ++spins) {
    if (__hip_atomic_load((LAS unsigned*)(lds + L_CNT) + idx, __ATOMIC_RELAXED, __HIP_MEMORY_SCOPE_WORKGROUP) != 0u) break;
    __builtin_amdgcn_s_sleep(1);
  }
  CFENCE();
}
DEVI void lds_wait_n(LAS char* lds, int idx, unsigned n) {
  for (unsigned spins = 0; spins < (SPIN_MAX << 6); ++spins) {
    if (__hip_atomic_load((LAS unsigned*)(lds + L_CNT) + idx, __ATOMIC_RELAXED, __HIP_MEMORY_SCOPE_WORKGROUP) >= n) break;
    __builtin_amdgcn_s_sleep(1);
  }
  CFENCE();
}
DEVI void group_wait(LAS char* lds, int idx, char* ws, unsigned off, int n, u64 T, bool poller, int lane) {
  if (poller) { __builtin_amdgcn_s_sleep(PDELAY); poll_flags(ws, off, n, T, lane); lds_signal(lds, idx, lane); } else lds_wait(lds, idx);
}
DEVI bool arrive_last(LAS char* lds, int idx, unsigned n, int lane) {
  unsigned old = 0;
  if (lane == 0) old = __hip_atomic_fetch_add((LAS unsigned*)(lds + L_CNT) + idx, 1u, __ATOMIC_RELAXED, __HIP_MEMORY_SCOPE_WORKGROUP);
  return (unsigned)__builtin_amdgcn_readfirstlane(old) == n - 1;
}
DEVI void publish(LAS char* lds, int cidx, unsigned nwaves, char* ws, unsigned flag_off, u64 T, int lane) {
  wait_vm<0>();
  if (arrive_last(lds, cidx, nwaves, lane)) set_flag(ws, flag_off, T, lane);
}

#define LANE_IDS() int tz_ = threadIdx.x; asm volatile("" : "+v"(tz_)); const int lane = tz_ & 63, l15 = lane & 15, q = lane >> 4; (void)l15; (void)q

#define LANE_IDS2() int tz2_ = threadIdx.x; asm volatile("" : "+v"(tz2_)); const int laneb = tz2_ & 63, l15b = laneb & 15, qb = laneb >> 4; (void)l15b; (void)qb

#define STAMP(k) do { } while (0)
#define STAMPC(k) do { } while (0)

template <int NT, int RING, int P0, int CU> constexpr int issued(int i) {
  int n = P0 < NT ? P0 : NT;
  for (int k = 0; k <= i; ++k) { int m = n + CU; if (m > k + RING) m = k + RING; if (m > NT) m = NT; n = m; }
  return n;
}
constexpr int cap63(int n, int pc) { return n > 63 - pc ? 63 - pc : n; }
template <int I, int IEND, int NT, int RING, int PC, int P0, int CU, int XI, class Issue, class Hook> DEVI void loader_steps(Issue& issue, Hook& hook) {
  if constexpr (I < IEND) {
    constexpr int before = issued<NT, RING, P0, CU>(I - 1), after = issued<NT, RING, P0, CU>(I);
    wait_vm<cap63((before - (I + 2)) * PC, (after - before) * PC)>(); BAR();
    if constexpr (after > before) issue(before, before % RING);
    if constexpr (after > before + 1) issue(before + 1, (before + 1) % RING);
    if constexpr (after > before + 2) issue(before + 2, (before + 2) % RING);
    static_assert(after <= before + 3, "at most three tiles per step");
    hook(I);
    if constexpr (I == XI) BAR();
    loader_steps<I + 1, IEND, NT, RING, PC, P0, CU, XI>(issue, hook);
  }
}
template <int NT, int RING, int P0, int CU> constexpr int steady_from() {
  for (int i = 0; i < NT; ++i) if (issued<NT, RING, P0, CU>(i - 1) == i - 1 + RING) return i;
  return NT;
}
template <int T0, int T1, int RING, class Issue> DEVI void loader_fill(Issue& issue) {
#pragma unroll 1
  for (int t = T0; t < T1; ++t) issue(t, t);
}
struct NoHook { DEVI void operator()(int) const {} };
template <int NT, int RING, int PC, int P0, int CU, int XI, class Issue, class Hook = NoHook> DEVI void loader_run(Issue& issue, Hook hook = Hook()) {
  constexpr int P = P0 < NT ? P0 : NT;
  wait_vm<cap63((P - 1) * PC, 0)>(); BAR();
  constexpr int S0 = steady_from<NT, RING, P0, CU>(), S1 = NT - RING + 1;
  if constexpr (XI < 0 && S0 + 2 < S1) {
    loader_steps<0, S0, NT, RING, PC, P0, CU, XI>(issue, hook);
    int slot = (S0 - 1 + RING) % RING;
#pragma unroll 1
    for (int i = S0; i < S1; ++i) {
      wait_vm<cap63((RING - 3) * PC, PC)>(); BAR();
      issue(i - 1 + RING, slot);
      hook(i);
      slot = (slot + 1 == RING) ? 0 : slot + 1;
    }
    loader_steps<S1, NT - 1, NT, RING, PC, P0, CU, XI>(issue, hook);
  } else {
    loader_steps<0, NT - 1, NT, RING, PC, P0, CU, XI>(issue, hook);
  }
}
template <int NBAR> DEVI void idle_phase() {
#pragma unroll 1
  for (int i = 0; i < NBAR; ++i) BAR();
}

template <int I, int NMF, int NRD, int PER> struct Interleave {
  static DEVI void run() {
    if constexpr (I < NMF) {
      __builtin_amdgcn_sched_group_barrier(0x8, 1, 0);
      constexpr int left = NRD - I * PER, now = left < PER ? left : PER;
      if constexpr (now > 0) __builtin_amdgcn_sched_group_barrier(0x100, now, 0);
      Interleave<I + 1, NMF, NRD, PER>::run();
    }
  }
};
template <int NMF, int NRD, int PER> DEVI void interleave() { Interleave<0, NMF, NRD, PER>::run(); }

#define AW 4
#define PF2 15
#define PF3 8
#define PF4 9
#define PF5 14
constexpr int P1_SA = 6, P1_DB = 5, P1_DOFF = 98304, P1_X = 32768;
constexpr int P2_RING = 15, P2_X = 51200;
constexpr int P3_RING = 8, P3_X = 114688;
constexpr int P4_SA = 9, P4_DB = 5, P4_DOFF = 73728, P4_X = 40960;
constexpr int L_X1 = 145408;
constexpr int P5_RING = 14, P5_X = 0;

DEVI f32x4 sel4(int c, f32x4 a, f32x4 b) { asm volatile("" : "+v"(a), "+v"(b)); return c ? a : b; }
DEVI float sel1(int c, float a, float b) { asm volatile("" : "+v"(a), "+v"(b)); return c ? a : b; }
DEVI void lds_put(LAS char* p, f32x4 v) { *(LAS f32x4*)p = v; }
DEVI f32x4 lds_get(LAS char* p) { return *(LAS f32x4*)p; }
DEVI u64 epoch_tag(u64 e) {
  return (((u64)(unsigned)__builtin_amdgcn_readfirstlane((unsigned)(e >> 32)) << 32) | (unsigned)__builtin_amdgcn_readfirstlane((unsigned)e)) + 1;
}
DEVI void chan(float& n, float& m, float& M2, float no, float mo, float Mo) {
  const float nt = n + no, d = mo - m, r = __builtin_amdgcn_rcpf(nt);
  M2 = M2 + Mo + d * d * (n * no * r);
  m = m + d * (no * r);
  n = nt;
}

__global__ __launch_bounds__(768, 3) void adaLN_cond__spectral_fnet__mixer_gmlp__prompt_kernel(Params p) {
  extern __shared__ __attribute__((aligned(16))) char smem_[];
  LAS char* lds = (LAS char*)smem_;
  const int tid = threadIdx.x, wid = __builtin_amdgcn_readfirstlane(tid >> 6);
  const int b = blockIdx.x, xg = b & 7, i8 = b >> 3;
  const int g = xg * 4 + (i8 >> 3), j = i8 & 7;
  char* const ws = p.ws;
  const int rot = xg * 4;

  { LANE_IDS(); STAMP(0); STAMPC(30); }
  const u64 Ein = __hip_atomic_load((GAS u64*)(ws + WS_EPOCH), __ATOMIC_RELAXED, __HIP_MEMORY_SCOPE_AGENT);
  if (tid < 32) *((LAS unsigned*)(lds + L_CNT) + tid) = 0u;

  if (wid < 8) {
    const int w = wid, m = w & 3, kh = w >> 2;
    __builtin_amdgcn_s_setprio(2);
    {
      LANE_IDS();
      const rsrc_t rx = mkrs(p.x, SEQ * DM * 2);
      u32x4 v[2];
#pragma unroll
      for (int h = 0; h < 2; ++h) v[h] = ld16<0>(rx, lane < 40 ? (unsigned)((lane + 40 * h) * 16) : OOB, (unsigned)(64 * g + 8 * j + w) * (DM * 2));
      wait_lgkm0();
      BAR();
      float f[16];
#pragma unroll
      for (int h = 0; h < 2; ++h)
#pragma unroll
        for (int i = 0; i < 4; ++i) { f[h * 8 + 2 * i] = bflo(v[h][i]); f[h * 8 + 2 * i + 1] = bfhi(v[h][i]); }
      float s = 0.f, sq = 0.f;
#pragma unroll
      for (int i = 0; i < 16; ++i) { s += f[i]; sq = __builtin_fmaf(f[i], f[i], sq); }
      const f32x2 ls = ln_stats(wave_sum(s), wave_sum(sq), 1.0f / DM);
      const float rstd = ls.y, c0 = -ls.x * ls.y;
      if (lane < 40) {
#pragma unroll
        for (int h = 0; h < 2; ++h) {
          u32x4 o;
#pragma unroll
          for (int i = 0; i < 4; ++i) o[i] = pk2(__builtin_fmaf(f[h * 8 + 2 * i], rstd, c0), __builtin_fmaf(f[h * 8 + 2 * i + 1], rstd, c0));
          *(LAS u32x4*)(lds + L_P0T + w * P0T_RS + (lane + 40 * h) * 16) = o;
        }
      }
      wait_lgkm0(); CFENCE();
      if (lane == 0) __hip_atomic_fetch_add((LAS unsigned*)(lds + L_CNT) + 0, 1u, __ATOMIC_RELAXED, __HIP_MEMORY_SCOPE_WORKGROUP);
      for (unsigned spins = 0; spins < SPIN_MAX; ++spins) {
        if (__hip_atomic_load((LAS unsigned*)(lds + L_CNT) + 0, __ATOMIC_RELAXED, __HIP_MEMORY_SCOPE_WORKGROUP) >= 8u) break;
      }
      CFENCE();
      const rsrc_t rxn = mkrs(ws + WS_XNT, 8 * 32 * 10240);
#pragma unroll
      for (int it = 0; it < 2; ++it) {
        const int pc = it == 0 ? q : 4, nf = 5 * w + pc;
        const int qq = l15 >> 2, pp = l15 & 3;
        LAS char* a0 = lds + L_P0T + qq * P0T_RS + (16 * nf + 4 * pp) * 2;
        const u32x2 lo = __builtin_bit_cast(u32x2, __builtin_amdgcn_ds_read_tr16_b64_v4i16((LAS s16x4*)a0));
        const u32x2 hi = __builtin_bit_cast(u32x2, __builtin_amdgcn_ds_read_tr16_b64_v4i16((LAS s16x4*)(a0 + 4 * P0T_RS)));
        const unsigned off = (unsigned)(((w * 32 + g) * 10 + (j >> 2) * 5 + pc) * 1024 + ((j & 3) * 16 + l15) * 16);
        st16<SC1>((u32x4){lo.x, lo.y, hi.x, hi.y}, rxn, (it == 0 || q == 0) ? off : OOB, 0);
      }
    }
    { LANE_IDS(); if (lane == 0) __hip_atomic_fetch_add((LAS unsigned*)(lds + L_CNT) + 13, 1u, __ATOMIC_RELAXED, __HIP_MEMORY_SCOPE_WORKGROUP); }
    const u64 T = epoch_tag(Ein);
    { LANE_IDS(); wait_vm<0>(); if (arrive_last(lds, 1, 8, lane)) {
      if (lane < 8) __hip_atomic_store((GAS u64*)(ws + WS_FLAG0 + lane * 4096 + g * 128 + j * 8), T, __ATOMIC_RELAXED, __HIP_MEMORY_SCOPE_AGENT);
      lds_signal(lds, 14, lane); } STAMP(1); }

    unsigned z1p[8];

    {
      LANE_IDS();
      f32x4 acc[2][5];
#pragma unroll
      for (int a = 0; a < 2; ++a)
#pragma unroll
        for (int n = 0; n < 5; ++n) acc[a][n] = (f32x4){0.f, 0.f, 0.f, 0.f};
      f32x4 rs[2] = {{0.f, 0.f, 0.f, 0.f}, {0.f, 0.f, 0.f, 0.f}};
      u32x4 ones4 = {0x3f803f80u, 0x3f803f80u, 0x3f803f80u, 0x3f803f80u}; asm volatile("" : "+v"(ones4));
      const bf16x8 ones = asfrag(ones4);
      const unsigned aoff = (m * 16 + l15) * 128 + (((kh * 4 + q) ^ (l15 >> 1)) << 4);
      const unsigned boff = P1_DOFF + kh * 5120 + lane * 16;
      bf16x8 XA[2], XB[5], YA[2], YB[5];
      int sa = 0, sb = 0;
      auto rd = [&](bf16x8(&A)[2], bf16x8(&B)[5]) {
        LAS char* pa = lds + sa * 16384 + aoff;
        LAS char* pb = lds + sb * 10240 + boff;
        A[0] = ldsfrag(pa); A[1] = ldsfrag(pa + 8192);
#pragma unroll
        for (int n = 0; n < 5; ++n) B[n] = ldsfrag(pb + n * 1024);
        sa = (sa + 1 == P1_SA) ? 0 : sa + 1; sb = (sb + 1 == P1_DB) ? 0 : sb + 1;
      };
      auto mm = [&](const bf16x8(&A)[2], const bf16x8(&B)[5]) {
#pragma unroll
        for (int a = 0; a < 2; ++a) {
#pragma unroll
          for (int n = 0; n < 5; ++n) acc[a][n] = mfma(B[n], A[a], acc[a][n]);
          rs[a] = mfma(ones, A[a], rs[a]);
        }
      };
      auto pairs = [&](int n) {
#pragma unroll 1
        for (int i = 0; i < n; ++i) {
          BAR(); rd(YA, YB); mm(XA, XB); interleave<12, 7, 1>(); SCHED();
          BAR(); rd(XA, XB); mm(YA, YB); interleave<12, 7, 1>(); SCHED();
        }
      };
      BAR();
      STAMP(2); STAMPC(28);
      rd(XA, XB);
      pairs(6);
      u64 fb[4];
      if (w == 0) {
#pragma unroll
        for (int i = 0; i < 4; ++i) fb[i] = __hip_atomic_load((GAS u64*)(ws + WS_FLAGB + (i * 8 + (lane >> 3)) * 128 + (lane & 7) * 8), __ATOMIC_RELAXED, __HIP_MEMORY_SCOPE_AGENT);
      }
      pairs(3);
      f32x4 mp[2][4];
      const int vs0 = lane < 20 ? 1 : lane < 40 ? 0 : 2, vs1 = lane < 20 ? 4 : 5;
      const int vl = lane < 20 ? lane : lane < 40 ? lane - 20 : lane - 40;
      if (w == 0) {
        if (!__all(fb[0] == T && fb[1] == T && fb[2] == T && fb[3] == T)) poll_flags256(ws, WS_FLAGB, T, lane);
        CFENCE();
        const rsrc_t rm = mkrs(ws + WS_MODP, 4 * 3840 * 4);
#pragma unroll
        for (int k = 0; k < 4; ++k) {
          mp[0][k] = __builtin_bit_cast(f32x4, ld16<SC1>(rm, lane < 60 ? (unsigned)((vs0 * DM + 80 * j + vl * 4) * 4) : OOB, k * 3840 * 4));
          mp[1][k] = __builtin_bit_cast(f32x4, ld16<SC1>(rm, lane < 40 ? (unsigned)((vs1 * DM + 80 * j + vl * 4) * 4) : OOB, k * 3840 * 4));
        }
      }
      pairs(3);
      if (w == 0) {
        f32x4 s0 = (mp[0][0] + mp[0][1]) + (mp[0][2] + mp[0][3]), s1 = (mp[1][0] + mp[1][1]) + (mp[1][2] + mp[1][3]);
#pragma unroll
        for (int i = 0; i < 4; ++i) {
          s0[i] = rbf(s0[i]); if (lane < 20) s0[i] = rbf(1.0f + s0[i]);
          s1[i] = rbf(s1[i]); if (lane < 20) s1[i] = rbf(1.0f + s1[i]);
        }
        if (lane < 60) lds_put(lds + L_TAB + ((lane < 20 ? 0 : lane < 40 ? 1 : 2) * 80 + vl * 4) * 4, s0);
        if (lane < 40) lds_put(lds + L_TAB + ((lane < 20 ? 3 : 4) * 80 + vl * 4) * 4, s1);
      }
      pairs(3);
      BAR(); rd(YA, YB); mm(XA, XB); interleave<12, 7, 1>(); SCHED();
      mm(YA, YB);
      STAMP(3); STAMPC(29);
      {
        LAS char* xo = lds + P1_X + w * 6144 + lane * 16;
        LAS char* xi = lds + P1_X + (w ^ 4) * 6144 + lane * 16;
#pragma unroll
        for (int n = 0; n < 5; ++n) lds_put(xo + n * 1024, sel4(kh, acc[0][n], acc[1][n]));
        *(LAS float*)(lds + P1_X + w * 6144 + 5120 + lane * 4) = sel1(kh, rs[0][0], rs[1][0]);
        wait_lgkm0();
        BAR();
        const float rsum = sel1(kh, rs[1][0], rs[0][0]) + *(LAS float*)(lds + P1_X + (w ^ 4) * 6144 + 5120 + lane * 4);
        u32x2 pkd[5];
#pragma unroll
        for (int n = 0; n < 5; ++n) {
          const f32x4 a = sel4(kh, acc[1][n], acc[0][n]) + lds_get(xi + n * 1024);
          const f32x4 c1 = lds_get(lds + L_TAB + (0 * 80 + 16 * n + 4 * q) * 4), sa4 = lds_get(lds + L_TAB + (1 * 80 + 16 * n + 4 * q) * 4);
          f32x4 v = a * c1 + rsum * sa4;
          if (kh) v = -v;
          pkd[n] = (u32x2){pk2(v[0], v[1]), pk2(v[2], v[3])};
        }
        const rsrc_t rpq = mkrs(ws + WS_PQ, 32 * 40 * 4 * 1024);
        auto pq_off = [&](int nf) { const int c = 10 * j + 2 * nf + (q >> 1); return (unsigned)(((g * 40 + kh * 20 + (c >> 2)) * 4 + m) * 1024 + ((c & 3) * 16 + l15) * 16); };
        const int odd = q & 1;
        st16<SC1>(pair_swap(pkd[0], pkd[1]), rpq, pq_off(odd), 0);
        st16<SC1>(pair_swap(pkd[2], pkd[3]), rpq, pq_off(2 + odd), 0);
        st8<SC1>(pkd[4], rpq, pq_off(4) + odd * 8, 0);
      }
      BAR();
      publish(lds, 3, 8, ws, WS_FLAG1 + (g * 8 + j) * 128, T, lane);
      STAMP(4);
    }

    {
      LANE_IDS();
      constexpr int NT = 20;
      const int nb = kh ? 3 : 0;
      group_wait(lds, 8, ws, WS_FLAG1 + g * 1024, 8, T, w == 0, lane);
      STAMP(5);
      const rsrc_t ra = mkrs(ws + WS_PQ + (size_t)g * 40 * 4 * 1024, 40 * 4 * 1024);
      u32x4 A[NT];
#pragma unroll
      for (int t = 0; t < AW; ++t) A[t] = ld16<SC1>(ra, lane * 16, ((2 * t + kh) * 4 + m) * 1024);
      f32x4 acc[5];
#pragma unroll
      for (int n = 0; n < 5; ++n) acc[n] = (f32x4){0.f, 0.f, 0.f, 0.f};
      const unsigned boff = l15 * 128 + (((kh * 4 + q) ^ (l15 >> 1)) << 4);
      bf16x8 B[2][5];
      auto rd = [&](bf16x8(&Bv)[5], int slot) {
#pragma unroll
        for (int n = 0; n < 5; ++n) Bv[n] = ldsfrag(lds + slot * 10240 + boff + n * 2048);
      };
      BAR();
      STAMP(6);
      rd(B[0], 0);
#pragma unroll
      for (int t = 0; t < NT; ++t) {
        if (t + 1 < NT) { BAR(); rd(B[(t + 1) & 1], (t + 1) % P2_RING); }
        if (t + AW < NT) A[t + AW] = ld16<SC1>(ra, lane * 16, ((2 * (t + AW) + kh) * 4 + m) * 1024);
#pragma unroll
        for (int n = 0; n < 5; ++n) acc[n] = mfma(B[t & 1][n], asfrag(A[t]), acc[n]);
        interleave<5, 5, 1>(); SCHED();
      }
      STAMP(7);
      LANE_IDS2();
      const rsrc_t rx = mkrs(p.x, SEQ * DM * 2);
      u32x2 xin[3];
#pragma unroll
      for (int n = 0; n < 3; ++n) xin[n] = ld8<0>(rx, (kh && n == 2) ? OOB : (unsigned)((64 * g + 16 * m + l15b) * (DM * 2) + (80 * j + 16 * (nb + n) + 4 * qb) * 2), 0);
      LAS char* xo = lds + P2_X + w * 3072 + lane * 16;
      LAS char* xi = lds + P2_X + (w ^ 4) * 3072 + lane * 16;
      if (kh) { lds_put(xo, acc[0]); lds_put(xo + 1024, acc[1]); lds_put(xo + 2048, acc[2]); }
      else { lds_put(xo, acc[3]); lds_put(xo + 1024, acc[4]); }
      wait_lgkm0();
      BAR();
      float s = 0.f, sq = 0.f;
      u32x2 cp[3]; unsigned x1p[6];
      auto fin = [&](int n, f32x4 own) {
        const f32x4 y = own + lds_get(xi + n * 1024);
        const int col = 16 * (nb + n) + 4 * q;
        const f32x4 ga = lds_get(lds + L_TAB + (2 * 80 + col) * 4), cb1 = lds_get(lds + L_TAB + (3 * 80 + col) * 4);
        const f32x4 xv = {bflo(xin[n].x), bfhi(xin[n].x), bflo(xin[n].y), bfhi(xin[n].y)};
        const f32x4 x1 = xv + ga * y, c = x1 * cb1;
        x1p[2 * n] = pk2(x1[0], x1[1]); x1p[2 * n + 1] = pk2(x1[2], x1[3]);
        cp[n] = (u32x2){pk2(c[0], c[1]), pk2(c[2], c[3])};
        s += (x1[0] + x1[1]) + (x1[2] + x1[3]);
        sq = __builtin_fmaf(x1[0], x1[0], __builtin_fmaf(x1[1], x1[1], __builtin_fmaf(x1[2], x1[2], __builtin_fmaf(x1[3], x1[3], sq))));
      };
      if (kh) { fin(0, acc[3]); fin(1, acc[4]); cp[2] = (u32x2){0u, 0u}; x1p[4] = 0u; x1p[5] = 0u; }
      else { fin(0, acc[0]); fin(1, acc[1]); fin(2, acc[2]); }
      s = xq_sum(s); sq = xq_sum(sq);
      const rsrc_t rc = mkrs(ws + WS_X1C, 32 * 20 * 4 * 1024);
      auto c_off = [&](int nf) { const int c = 10 * j + 2 * nf + (q >> 1); return (unsigned)(((g * 20 + (c >> 2)) * 4 + m) * 1024 + ((c & 3) * 16 + l15) * 16); };
      const int odd = q & 1;
      st16<SC1>(pair_swap(cp[0], cp[1]), rc, c_off(nb + odd), 0);
      st8<SC1>(cp[2], rc, kh ? OOB : c_off(2) + odd * 8, 0);
      const rsrc_t rs1 = mkrs(ws + WS_ST1, SEQ * 16 * 8);
      st8<SC1>((u32x2){__float_as_uint(s), __float_as_uint(sq)}, rs1, q == 0 ? (unsigned)(((64 * g + 16 * m + l15) * 16 + 2 * j + kh) * 8) : OOB, 0);
#pragma unroll
      for (int n = 0; n < 3; ++n) *(LAS u32x2*)(lds + L_X1 + (w * 3 + n) * 512 + lane * 8) = (u32x2){x1p[2 * n], x1p[2 * n + 1]};
      wait_lgkm0();
      BAR();
      publish(lds, 4, 8, ws, WS_FLAG2 + (g * 8 + j) * 128, T, lane);
      STAMP(8);
    }

    {
      LANE_IDS();
      constexpr int NT = 10;
      const int nb = kh ? 4 : 0, nn = kh ? 3 : 4;
      {
        const rsrc_t ruv = mkrs(ws + WS_UV, 2 * 896 * 16);
        const int col = w * 28 + lane, part = col >= 112 ? 1 : 0, cc = col - 112 * part;
        const unsigned off = lane < 28 ? (unsigned)((part * 896 + 112 * j + cc) * 16) : OOB;
        u32x4 gv;
        for (unsigned spins = 0;; ++spins) {
          gv = ld16<SC1>(ruv, off, 0);
          const bool ok = lane >= 28 || (gv.z == (unsigned)T && gv.w == (unsigned)(T >> 32));
          if (__all(ok)) break;
          if (spins > SPIN_MAX) { note_timeout(ws, 0x300, lane); break; }
          __builtin_amdgcn_s_sleep(1); CFENCE();
        }
        if (lane < 28) { *(LAS unsigned*)(lds + L_UV + col * 4) = gv.x; *(LAS unsigned*)(lds + L_UV + (224 + col) * 4) = gv.y; }
      }
      group_wait(lds, 9, ws, WS_FLAG2 + g * 1024, 8, T, w == 0, lane);
      STAMP(9);
      const rsrc_t ra = mkrs(ws + WS_X1C + (size_t)g * 20 * 4 * 1024, 20 * 4 * 1024);
      u32x4 A[NT];
#pragma unroll
      for (int t = 0; t < AW; ++t) A[t] = ld16<SC1>(ra, lane * 16, ((2 * t + kh) * 4 + m) * 1024);
      const rsrc_t rs1 = mkrs(ws + WS_ST1, SEQ * 16 * 8);
      const unsigned s1o = (unsigned)(((64 * g + 16 * m + l15) * 16 + 4 * q) * 8);
      const u32x4 st0 = ld16<SC1>(rs1, s1o, 0), st1 = ld16<SC1>(rs1, s1o + 16, 0);
      f32x4 acc[7];
      bf16x8 B[2][7];
      auto rd = [&](bf16x8(&Bv)[7], int slot) {
#pragma unroll
        for (int n = 0; n < 7; ++n) Bv[n] = ldsfrag(lds + slot * 14336 + kh * 7168 + n * 1024 + lane * 16);
      };
      wait_lgkm0();
      BAR();
      STAMP(10);
      rd(B[0], 0); wait_lgkm0();
      float mean1, rstd1;
      {
        const float sm = xq_sum((__uint_as_float(st0.x) + __uint_as_float(st0.z)) + (__uint_as_float(st1.x) + __uint_as_float(st1.z)));
        const float sq = xq_sum((__uint_as_float(st0.y) + __uint_as_float(st0.w)) + (__uint_as_float(st1.y) + __uint_as_float(st1.w)));
        const f32x2 ls = ln_stats(sm, sq, 1.0f / DM);
        mean1 = ls.x; rstd1 = ls.y;
      }
      LAS char* xo = lds + P3_X + (kh ? 12288 + m * 4096 : m * 3072) + lane * 16;
      LAS char* xi = lds + P3_X + (kh ? m * 3072 : 12288 + m * 4096) + lane * 16;
#pragma unroll
      for (int n = 0; n < 7; ++n) acc[n] = (f32x4){0.f, 0.f, 0.f, 0.f};
#pragma unroll
      for (int t = 0; t < NT; ++t) {
        BAR(); if (t + 1 < NT) rd(B[(t + 1) & 1], (t + 1) % P3_RING);
        if (t + AW < NT) A[t + AW] = ld16<SC1>(ra, lane * 16, ((2 * (t + AW) + kh) * 4 + m) * 1024);
#pragma unroll
        for (int n = 0; n < 7; ++n) acc[n] = mfma(asfrag(A[t]), B[t & 1][n], acc[n]);
        interleave<7, 7, 1>(); SCHED();
      }
      STAMP(25);
      if (kh) {
#pragma unroll
        for (int n = 0; n < 4; ++n) lds_put(xo + n * 1024, acc[n]);
      } else {
#pragma unroll
        for (int n = 0; n < 3; ++n) lds_put(xo + n * 1024, acc[4 + n]);
      }
      wait_lgkm0();
      BAR();
      u32x2 z2p[4];
      {
        float rr[4], c1[4];
#pragma unroll
        for (int r = 0; r < 4; ++r) { rr[r] = __shfl(rstd1, 4 * q + r); c1[r] = __shfl(-rstd1 * mean1, 4 * q + r); }
        float zs[4] = {0.f, 0.f, 0.f, 0.f}, zq[4] = {0.f, 0.f, 0.f, 0.f};
        auto fin = [&](int n, f32x4 own) {
          const f32x4 a = own + lds_get(xi + n * 1024);
          const int col = 16 * (nb + n) + l15;
          const float u = *(LAS float*)(lds + L_UV + col * 4), v = *(LAS float*)(lds + L_UV + (224 + col) * 4);
          float z[4];
#pragma unroll
          for (int r = 0; r < 4; ++r) {
            z[r] = gelu_tanh(__builtin_fmaf(rr[r], a[r], __builtin_fmaf(c1[r], u, v)));
            zs[r] += z[r]; zq[r] = __builtin_fmaf(z[r], z[r], zq[r]);
          }
          z2p[n] = (u32x2){pk2(z[0], z[1]), pk2(z[2], z[3])};
        };
        if (kh) { fin(0, acc[4]); fin(1, acc[5]); fin(2, acc[6]); z2p[3] = (u32x2){0u, 0u}; }
        else { fin(0, acc[0]); fin(1, acc[1]); fin(2, acc[2]); fin(3, acc[3]); }
#pragma unroll
        for (int r = 0; r < 4; ++r) { zs[r] = row16_sum(zs[r]); zq[r] = row16_sum(zq[r]); }
        const float ms = l15 == 0 ? zs[0] : l15 == 1 ? zs[1] : l15 == 2 ? zs[2] : zs[3];
        const float Ms = l15 == 0 ? zq[0] : l15 == 1 ? zq[1] : l15 == 2 ? zq[2] : zq[3];
        const rsrc_t rs2 = mkrs(ws + WS_ST2, SEQ * 16 * 16);
        st16<SC1>((u32x4){__float_as_uint(ms), __float_as_uint(Ms), (unsigned)T, (unsigned)(T >> 32)}, rs2,
                  l15 < 4 ? (unsigned)(((64 * g + 16 * m + 4 * q + l15) * 16 + 2 * j + kh) * 16) : OOB, 0);
      }
      STAMP(11);
      rd(B[0], NT % P3_RING); wait_lgkm0();
      const rsrc_t rs2 = mkrs(ws + WS_ST2, SEQ * 16 * 16);
      const unsigned soff = (unsigned)(((64 * g + 16 * m + l15) * 16 + 4 * q) * 16);
      u32x4 gs[4];
#pragma unroll
      for (int n = 0; n < 7; ++n) acc[n] = (f32x4){0.f, 0.f, 0.f, 0.f};
#pragma unroll
      for (int t = 0; t < NT; ++t) {
        if (t + 1 < NT) { BAR(); rd(B[(t + 1) & 1], (NT + t + 1) % P3_RING); }
        if (t == 7) {
#pragma unroll
          for (int i = 0; i < 4; ++i) gs[i] = ld16<SC1>(rs2, soff + 16 * i, 0);
        }
#pragma unroll
        for (int n = 0; n < 7; ++n) acc[n] = mfma(B[t & 1][n], asfrag(A[t]), acc[n]);
        interleave<7, 7, 1>(); SCHED();
      }
      STAMP(12);
      for (unsigned spins = 0;; ++spins) {
        bool ok = true;
#pragma unroll
        for (int i = 0; i < 4; ++i) ok &= gs[i].z == (unsigned)T && gs[i].w == (unsigned)(T >> 32);
        if (__all(ok)) break;
        if (spins > SPIN_MAX) { note_timeout(ws, 0x400, lane); break; }
        __builtin_amdgcn_s_sleep(1); CFENCE();
#pragma unroll
        for (int i = 0; i < 4; ++i) gs[i] = ld16<SC1>(rs2, soff + 16 * i, 0);
      }
      {
        const float sm = xq_sum((__uint_as_float(gs[0].x) + __uint_as_float(gs[1].x)) + (__uint_as_float(gs[2].x) + __uint_as_float(gs[3].x)));
        const float sq = xq_sum((__uint_as_float(gs[0].y) + __uint_as_float(gs[1].y)) + (__uint_as_float(gs[2].y) + __uint_as_float(gs[3].y)));
        const f32x2 ls = ln_stats(sm, sq, 1.0f / FH);
        float rr[4], c2[4];
#pragma unroll
        for (int r = 0; r < 4; ++r) { rr[r] = __shfl(ls.y, 4 * q + r); c2[r] = __shfl(-ls.x * ls.y, 4 * q + r); }
        u32x2 zn[4];
#pragma unroll
        for (int n = 0; n < 4; ++n)
          zn[n] = (u32x2){pk2(__builtin_fmaf(bflo(z2p[n].x), rr[0], c2[0]), __builtin_fmaf(bfhi(z2p[n].x), rr[1], c2[1])),
                          pk2(__builtin_fmaf(bflo(z2p[n].y), rr[2], c2[2]), __builtin_fmaf(bfhi(z2p[n].y), rr[3], c2[3]))};
        const rsrc_t rz2 = mkrs(ws + WS_Z2T, 8 * 32 * 14336);
        auto z_off = [&](int nf) { return (unsigned)(((j * 32 + g) * 14 + (m >> 1) * 7 + nf) * 1024 + ((2 * (m & 1) + (q >> 1)) * 16 + l15) * 16); };
        const int odd = q & 1;
        st16<SC1>(pair_swap(zn[0], zn[1]), rz2, z_off(nb + odd), 0);
        if (kh) st8<SC1>(zn[2], rz2, z_off(6) + odd * 8, 0);
        else st16<SC1>(pair_swap(zn[2], zn[3]), rz2, z_off(2 + odd), 0);
      }
      STAMP(27);
      BAR();
      publish(lds, 5, 8, ws, WS_FLAG3 + (j * 32 + g) * 128, T, lane);
      if (kh) {
#pragma unroll
        for (int n = 0; n < 4; ++n) lds_put(xo + n * 1024, acc[n]);
      } else {
#pragma unroll
        for (int n = 0; n < 3; ++n) lds_put(xo + n * 1024, acc[4 + n]);
      }
      wait_lgkm0();
      BAR();
      {
        const float c1 = -rstd1 * mean1;
        auto fin = [&](int n, f32x4 own) {
          const f32x4 a = own + lds_get(xi + n * 1024);
          const int col = 112 + 16 * (nb + n) + 4 * q;
          const f32x4 u = lds_get(lds + L_UV + col * 4), v = lds_get(lds + L_UV + (224 + col) * 4);
          const f32x4 pre = a * rstd1 + (u * c1 + v);
          z1p[2 * n] = pk2(gelu_tanh(pre[0]), gelu_tanh(pre[1])); z1p[2 * n + 1] = pk2(gelu_tanh(pre[2]), gelu_tanh(pre[3]));
        };
        if (kh) { fin(0, acc[4]); fin(1, acc[5]); fin(2, acc[6]); z1p[6] = 0u; z1p[7] = 0u; }
        else { fin(0, acc[0]); fin(1, acc[1]); fin(2, acc[2]); fin(3, acc[3]); }
      }
#pragma unroll
      for (int i = 0; i < 8; ++i) asm volatile("" : "+v"(z1p[i]));
      STAMP(26);
      STAMP(13);
    }

    {
      LANE_IDS();
      const int mh = m >> 1, k4 = m & 1, nh = kh;
      f32x4 acc[2][4];
#pragma unroll
      for (int a = 0; a < 2; ++a)
#pragma unroll
        for (int n = 0; n < 4; ++n) acc[a][n] = (f32x4){0.f, 0.f, 0.f, 0.f};
      const unsigned aoff = (mh * 32 + l15) * 128 + (((k4 * 4 + q) ^ (l15 >> 1)) << 4);
      const unsigned boff = P4_DOFF + k4 * 7168 + nh * 4096 + lane * 16;
      const rsrc_t rb = mkrs(p.bsp, SEQ * 2);
      const float bs = bflo(ld2<0>(rb, (64 * g + 16 * m + l15) * 2, 0));
      bf16x8 XA[2], XB[4], YA[2], YB[4];
      int sa = 0, sb = 0;
      if (nh == 0) {
        auto rd = [&](bf16x8(&A)[2], bf16x8(&B)[4]) {
          LAS char* pa = lds + sa * 8192 + aoff;
          LAS char* pb = lds + sb * 14336 + boff;
          A[0] = ldsfrag(pa); A[1] = ldsfrag(pa + 2048);
#pragma unroll
          for (int n = 0; n < 4; ++n) B[n] = ldsfrag(pb + n * 1024);
          sa = (sa + 1 == P4_SA) ? 0 : sa + 1; sb = (sb + 1 == P4_DB) ? 0 : sb + 1;
        };
        auto mm = [&](const bf16x8(&A)[2], const bf16x8(&B)[4]) {
#pragma unroll
          for (int a = 0; a < 2; ++a)
#pragma unroll
            for (int n = 0; n < 4; ++n) acc[a][n] = mfma(B[n], A[a], acc[a][n]);
        };
        BAR();
        STAMP(14);
        rd(XA, XB);
#pragma unroll 1
        for (int i = 0; i < 15; ++i) {
          BAR(); rd(YA, YB); mm(XA, XB); interleave<8, 6, 1>(); SCHED();
          BAR(); rd(XA, XB); mm(YA, YB); interleave<8, 6, 1>(); SCHED();
        }
        BAR(); rd(YA, YB); mm(XA, XB); interleave<8, 6, 1>(); SCHED();
        mm(YA, YB);
      } else {
        auto rd = [&](bf16x8(&A)[2], bf16x8(&B)[4]) {
          LAS char* pa = lds + sa * 8192 + aoff;
          LAS char* pb = lds + sb * 14336 + boff;
          A[0] = ldsfrag(pa); A[1] = ldsfrag(pa + 2048);
#pragma unroll
          for (int n = 0; n < 3; ++n) B[n] = ldsfrag(pb + n * 1024);
          sa = (sa + 1 == P4_SA) ? 0 : sa + 1; sb = (sb + 1 == P4_DB) ? 0 : sb + 1;
        };
        auto mm = [&](const bf16x8(&A)[2], const bf16x8(&B)[4]) {
#pragma unroll
          for (int a = 0; a < 2; ++a)
#pragma unroll
            for (int n = 0; n < 3; ++n) acc[a][n] = mfma(B[n], A[a], acc[a][n]);
        };
        BAR();
        STAMP(14);
        rd(XA, XB);
#pragma unroll 1
        for (int i = 0; i < 15; ++i) {
          BAR(); rd(YA, YB); mm(XA, XB); interleave<6, 5, 1>(); SCHED();
          BAR(); rd(XA, XB); mm(YA, YB); interleave<6, 5, 1>(); SCHED();
        }
        BAR(); rd(YA, YB); mm(XA, XB); interleave<6, 5, 1>(); SCHED();
        mm(YA, YB);
      }
      STAMP(15);
      LAS char* xo = lds + P4_X + w * 4096 + lane * 16;
      LAS char* xi = lds + P4_X + (w ^ 1) * 4096 + lane * 16;
#pragma unroll
      for (int n = 0; n < 4; ++n) lds_put(xo + n * 1024, sel4(k4, acc[0][n], acc[1][n]));
      wait_lgkm0();
      BAR();
      u32x2 up[4];
#pragma unroll
      for (int n = 0; n < 4; ++n) {
        const f32x4 gv = sel4(k4, acc[1][n], acc[0][n]) + lds_get(xi + n * 1024) + bs;
        up[n] = (u32x2){pk2(bflo(z1p[2 * n]) * gv[0], bfhi(z1p[2 * n]) * gv[1]), pk2(bflo(z1p[2 * n + 1]) * gv[2], bfhi(z1p[2 * n + 1]) * gv[3])};
      }
      const rsrc_t ru = mkrs(ws + WS_U, 32 * 28 * 4 * 1024);
      auto u_off = [&](int nf) { const int c = 14 * j + 2 * nf + (q >> 1); return (unsigned)(((g * 28 + (c >> 2)) * 4 + m) * 1024 + ((c & 3) * 16 + l15) * 16); };
      const int odd = q & 1;
      st16<SC1>(pair_swap(up[0], up[1]), ru, u_off(4 * nh + odd), 0);
      if (nh) st8<SC1>(up[2], ru, u_off(6) + odd * 8, 0);
      else st16<SC1>(pair_swap(up[2], up[3]), ru, u_off(2 + odd), 0);
      STAMP(24);
      BAR();
      publish(lds, 6, 8, ws, WS_FLAG4 + (g * 8 + j) * 128, T, lane);
      if (b == 0 && w == 0 && lane == 0) __hip_atomic_store((GAS u64*)(ws + WS_EPOCH), T, __ATOMIC_RELAXED, __HIP_MEMORY_SCOPE_AGENT);
      STAMP(16);
    }

    {
      LANE_IDS();
      constexpr int NT = 14;
      const int nb = kh ? 3 : 0;
      group_wait(lds, 10, ws, WS_FLAG4 + g * 1024, 8, T, w == 0, lane);
      STAMP(17);
      const rsrc_t ra = mkrs(ws + WS_U + (size_t)g * 28 * 4 * 1024, 28 * 4 * 1024);
      u32x4 A[NT];
#pragma unroll
      for (int t = 0; t < AW; ++t) A[t] = ld16<SC1>(ra, lane * 16, ((2 * t + kh) * 4 + m) * 1024);
      f32x4 acc[5];
#pragma unroll
      for (int n = 0; n < 5; ++n) acc[n] = (f32x4){0.f, 0.f, 0.f, 0.f};
      bf16x8 B[2][5];
      auto rd = [&](bf16x8(&Bv)[5], int slot) {
#pragma unroll
        for (int n = 0; n < 5; ++n) Bv[n] = ldsfrag(lds + slot * 10240 + kh * 5120 + n * 1024 + lane * 16);
      };
      BAR();
      STAMP(18);
      rd(B[0], 0);
#pragma unroll
      for (int t = 0; t < NT; ++t) {
        if (t + 1 < NT) { BAR(); rd(B[(t + 1) & 1], t + 1); }
        if (t + AW < NT) A[t + AW] = ld16<SC1>(ra, lane * 16, ((2 * (t + AW) + kh) * 4 + m) * 1024);
#pragma unroll
        for (int n = 0; n < 5; ++n) acc[n] = mfma(B[t & 1][n], asfrag(A[t]), acc[n]);
        interleave<5, 5, 1>(); SCHED();
      }
      STAMP(19);
      LAS char* xo = lds + P5_X + w * 3072 + lane * 16;
      LAS char* xi = lds + P5_X + (w ^ 4) * 3072 + lane * 16;
      if (kh) { lds_put(xo, acc[0]); lds_put(xo + 1024, acc[1]); lds_put(xo + 2048, acc[2]); }
      else { lds_put(xo, acc[3]); lds_put(xo + 1024, acc[4]); }
      wait_lgkm0();
      BAR();
      u32x2 op[3];
#pragma unroll
      for (int n = 0; n < 3; ++n) {
        const u32x2 x1v = *(LAS u32x2*)(lds + L_X1 + (w * 3 + n) * 512 + lane * 8);
        const unsigned x1p[6] = {x1v.x, x1v.y, x1v.x, x1v.y, x1v.x, x1v.y};
        const f32x4 o = sel4(kh, acc[n == 2 ? 4 : 3 + n], acc[n]) + lds_get(xi + n * 1024);
        const int col = (kh && n == 2) ? 0 : 16 * (nb + n) + 4 * q;
        const f32x4 gb = lds_get(lds + L_TAB + (4 * 80 + col) * 4);
        op[n] = (u32x2){pk2(__builtin_fmaf(gb[0], o[0], bflo(x1p[2 * n])), __builtin_fmaf(gb[1], o[1], bfhi(x1p[2 * n]))),
                        pk2(__builtin_fmaf(gb[2], o[2], bflo(x1p[2 * n + 1])), __builtin_fmaf(gb[3], o[3], bfhi(x1p[2 * n + 1])))};
      }
      const rsrc_t ro = mkrs(p.out, SEQ * DM * 2);
      const unsigned rowb = (unsigned)((64 * g + 16 * m + l15) * (DM * 2) + 80 * j * 2);
      const int odd = q & 1;
      st16<SC1>(pair_swap(op[0], op[1]), ro, rowb + (16 * (nb + odd) + 8 * (q >> 1)) * 2, 0);
      st8<SC1>(op[2], ro, kh ? OOB : rowb + (32 + 4 * q) * 2, 0);
      STAMP(20); STAMPC(31);
    }
  } else if (wid < 10) {
    LANE_IDS();
    const int h = wid - 8;
    const unsigned sw0 = (unsigned)(((lane & 7) ^ (lane >> 4)) * 16), sw1 = sw0 ^ 64;
    wait_lgkm0();
    BAR();
    {
      const rsrc_t r = mkrs((h ? p.wsim : p.wsre) + (size_t)g * 64 * SEQ, 64 * SEQ * 2);
      const unsigned v0 = (lane >> 3) * 4096 + sw0, v1 = (lane >> 3) * 4096 + sw1;
      auto issue = [&](int t, int slot) {
        const unsigned k = (unsigned)((t + rot) & 31) * 128;
#pragma unroll
        for (int pc = 0; pc < 8; ++pc) dma16<0>(r, lds + slot * 16384 + (h * 8 + pc) * 1024, (pc & 1) ? v1 : v0, k + pc * 32768);
      };
      auto touch = [&](int it) {
        __builtin_amdgcn_raw_ptr_buffer_load_lds(r, (LAS void*)(lds + L_SINK), 4, (int)((j * 8 + (lane >> 3)) * 4096 + ((it * 8 + (lane & 7) + rot) & 31) * 128), 0, 0, 0);
      };
      lds_wait(lds, 15); lds_wait(lds, 16);
      if (h == 1) {
        const int cg = b % 60, kq = b / 60;
        const rsrc_t rw = mkrs(p.wmod, b < 240 ? DM * 3840 * 2 : 0), rc = mkrs(p.c, DM * 2);
        u32x4 wv[20]; unsigned cv[20];
#pragma unroll
        for (int it = 0; it < 20; ++it) {
          const int row = (kq & 3) * 160 + it * 8 + (lane >> 3);
          wv[it] = ld16<0>(rw, (unsigned)(row * 7680 + cg * 128 + (lane & 7) * 16), 0);
          cv[it] = ld2<0>(rc, row * 2, 0);
        }
        touch(0); if (T0N > 1) touch(1);
        float a8[8] = {0.f, 0.f, 0.f, 0.f, 0.f, 0.f, 0.f, 0.f};
#pragma unroll
        for (int it = 0; it < 20; ++it) {
          const float cf = bflo(cv[it]);
          const float sl = rbf(cf * __builtin_amdgcn_rcpf(1.0f + __builtin_amdgcn_exp2f(-1.4426950408889634f * cf)));
#pragma unroll
          for (int i = 0; i < 4; ++i) { a8[2 * i] += sl * bflo(wv[it][i]); a8[2 * i + 1] += sl * bfhi(wv[it][i]); }
        }
#pragma unroll
        for (int i = 0; i < 8; ++i) { a8[i] += __shfl_xor(a8[i], 8); a8[i] = xq_sum(a8[i]); }
        const rsrc_t rm = mkrs(ws + WS_MODP, 4 * 3840 * 4);
        const unsigned mo = (unsigned)((kq * 3840 + cg * 64 + (lane & 7) * 8) * 4);
        const bool on = lane < 8 && b < 240;
        st16<SC1>((u32x4){__float_as_uint(a8[0]), __float_as_uint(a8[1]), __float_as_uint(a8[2]), __float_as_uint(a8[3])}, rm, on ? mo : OOB, 0);
        st16<SC1>((u32x4){__float_as_uint(a8[4]), __float_as_uint(a8[5]), __float_as_uint(a8[6]), __float_as_uint(a8[7])}, rm, on ? mo + 16 : OOB, 0);
      } else {
        touch(0); if (T0N > 1) touch(1);
      }
      lds_wait(lds, ASIG);
      loader_fill<0, 2, P1_SA>(issue);
      STAMP(1);
      if (h == 1) {
        wait_vm<8>();
        if (arrive_last(lds, 7, 3, lane)) set_flag(ws, WS_FLAGB + g * 128 + j * 8, epoch_tag(Ein), lane);
      }
      loader_run<32, P1_SA, 8, 2, 2, -1>(issue, [&](int i) { if (T0N < 2 && i == TH1) touch(1); if (i == TH2) touch(2); if (i == TH3) touch(3); });
      BAR(); BAR();
    }
    {
      const unsigned v0 = (lane >> 3) * 1280 + (h ? sw1 : sw0), v1 = (lane >> 3) * 1280 + (h ? sw0 : sw1);
      auto issue = [&](int t, int slot) {
        const int im = t >= 10;
        const rsrc_t r = mkrs((im ? p.wdim : p.wdre) + (size_t)j * 80 * DM, 80 * DM * 2);
        const unsigned k = (unsigned)(t - 10 * im) * 128 + h * 5 * 8 * 1280;
#pragma unroll
        for (int pc = 0; pc < 5; ++pc) dma16<0>(r, lds + slot * 10240 + (h * 5 + pc) * 1024, (pc & 1) ? v1 : v0, k + pc * 8 * 1280);
      };
      loader_fill<0, PF2, P2_RING>(issue);
      STAMP(2);
      loader_run<20, P2_RING, 5, PF2, 2, -1>(issue);
      BAR(); BAR();
    }
    {
      const rsrc_t r = mkrs(ws + WS_WINR + (size_t)j * 20 * 14336, 20 * 14336);
      auto issue = [&](int t, int slot) {
#pragma unroll
        for (int pc = 0; pc < 7; ++pc) dma16<SC1>(r, lds + slot * 14336 + (h * 7 + pc) * 1024, lane * 16, (unsigned)(t * 14336 + (h * 7 + pc) * 1024));
      };
      loader_fill<0, PF3, P3_RING>(issue);
      STAMP(3);
      loader_run<20, P3_RING, 7, PF3, 2, 9>(issue);
      BAR();
    }
    {
      const rsrc_t r = mkrs(p.wsp + (size_t)g * 64 * SEQ, 64 * SEQ * 2);
      const unsigned v0 = (lane >> 3) * 4096 + sw0, v1 = (lane >> 3) * 4096 + sw1;
      auto issue = [&](int t, int slot) {
        const unsigned k = (unsigned)((t + rot) & 31) * 128 + h * 4 * 32768;
#pragma unroll
        for (int pc = 0; pc < 4; ++pc) dma16<0>(r, lds + slot * 8192 + (h * 4 + pc) * 1024, (pc & 1) ? v1 : v0, k + pc * 32768);
      };
      loader_fill<0, PF4, P4_SA>(issue);
      BAR();
      STAMP(4);
      loader_run<32, P4_SA, 4, PF4, 2, -1>(issue);
      BAR(); BAR();
    }
    {
      const rsrc_t r = mkrs(ws + WS_WOUTR + (size_t)j * 14 * 10240, 14 * 10240);
      auto issue = [&](int t, int slot) {
#pragma unroll
        for (int pc = 0; pc < 5; ++pc) dma16<SC1>(r, lds + slot * 10240 + (h * 5 + pc) * 1024, lane * 16, (unsigned)(t * 10240 + (h * 5 + pc) * 1024));
      };
      loader_fill<0, PF5, P5_RING>(issue);
      STAMP(5);
      loader_run<14, P5_RING, 5, PF5, 2, -1>(issue);
      BAR();
    }
  } else {
    LANE_IDS();
    const int h = wid - 10;
    wait_lgkm0();
    BAR();
    {
      LAS char* sc = lds + L_RT + h * 10240;
      const int u = b * 2 + h;
      const int qq = l15 >> 2, pp = l15 & 3;
      if (u < 320) {
        const int jp = u / 20, kf = u - 20 * jp, jj = jp >> 1, part = jp & 1;
        const rsrc_t r = mkrs(p.win, DM * FF * 2);
#pragma unroll
        for (int pc = 0; pc < 7; ++pc) {
          const int idx = pc * 64 + lane, row = idx / 14, ch = idx - 14 * row;
          const bool ok = 112 * jj + 8 * ch < FH;
          dma16<0>(r, sc + pc * 1024, ok ? (unsigned)((32 * kf + row) * (FF * 2) + (part ? 0 : FH * 2) + 224 * jj + ch * 16) : OOB, 0);
        }
      } else if (u < 432) {
        const int uu = u - 320, jj = uu / 14, kp = uu - 14 * jj;
        const rsrc_t r = mkrs(p.wout, FH * DM * 2);
#pragma unroll
        for (int pc = 0; pc < 10; ++pc) {
          const int idx = pc * 64 + lane, row = idx / 10, ch = idx - 10 * row;
          dma16<0>(r, sc + pc * 1024, (unsigned)((64 * kp + row) * (DM * 2) + 160 * jj + ch * 16), 0);
        }
      }
      lds_signal(lds, 15 + h, lane);
      wait_vm<0>();
      CFENCE();
      if (u < 320) {
        const int jp = u / 20, kf = u - 20 * jp, jj = jp >> 1, part = jp & 1;
        const rsrc_t ro = mkrs(ws + WS_WINR, 8 * 20 * 14336);
#pragma unroll
        for (int nf = 0; nf < 7; ++nf) {
          LAS char* a0 = sc + (8 * q + qq) * 224 + (16 * nf + 4 * pp) * 2;
          const u32x2 lo = __builtin_bit_cast(u32x2, __builtin_amdgcn_ds_read_tr16_b64_v4i16((LAS s16x4*)a0));
          const u32x2 hi = __builtin_bit_cast(u32x2, __builtin_amdgcn_ds_read_tr16_b64_v4i16((LAS s16x4*)(a0 + 4 * 224)));
          st16<SC1>((u32x4){lo.x, lo.y, hi.x, hi.y}, ro, (unsigned)((((jj * 2 + part) * 20 + kf) * 7 + nf) * 1024 + lane * 16), 0);
        }
      } else if (u < 432) {
        const int uu = u - 320, jj = uu / 14, kp = uu - 14 * jj;
        const rsrc_t ro = mkrs(ws + WS_WOUTR, 8 * 14 * 10240);
#pragma unroll
        for (int f = 0; f < 10; ++f) {
          const int kk = f / 5, nf = f - 5 * kk;
          LAS char* a0 = sc + (32 * kk + 8 * q + qq) * 160 + (16 * nf + 4 * pp) * 2;
          const u32x2 lo = __builtin_bit_cast(u32x2, __builtin_amdgcn_ds_read_tr16_b64_v4i16((LAS s16x4*)a0));
          const u32x2 hi = __builtin_bit_cast(u32x2, __builtin_amdgcn_ds_read_tr16_b64_v4i16((LAS s16x4*)(a0 + 4 * 160)));
          st16<SC1>((u32x4){lo.x, lo.y, hi.x, hi.y}, ro, (unsigned)(((jj * 14 + kp) * 10 + f) * 1024 + lane * 16), 0);
        }
      }
      wait_lgkm0(); CFENCE();
    }
    const u64 T = epoch_tag(Ein);
    STAMP(1);
    {
      if (h == 0) {
        lds_wait(lds, 14);
        __builtin_amdgcn_s_sleep(F0DELAY);
        poll_flags256(ws, WS_FLAG0 + xg * 4096, T, lane);
        lds_signal(lds, 12, lane);
      } else {
        lds_wait(lds, 12);
      }
      STAMP(2);
      const rsrc_t r = mkrs(ws + WS_XNT + (size_t)j * 32 * 10240, 32 * 10240);
      auto issue = [&](int t, int slot) {
        const unsigned k = (unsigned)((t + rot) & 31) * 10240;
#pragma unroll
        for (int pc = 0; pc < 5; ++pc) dma16<SC1>(r, lds + P1_DOFF + slot * 10240 + (h * 5 + pc) * 1024, lane * 16, k + (h * 5 + pc) * 1024);
      };
      loader_fill<0, 1, P1_DB>(issue);
      if (h == 0) lds_signal(lds, 11, lane);
      loader_fill<1, 2, P1_DB>(issue);
      wait_vm<5>();
      if (arrive_last(lds, 7, 3, lane)) set_flag(ws, WS_FLAGB + g * 128 + j * 8, T, lane);
      loader_run<32, P1_DB, 5, 2, 2, -1>(issue, [&](int i) {
        if (i == 20 && h == 1) {
          const int bi = g * 8 + j;
          const rsrc_t r3 = mkrs(p.wdre, DM * DM * 2), r4 = mkrs(p.wdim, DM * DM * 2);
          __builtin_amdgcn_raw_ptr_buffer_load_lds(r3, (LAS void*)(lds + L_SINK), 4, lane < 25 ? (int)((bi * 25 + lane) * 128) : (int)OOB, 0, 0, 0);
          __builtin_amdgcn_raw_ptr_buffer_load_lds(r4, (LAS void*)(lds + L_SINK), 4, lane < 25 ? (int)((bi * 25 + lane) * 128) : (int)OOB, 0, 0, 0);
        }
      });
      BAR(); BAR();
    }
    if (h == 0 && b < 112) {
      const int jj = b / 14, pn = b - 14 * jj, part = pn / 7, nf = pn - 7 * part;
      const rsrc_t rw = mkrs(ws + WS_WINR, 8 * 20 * 14336), rm = mkrs(ws + WS_MODP, 4 * 3840 * 4);
      u32x4 wf[20];
#pragma unroll
      for (int k = 0; k < 20; ++k) wf[k] = ld16<SC1>(rw, lane * 16, (unsigned)((((jj * 2 + part) * 20 + k) * 7 + nf) * 1024));
#pragma unroll
      for (int it = 0; it < 5; ++it) {
        const int c4 = (it * 64 + lane) * 4, isS = c4 >= DM, cc = c4 - DM * isS;
        const unsigned o = (unsigned)(((isS ? 3 : 4) * DM + cc) * 4);
        const f32x4 m0 = __builtin_bit_cast(f32x4, ld16<SC1>(rm, o, 0)), m1 = __builtin_bit_cast(f32x4, ld16<SC1>(rm, o, 3840 * 4)),
                    m2 = __builtin_bit_cast(f32x4, ld16<SC1>(rm, o, 2 * 3840 * 4)), m3 = __builtin_bit_cast(f32x4, ld16<SC1>(rm, o, 3 * 3840 * 4));
        f32x4 s = (m0 + m1) + (m2 + m3);
#pragma unroll
        for (int i = 0; i < 4; ++i) { s[i] = rbf(s[i]); if (!isS) s[i] = 1.0f + s[i]; }
        *(LAS u32x2*)(lds + L_TABE + (isS * DM + cc) * 2) = (u32x2){pk2(s[0], s[1]), pk2(s[2], s[3])};
      }
      wait_lgkm0(); CFENCE();
      f32x4 acc = {0.f, 0.f, 0.f, 0.f};
#pragma unroll
      for (int k = 0; k < 20; ++k) {
        u32x4 e = *(LAS u32x4*)(lds + L_TABE + ((l15 & 1) * DM + 32 * k + 8 * q) * 2);
        if (l15 >= 2) e = (u32x4){0u, 0u, 0u, 0u};
        acc = mfma(asfrag(e), asfrag(wf[k]), acc);
      }
      const rsrc_t ruv = mkrs(ws + WS_UV, 2 * 896 * 16);
      st16<SC1>((u32x4){__float_as_uint(acc[0]), __float_as_uint(acc[1]), (unsigned)T, (unsigned)(T >> 32)}, ruv,
                q == 0 ? (unsigned)((part * 896 + 112 * jj + 16 * nf + l15) * 16) : OOB, 0);
    }
    STAMP(3);
    idle_phase<22>();
    idle_phase<12>();
    if (h == 1) {
      const rsrc_t r1 = mkrs(p.wsp + (size_t)(64 * g + 8 * j) * SEQ, 8 * SEQ * 2);
#pragma unroll
      for (int it = 0; it < 4; ++it)
        __builtin_amdgcn_raw_ptr_buffer_load_lds(r1, (LAS void*)(lds + L_SINK), 4, (int)((lane >> 3) * 4096 + ((it * 8 + (lane & 7) + rot) & 31) * 128), 0, 0, 0);
    }
    idle_phase<11>();
    {
      group_wait(lds, 17, ws, WS_FLAG3 + j * 4096, 32, T, h == 0, lane);
      STAMP(4);
      const rsrc_t r = mkrs(ws + WS_Z2T + (size_t)j * 32 * 14336, 32 * 14336);
      auto issue = [&](int t, int slot) {
        const unsigned k = (unsigned)((t + rot) & 31) * 14336;
#pragma unroll
        for (int pc = 0; pc < 7; ++pc) dma16<SC1>(r, lds + P4_DOFF + slot * 14336 + (h * 7 + pc) * 1024, lane * 16, k + (h * 7 + pc) * 1024);
      };
      loader_fill<0, 2, P4_DB>(issue);
      loader_run<32, P4_DB, 7, 2, 2, -1>(issue);
      BAR(); BAR();
    }
    idle_phase<15>();
  }
}

extern "C" void kernel_launch(void* const* d_in, const int* in_sizes, int n_in, void* d_out, int out_size, void* d_ws, size_t ws_size,
                              hipStream_t stream) {
  Params p;
  p.x = (const u16*)d_in[0]; p.wsre = (const u16*)d_in[1]; p.wsim = (const u16*)d_in[2]; p.wdre = (const u16*)d_in[3]; p.wdim = (const u16*)d_in[4];
  p.win = (const u16*)d_in[6]; p.wsp = (const u16*)d_in[7]; p.bsp = (const u16*)d_in[8]; p.wout = (const u16*)d_in[9];
  p.c = (const u16*)d_in[10]; p.wmod = (const u16*)d_in[11];
  p.out = (u16*)d_out; p.ws = (char*)d_ws + ((ws_size - WS_USED) & ~(size_t)0x1fffff);
  (void)hipFuncSetAttribute((const void*)adaLN_cond__spectral_fnet__mixer_gmlp__prompt_kernel, hipFuncAttributeMaxDynamicSharedMemorySize, LDS_BYTES);
  adaLN_cond__spectral_fnet__mixer_gmlp__prompt_kernel<<<256, 768, LDS_BYTES, stream>>>(p);
}
```

```cpp
#include <hip/hip_runtime.h>
#include <hip/hip_bf16.h>

typedef unsigned short u16;
typedef unsigned long long u64;
typedef short bf16x8 __attribute__((ext_vector_type(8)));
typedef short s16x4 __attribute__((ext_vector_type(4)));
typedef __bf16 bf16x2 __attribute__((ext_vector_type(2)));
typedef float f32x2 __attribute__((ext_vector_type(2)));
typedef float f32x4 __attribute__((ext_vector_type(4)));
typedef unsigned u32x2 __attribute__((ext_vector_type(2)));
typedef unsigned u32x4 __attribute__((ext_vector_type(4)));
typedef __amdgpu_buffer_rsrc_t rsrc_t;
#define LAS __attribute__((address_space(3)))
#define GAS __attribute__((address_space(1)))
#define DEVI __device__ __forceinline__

constexpr int SEQ = 2048, DM = 640, FF = 1728, FH = 864;

constexpr unsigned WS_EPOCH = 0x0;
constexpr unsigned WS_TMO   = 0x100;
constexpr unsigned WS_FLAG0 = 0x1A00000;
constexpr unsigned WS_FLAGB = 0x1A08000;
constexpr unsigned WS_FLAG1 = 0x1A10000;
constexpr unsigned WS_FLAG2 = 0x1A18000;
constexpr unsigned WS_FLAG4 = 0x1A20000;
constexpr unsigned WS_FLAG3 = 0x1A28000;
constexpr unsigned WS_MODP  = 0x10000;
constexpr unsigned WS_UV    = 0x20000;
constexpr unsigned WS_ST1   = 0x40000;
constexpr unsigned WS_ST2   = 0x80000;
constexpr unsigned WS_XNT   = 0x100000;
constexpr unsigned WS_PQ    = 0x400000;
constexpr unsigned WS_X1C   = 0xA00000;
constexpr unsigned WS_Z2T   = 0xD00000;
constexpr unsigned WS_U     = 0x1100000;
constexpr unsigned WS_WINR  = 0x1500000;
constexpr unsigned WS_WOUTR = 0x1800000;
constexpr unsigned WS_USED  = 0x2600000;

constexpr int LDS_BYTES = 163840;
constexpr int L_MISC = 159744;
constexpr int L_CNT  = L_MISC;
constexpr int L_TAB  = L_MISC + 128;
constexpr int L_UV   = L_MISC + 1728;
constexpr int L_SINK = L_MISC + 3584;
constexpr int L_TABE = 153600;
constexpr int L_P0T  = 98304;
constexpr int L_RT   = 118784;
constexpr int P0T_RS = 1296;

constexpr int F0DELAY = 38, PDELAY = 4;
constexpr int TH1 = 0, TH2 = 5, TH3 = 12;
constexpr int GST = 4;
constexpr unsigned SPIN_MAX = 1u << 17;

struct Params {
  const u16 *x, *wsre, *wsim, *wdre, *wdim, *win, *wsp, *bsp, *wout, *c, *wmod;
  u16* out;
  char* ws;
};

DEVI rsrc_t mkrs(const void* p, unsigned bytes) { return __builtin_amdgcn_make_buffer_rsrc((void*)p, 0, (int)bytes, 0x00020000); }
template <int AUX> DEVI u32x4 ld16(rsrc_t r, unsigned v, unsigned s) { return __builtin_amdgcn_raw_buffer_load_b128(r, (int)v, (int)s, AUX); }
template <int AUX> DEVI u32x2 ld8(rsrc_t r, unsigned v, unsigned s) { return __builtin_amdgcn_raw_buffer_load_b64(r, (int)v, (int)s, AUX); }
template <int AUX> DEVI unsigned ld2(rsrc_t r, unsigned v, unsigned s) { return (unsigned)__builtin_amdgcn_raw_buffer_load_b16(r, (int)v, (int)s, AUX) & 0xffffu; }
template <int AUX> DEVI void st16(u32x4 d, rsrc_t r, unsigned v, unsigned s) { __builtin_amdgcn_raw_buffer_store_b128(d, r, (int)v, (int)s, AUX); }
template <int AUX> DEVI void st8(u32x2 d, rsrc_t r, unsigned v, unsigned s) { __builtin_amdgcn_raw_buffer_store_b64(d, r, (int)v, (int)s, AUX); }
template <int AUX> DEVI void dma16(rsrc_t r, LAS char* dst, unsigned v, unsigned s) { __builtin_amdgcn_raw_ptr_buffer_load_lds(r, (LAS void*)dst, 16, (int)v, (int)s, 0, AUX); }
constexpr int SC1 = 16;
constexpr unsigned OOB = 0x7ffffff0u;

template <int N> DEVI void wait_vm() { asm volatile("s_waitcnt vmcnt(%0)" ::"n"(N > 63 ? 63 : N) : "memory"); }
DEVI void wait_lgkm0() { __builtin_amdgcn_s_waitcnt(0xC07F); }
DEVI void BAR() { asm volatile("s_barrier" ::: "memory"); }
DEVI void CFENCE() { asm volatile("" ::: "memory"); }
#define SCHED() __builtin_amdgcn_sched_barrier(0)

DEVI float bflo(unsigned v) { return __uint_as_float(v << 16); }
DEVI float bfhi(unsigned v) { return __uint_as_float(v & 0xffff0000u); }
DEVI unsigned pk2(float lo, float hi) { f32x2 f = {lo, hi}; return __builtin_bit_cast(unsigned, __builtin_convertvector(f, bf16x2)); }
DEVI float rbf(float v) { return bflo(pk2(v, 0.f)); }
DEVI f32x4 mfma(bf16x8 a, bf16x8 b, f32x4 c) { return __builtin_amdgcn_mfma_f32_16x16x32_bf16(a, b, c, 0, 0, 0); }
DEVI bf16x8 asfrag(u32x4 v) { return __builtin_bit_cast(bf16x8, v); }
DEVI bf16x8 ldsfrag(LAS char* p) { return *(LAS bf16x8*)p; }

template <int CTRL> DEVI float dpp_add(float v) { return v + __int_as_float(__builtin_amdgcn_update_dpp(0, __float_as_int(v), CTRL, 0xf, 0xf, false)); }
DEVI float row16_sum(float v) { v = dpp_add<0x128>(v); v = dpp_add<0x124>(v); v = dpp_add<0x122>(v); return dpp_add<0x121>(v); }
DEVI f32x2 both16(float v) { auto r = __builtin_amdgcn_permlane16_swap(__float_as_uint(v), __float_as_uint(v), false, false); return (f32x2){__uint_as_float(r[0]), __uint_as_float(r[1])}; }
DEVI f32x2 both32(float v) { auto r = __builtin_amdgcn_permlane32_swap(__float_as_uint(v), __float_as_uint(v), false, false); return (f32x2){__uint_as_float(r[0]), __uint_as_float(r[1])}; }
DEVI float xq_sum(float v) { f32x2 a = both16(v); v = a.x + a.y; a = both32(v); return a.x + a.y; }
DEVI float wave_sum(float v) { return xq_sum(row16_sum(v)); }

DEVI float gelu_tanh(float x) {
  constexpr float k1 = -2.0f * 0.7978845608028654f * 1.4426950408889634f, k2 = k1 * 0.044715f;
  return x * __builtin_amdgcn_rcpf(1.0f + __builtin_amdgcn_exp2f(x * __builtin_fmaf(x * x, k2, k1)));
}
DEVI f32x2 ln_stats(float s, float q, float inv_n) {
  const float m = s * inv_n, v = __builtin_fmaf(q, inv_n, -m * m);
  return (f32x2){m, __builtin_amdgcn_rsqf((v > 0.f ? v : 0.f) + 1e-5f)};
}

DEVI u32x4 pair_swap(u32x2 a, u32x2 b) {
  auto s0 = __builtin_amdgcn_permlane16_swap(a.x, b.x, false, false);
  auto s1 = __builtin_amdgcn_permlane16_swap(a.y, b.y, false, false);
  return (u32x4){s0[0], s1[0], s0[1], s1[1]};
}

DEVI void note_timeout(char* ws, unsigned code, int lane) {
  if (lane == 0) __hip_atomic_store((GAS unsigned*)(ws + WS_TMO), code, __ATOMIC_RELAXED, __HIP_MEMORY_SCOPE_AGENT);
}
DEVI void poll_flags(char* ws, unsigned off, int n, u64 T, int lane) {
  for (unsigned spins = 0;; ++spins) {
    u64 f = T;
    if (lane < n) f = __hip_atomic_load((GAS u64*)(ws + off + lane * 128), __ATOMIC_RELAXED, __HIP_MEMORY_SCOPE_AGENT);
    if (__all(f == T)) break;
    if (spins > SPIN_MAX) { note_timeout(ws, 0x100u | ((off >> 15) & 0xff), lane); break; }
    __builtin_amdgcn_s_sleep(1);
  }
  CFENCE();
}
DEVI void poll_flags256(char* ws, unsigned off, u64 T, int lane) {
  for (unsigned spins = 0;; ++spins) {
    bool ok = true;
#pragma unroll
    for (int i = 0; i < 4; ++i) ok &= __hip_atomic_load((GAS u64*)(ws + off + (i * 8 + (lane >> 3)) * 128 + (lane & 7) * 8), __ATOMIC_RELAXED, __HIP_MEMORY_SCOPE_AGENT) == T;
    if (__all(ok)) break;
    if (spins > SPIN_MAX) { note_timeout(ws, 0x200u | ((off >> 15) & 0xff), lane); break; }
    __builtin_amdgcn_s_sleep(4);
  }
  CFENCE();
}
DEVI void set_flag(char* ws, unsigned off, u64 T, int lane) {
  if (lane == 0) __hip_atomic_store((GAS u64*)(ws + off), T, __ATOMIC_RELAXED, __HIP_MEMORY_SCOPE_AGENT);
}
DEVI void lds_signal(LAS char* lds, int idx, int lane) {
  CFENCE();
  if (lane == 0) __hip_atomic_store((LAS unsigned*)(lds + L_CNT) + idx, 1u, __ATOMIC_RELAXED, __HIP_MEMORY_SCOPE_WORKGROUP);
}
DEVI void lds_wait(LAS char* lds, int idx) {
  for (unsigned spins = 0; spins < (SPIN_MAX << 6); ++spins) {
    if (__hip_atomic_load((LAS unsigned*)(lds + L_CNT) + idx, __ATOMIC_RELAXED, __HIP_MEMORY_SCOPE_WORKGROUP) != 0u) break;
    __builtin_amdgcn_s_sleep(1);
  }
  CFENCE();
}
DEVI void group_wait(LAS char* lds, int idx, char* ws, unsigned off, int n, u64 T, bool poller, int lane) {
  if (poller) { __builtin_amdgcn_s_sleep(PDELAY); poll_flags(ws, off, n, T, lane); lds_signal(lds, idx, lane); } else lds_wait(lds, idx);
}
DEVI bool arrive_last(LAS char* lds, int idx, unsigned n, int lane) {
  unsigned old = 0;
  if (lane == 0) old = __hip_atomic_fetch_add((LAS unsigned*)(lds + L_CNT) + idx, 1u, __ATOMIC_RELAXED, __HIP_MEMORY_SCOPE_WORKGROUP);
  return (unsigned)__builtin_amdgcn_readfirstlane(old) == n - 1;
}
DEVI void publish(LAS char* lds, int cidx, unsigned nwaves, char* ws, unsigned flag_off, u64 T, int lane) {
  wait_vm<0>();
  if (arrive_last(lds, cidx, nwaves, lane)) set_flag(ws, flag_off, T, lane);
}

#define LANE_IDS() int tz_ = threadIdx.x; asm volatile("" : "+v"(tz_)); const int lane = tz_ & 63, l15 = lane & 15, q = lane >> 4; (void)l15; (void)q

#define LANE_IDS2() int tz2_ = threadIdx.x; asm volatile("" : "+v"(tz2_)); const int laneb = tz2_ & 63, l15b = laneb & 15, qb = laneb >> 4; (void)l15b; (void)qb

template <int NT, int RING, int P0, int CU> constexpr int issued(int i) {
  int n = P0 < NT ? P0 : NT;
  for (int k = 0; k <= i; ++k) { int m = n + CU; if (m > k + RING) m = k + RING; if (m > NT) m = NT; n = m; }
  return n;
}
constexpr int cap63(int n, int pc) { return n > 63 - pc ? 63 - pc : n; }
template <int I, int IEND, int NT, int RING, int PC, int P0, int CU, int XI, class Issue, class Hook> DEVI void loader_steps(Issue& issue, Hook& hook) {
  if constexpr (I < IEND) {
    constexpr int before = issued<NT, RING, P0, CU>(I - 1), after = issued<NT, RING, P0, CU>(I);
    wait_vm<cap63((before - (I + 2)) * PC, (after - before) * PC)>(); BAR();
    if constexpr (after > before) issue(before, before % RING);
    if constexpr (after > before + 1) issue(before + 1, (before + 1) % RING);
    if constexpr (after > before + 2) issue(before + 2, (before + 2) % RING);
    static_assert(after <= before + 3, "at most three tiles per step");
    hook(I);
    if constexpr (I == XI) BAR();
    loader_steps<I + 1, IEND, NT, RING, PC, P0, CU, XI>(issue, hook);
  }
}
template <int NT, int RING, int P0, int CU> constexpr int steady_from() {
  for (int i = 0; i < NT; ++i) if (issued<NT, RING, P0, CU>(i - 1) == i - 1 + RING) return i;
  return NT;
}
template <int T0, int T1, int PC, class Issue> DEVI void loader_fill(Issue& issue) {
#pragma unroll 1
  for (int t = T0; t < T1; ++t) { if (T1 * PC > 63) wait_vm<63 - PC>(); issue(t, t); }
}
struct NoHook { DEVI void operator()(int) const {} };
template <int NT, int RING, int PC, int P0, int CU, int XI, class Issue, class Hook = NoHook> DEVI void loader_run(Issue& issue, Hook hook = Hook()) {
  constexpr int P = P0 < NT ? P0 : NT;
  wait_vm<cap63((P - 1) * PC, 0)>(); BAR();
  constexpr int S0 = steady_from<NT, RING, P0, CU>(), S1 = NT - RING + 1;
  if constexpr (XI < 0 && S0 + 2 < S1) {
    loader_steps<0, S0, NT, RING, PC, P0, CU, XI>(issue, hook);
    int slot = (S0 - 1 + RING) % RING;
#pragma unroll 1
    for (int i = S0; i < S1; ++i) {
      wait_vm<cap63((RING - 3) * PC, PC)>(); BAR();
      issue(i - 1 + RING, slot);
      hook(i);
      slot = (slot + 1 == RING) ? 0 : slot + 1;
    }
    loader_steps<S1, NT - 1, NT, RING, PC, P0, CU, XI>(issue, hook);
  } else {
    loader_steps<0, NT - 1, NT, RING, PC, P0, CU, XI>(issue, hook);
  }
}
template <int NBAR> DEVI void idle_phase() {
#pragma unroll 1
  for (int i = 0; i < NBAR; ++i) BAR();
}

template <int I, int NMF, int NRD, int PER> struct Interleave {
  static DEVI void run() {
    if constexpr (I < NMF) {
      __builtin_amdgcn_sched_group_barrier(0x8, 1, 0);
      constexpr int left = NRD - I * PER, now = left < PER ? left : PER;
      if constexpr (now > 0) __builtin_amdgcn_sched_group_barrier(0x100, now, 0);
      Interleave<I + 1, NMF, NRD, PER>::run();
    }
  }
};
template <int NMF, int NRD, int PER> DEVI void interleave() { Interleave<0, NMF, NRD, PER>::run(); }

constexpr int AW = 4;
constexpr int PF2 = 15, PF3 = 8, PF4 = 9, PF5 = 14;
constexpr int P1_SA = 6, P1_DB = 5, P1_DOFF = 98304, P1_X = 32768;
constexpr int P2_RING = 15, P2_X = 51200;
constexpr int P3_RING = 8, P3_X = 114688;
constexpr int P4_SA = 9, P4_DB = 5, P4_DOFF = 73728, P4_X = 40960;
constexpr int L_X1 = 145408;
constexpr int P5_RING = 14, P5_X = 0;

DEVI f32x4 sel4(int c, f32x4 a, f32x4 b) { asm volatile("" : "+v"(a), "+v"(b)); return c ? a : b; }
DEVI float sel1(int c, float a, float b) { asm volatile("" : "+v"(a), "+v"(b)); return c ? a : b; }
DEVI void lds_put(LAS char* p, f32x4 v) { *(LAS f32x4*)p = v; }
DEVI f32x4 lds_get(LAS char* p) { return *(LAS f32x4*)p; }
DEVI u64 epoch_tag(u64 e) {
  return (((u64)(unsigned)__builtin_amdgcn_readfirstlane((unsigned)(e >> 32)) << 32) | (unsigned)__builtin_amdgcn_readfirstlane((unsigned)e)) + 1;
}
__global__ __launch_bounds__(768, 3) void adaLN_cond__spectral_fnet__mixer_gmlp__prompt_kernel(Params p) {
  extern __shared__ __attribute__((aligned(16))) char smem_[];
  LAS char* lds = (LAS char*)smem_;
  const int tid = threadIdx.x, wid = __builtin_amdgcn_readfirstlane(tid >> 6);
  const int b = blockIdx.x, xg = b & 7, i8 = b >> 3;
  const int g = xg * 4 + (i8 >> 3), j = i8 & 7;
  char* const ws = p.ws;
  const int rot = xg * 4;

  const u64 Ein = __hip_atomic_load((GAS u64*)(ws + WS_EPOCH), __ATOMIC_RELAXED, __HIP_MEMORY_SCOPE_AGENT);
  if (tid < 32) *((LAS unsigned*)(lds + L_CNT) + tid) = 0u;

  if (wid < 8) {
    const int w = wid, m = w & 3, kh = w >> 2;
    __builtin_amdgcn_s_setprio(2);
    {
      LANE_IDS();
      const rsrc_t rx = mkrs(p.x, SEQ * DM * 2);
      u32x4 v[2];
#pragma unroll
      for (int h = 0; h < 2; ++h) v[h] = ld16<0>(rx, lane < 40 ? (unsigned)((lane + 40 * h) * 16) : OOB, (unsigned)(64 * g + 8 * j + w) * (DM * 2));
      wait_lgkm0();
      BAR();
      float f[16];
#pragma unroll
      for (int h = 0; h < 2; ++h)
#pragma unroll
        for (int i = 0; i < 4; ++i) { f[h * 8 + 2 * i] = bflo(v[h][i]); f[h * 8 + 2 * i + 1] = bfhi(v[h][i]); }
      float s = 0.f, sq = 0.f;
#pragma unroll
      for (int i = 0; i < 16; ++i) { s += f[i]; sq = __builtin_fmaf(f[i], f[i], sq); }
      const f32x2 ls = ln_stats(wave_sum(s), wave_sum(sq), 1.0f / DM);
      const float rstd = ls.y, c0 = -ls.x * ls.y;
      if (lane < 40) {
#pragma unroll
        for (int h = 0; h < 2; ++h) {
          u32x4 o;
#pragma unroll
          for (int i = 0; i < 4; ++i) o[i] = pk2(__builtin_fmaf(f[h * 8 + 2 * i], rstd, c0), __builtin_fmaf(f[h * 8 + 2 * i + 1], rstd, c0));
          *(LAS u32x4*)(lds + L_P0T + w * P0T_RS + (lane + 40 * h) * 16) = o;
        }
      }
      wait_lgkm0(); CFENCE();
      if (lane == 0) __hip_atomic_fetch_add((LAS unsigned*)(lds + L_CNT) + 0, 1u, __ATOMIC_RELAXED, __HIP_MEMORY_SCOPE_WORKGROUP);
      for (unsigned spins = 0; spins < SPIN_MAX; ++spins) {
        if (__hip_atomic_load((LAS unsigned*)(lds + L_CNT) + 0, __ATOMIC_RELAXED, __HIP_MEMORY_SCOPE_WORKGROUP) >= 8u) break;
      }
      CFENCE();
      const rsrc_t rxn = mkrs(ws + WS_XNT, 8 * 32 * 10240);
#pragma unroll
      for (int it = 0; it < 2; ++it) {
        const int pc = it == 0 ? q : 4, nf = 5 * w + pc;
        const int qq = l15 >> 2, pp = l15 & 3;
        LAS char* a0 = lds + L_P0T + qq * P0T_RS + (16 * nf + 4 * pp) * 2;
        const u32x2 lo = __builtin_bit_cast(u32x2, __builtin_amdgcn_ds_read_tr16_b64_v4i16((LAS s16x4*)a0));
        const u32x2 hi = __builtin_bit_cast(u32x2, __builtin_amdgcn_ds_read_tr16_b64_v4i16((LAS s16x4*)(a0 + 4 * P0T_RS)));
        const unsigned off = (unsigned)(((w * 32 + g) * 10 + (j >> 2) * 5 + pc) * 1024 + ((j & 3) * 16 + l15) * 16);
        st16<SC1>((u32x4){lo.x, lo.y, hi.x, hi.y}, rxn, (it == 0 || q == 0) ? off : OOB, 0);
      }
    }
    const u64 T = epoch_tag(Ein);
    { LANE_IDS(); wait_vm<0>(); if (arrive_last(lds, 1, 8, lane)) {
      if (lane < 8) __hip_atomic_store((GAS u64*)(ws + WS_FLAG0 + lane * 4096 + g * 128 + j * 8), T, __ATOMIC_RELAXED, __HIP_MEMORY_SCOPE_AGENT);
      lds_signal(lds, 14, lane); } }

    unsigned z1p[8];

    {
      LANE_IDS();
      f32x4 acc[2][5];
#pragma unroll
      for (int a = 0; a < 2; ++a)
#pragma unroll
        for (int n = 0; n < 5; ++n) acc[a][n] = (f32x4){0.f, 0.f, 0.f, 0.f};
      f32x4 rs[2] = {{0.f, 0.f, 0.f, 0.f}, {0.f, 0.f, 0.f, 0.f}};
      u32x4 ones4 = {0x3f803f80u, 0x3f803f80u, 0x3f803f80u, 0x3f803f80u}; asm volatile("" : "+v"(ones4));
      const bf16x8 ones = asfrag(ones4);
      const unsigned aoff = (m * 16 + l15) * 128 + (((kh * 4 + q) ^ (l15 >> 1)) << 4);
      const unsigned boff = P1_DOFF + kh * 5120 + lane * 16;
      bf16x8 XA[2], XB[5], YA[2], YB[5];
      int sa = 0, sb = 0;
      auto rd = [&](bf16x8(&A)[2], bf16x8(&B)[5]) {
        LAS char* pa = lds + sa * 16384 + aoff;
        LAS char* pb = lds + sb * 10240 + boff;
        A[0] = ldsfrag(pa); A[1] = ldsfrag(pa + 8192);
#pragma unroll
        for (int n = 0; n < 5; ++n) B[n] = ldsfrag(pb + n * 1024);
        sa = (sa + 1 == P1_SA) ? 0 : sa + 1; sb = (sb + 1 == P1_DB) ? 0 : sb + 1;
      };
      auto mm = [&](const bf16x8(&A)[2], const bf16x8(&B)[5]) {
#pragma unroll
        for (int a = 0; a < 2; ++a) {
#pragma unroll
          for (int n = 0; n < 5; ++n) acc[a][n] = mfma(B[n], A[a], acc[a][n]);
          rs[a] = mfma(ones, A[a], rs[a]);
        }
      };
      auto pairs = [&](int n) {
#pragma unroll 1
        for (int i = 0; i < n; ++i) {
          BAR(); rd(YA, YB); mm(XA, XB); interleave<12, 7, 1>(); SCHED();
          BAR(); rd(XA, XB); mm(YA, YB); interleave<12, 7, 1>(); SCHED();
        }
      };
      BAR();
      rd(XA, XB);
      pairs(6);
      u64 fb[4];
      if (w == 0) {
#pragma unroll
        for (int i = 0; i < 4; ++i) fb[i] = __hip_atomic_load((GAS u64*)(ws + WS_FLAGB + (i * 8 + (lane >> 3)) * 128 + (lane & 7) * 8), __ATOMIC_RELAXED, __HIP_MEMORY_SCOPE_AGENT);
      }
      pairs(3);
      f32x4 mp[2][4];
      const int vs0 = lane < 20 ? 1 : lane < 40 ? 0 : 2, vs1 = lane < 20 ? 4 : 5;
      const int vl = lane < 20 ? lane : lane < 40 ? lane - 20 : lane - 40;
      if (w == 0) {
        if (!__all(fb[0] == T && fb[1] == T && fb[2] == T && fb[3] == T)) poll_flags256(ws, WS_FLAGB, T, lane);
        CFENCE();
        const rsrc_t rm = mkrs(ws + WS_MODP, 4 * 3840 * 4);
#pragma unroll
        for (int k = 0; k < 4; ++k) {
          mp[0][k] = __builtin_bit_cast(f32x4, ld16<SC1>(rm, lane < 60 ? (unsigned)((vs0 * DM + 80 * j + vl * 4) * 4) : OOB, k * 3840 * 4));
          mp[1][k] = __builtin_bit_cast(f32x4, ld16<SC1>(rm, lane < 40 ? (unsigned)((vs1 * DM + 80 * j + vl * 4) * 4) : OOB, k * 3840 * 4));
        }
      }
      pairs(3);
      if (w == 0) {
        f32x4 s0 = (mp[0][0] + mp[0][1]) + (mp[0][2] + mp[0][3]), s1 = (mp[1][0] + mp[1][1]) + (mp[1][2] + mp[1][3]);
#pragma unroll
        for (int i = 0; i < 4; ++i) {
          s0[i] = rbf(s0[i]); if (lane < 20) s0[i] = rbf(1.0f + s0[i]);
          s1[i] = rbf(s1[i]); if (lane < 20) s1[i] = rbf(1.0f + s1[i]);
        }
        if (lane < 60) lds_put(lds + L_TAB + ((lane < 20 ? 0 : lane < 40 ? 1 : 2) * 80 + vl * 4) * 4, s0);
        if (lane < 40) lds_put(lds + L_TAB + ((lane < 20 ? 3 : 4) * 80 + vl * 4) * 4, s1);
      }
      pairs(3);
      BAR(); rd(YA, YB); mm(XA, XB); interleave<12, 7, 1>(); SCHED();
      mm(YA, YB);

      {
        LAS char* xo = lds + P1_X + w * 6144 + lane * 16;
        LAS char* xi = lds + P1_X + (w ^ 4) * 6144 + lane * 16;
#pragma unroll
        for (int n = 0; n < 5; ++n) lds_put(xo + n * 1024, sel4(kh, acc[0][n], acc[1][n]));
        *(LAS float*)(lds + P1_X + w * 6144 + 5120 + lane * 4) = sel1(kh, rs[0][0], rs[1][0]);
        wait_lgkm0();
        BAR();
        const float rsum = sel1(kh, rs[1][0], rs[0][0]) + *(LAS float*)(lds + P1_X + (w ^ 4) * 6144 + 5120 + lane * 4);
        u32x2 pkd[5];
#pragma unroll
        for (int n = 0; n < 5; ++n) {
          const f32x4 a = sel4(kh, acc[1][n], acc[0][n]) + lds_get(xi + n * 1024);
          const f32x4 c1 = lds_get(lds + L_TAB + (0 * 80 + 16 * n + 4 * q) * 4), sa4 = lds_get(lds + L_TAB + (1 * 80 + 16 * n + 4 * q) * 4);
          f32x4 v = a * c1 + rsum * sa4;
          if (kh) v = -v;
          pkd[n] = (u32x2){pk2(v[0], v[1]), pk2(v[2], v[3])};
        }
        const rsrc_t rpq = mkrs(ws + WS_PQ, 32 * 40 * 4 * 1024);
        auto pq_off = [&](int nf) { const int c = 10 * j + 2 * nf + (q >> 1); return (unsigned)(((g * 40 + kh * 20 + (c >> 2)) * 4 + m) * 1024 + ((c & 3) * 16 + l15) * 16); };
        const int odd = q & 1;
        st16<SC1>(pair_swap(pkd[0], pkd[1]), rpq, pq_off(odd), 0);
        st16<SC1>(pair_swap(pkd[2], pkd[3]), rpq, pq_off(2 + odd), 0);
        st8<SC1>(pkd[4], rpq, pq_off(4) + odd * 8, 0);
      }
      BAR();
      publish(lds, 3, 8, ws, WS_FLAG1 + (g * 8 + j) * 128, T, lane);
    }

    {
      LANE_IDS();
      constexpr int NT = 20;
      const int nb = kh ? 3 : 0;
      group_wait(lds, 8, ws, WS_FLAG1 + g * 1024, 8, T, w == 0, lane);
      const rsrc_t ra = mkrs(ws + WS_PQ + (size_t)g * 40 * 4 * 1024, 40 * 4 * 1024);
      u32x4 A[NT];
#pragma unroll
      for (int t = 0; t < AW; ++t) A[t] = ld16<SC1>(ra, lane * 16, ((2 * t + kh) * 4 + m) * 1024);
      f32x4 acc[5];
#pragma unroll
      for (int n = 0; n < 5; ++n) acc[n] = (f32x4){0.f, 0.f, 0.f, 0.f};
      const unsigned boff = l15 * 128 + (((kh * 4 + q) ^ (l15 >> 1)) << 4);
      u32x2 xin[3];
      bf16x8 B[2][5];
      auto rd = [&](bf16x8(&Bv)[5], int slot) {
#pragma unroll
        for (int n = 0; n < 5; ++n) Bv[n] = ldsfrag(lds + slot * 10240 + boff + n * 2048);
      };
      BAR();
      rd(B[0], 0);
#pragma unroll
      for (int t = 0; t < NT; ++t) {
        if (t == NT - 3) {
          LANE_IDS2();
          const rsrc_t rx = mkrs(p.x, SEQ * DM * 2);
#pragma unroll
          for (int n = 0; n < 3; ++n) xin[n] = ld8<0>(rx, (kh && n == 2) ? OOB : (unsigned)((64 * g + 16 * m + l15b) * (DM * 2) + (80 * j + 16 * (nb + n) + 4 * qb) * 2), 0);
        }
        if (t + 1 < NT) { BAR(); rd(B[(t + 1) & 1], (t + 1) % P2_RING); }
        if (t + AW < NT) A[t + AW] = ld16<SC1>(ra, lane * 16, ((2 * (t + AW) + kh) * 4 + m) * 1024);
#pragma unroll
        for (int n = 0; n < 5; ++n) acc[n] = mfma(B[t & 1][n], asfrag(A[t]), acc[n]);
        interleave<5, 5, 1>(); SCHED();
      }

      LAS char* xo = lds + P2_X + w * 3072 + lane * 16;
      LAS char* xi = lds + P2_X + (w ^ 4) * 3072 + lane * 16;
      if (kh) { lds_put(xo, acc[0]); lds_put(xo + 1024, acc[1]); lds_put(xo + 2048, acc[2]); }
      else { lds_put(xo, acc[3]); lds_put(xo + 1024, acc[4]); }
      wait_lgkm0();
      BAR();
      float s = 0.f, sq = 0.f;
      u32x2 cp[3]; unsigned x1p[6];
      auto fin = [&](int n, f32x4 own) {
        const f32x4 y = own + lds_get(xi + n * 1024);
        const int col = 16 * (nb + n) + 4 * q;
        const f32x4 ga = lds_get(lds + L_TAB + (2 * 80 + col) * 4), cb1 = lds_get(lds + L_TAB + (3 * 80 + col) * 4);
        const f32x4 xv = {bflo(xin[n].x), bfhi(xin[n].x), bflo(xin[n].y), bfhi(xin[n].y)};
        const f32x4 x1 = xv + ga * y, c = x1 * cb1;
        x1p[2 * n] = pk2(x1[0], x1[1]); x1p[2 * n + 1] = pk2(x1[2], x1[3]);
        cp[n] = (u32x2){pk2(c[0], c[1]), pk2(c[2], c[3])};
        s += (x1[0] + x1[1]) + (x1[2] + x1[3]);
        sq = __builtin_fmaf(x1[0], x1[0], __builtin_fmaf(x1[1], x1[1], __builtin_fmaf(x1[2], x1[2], __builtin_fmaf(x1[3], x1[3], sq))));
      };
      if (kh) { fin(0, acc[3]); fin(1, acc[4]); cp[2] = (u32x2){0u, 0u}; x1p[4] = 0u; x1p[5] = 0u; }
      else { fin(0, acc[0]); fin(1, acc[1]); fin(2, acc[2]); }
      s = xq_sum(s); sq = xq_sum(sq);
      const rsrc_t rc = mkrs(ws + WS_X1C, 32 * 20 * 4 * 1024);
      auto c_off = [&](int nf) { const int c = 10 * j + 2 * nf + (q >> 1); return (unsigned)(((g * 20 + (c >> 2)) * 4 + m) * 1024 + ((c & 3) * 16 + l15) * 16); };
      const int odd = q & 1;
      st16<SC1>(pair_swap(cp[0], cp[1]), rc, c_off(nb + odd), 0);
      st8<SC1>(cp[2], rc, kh ? OOB : c_off(2) + odd * 8, 0);
      const rsrc_t rs1 = mkrs(ws + WS_ST1, SEQ * 16 * 8);
      st8<SC1>((u32x2){__float_as_uint(s), __float_as_uint(sq)}, rs1, q == 0 ? (unsigned)(((64 * g + 16 * m + l15) * 16 + 2 * j + kh) * 8) : OOB, 0);
#pragma unroll
      for (int n = 0; n < 3; ++n) *(LAS u32x2*)(lds + L_X1 + (w * 3 + n) * 512 + lane * 8) = (u32x2){x1p[2 * n], x1p[2 * n + 1]};
      wait_lgkm0();
      BAR();
      publish(lds, 4, 8, ws, WS_FLAG2 + (g * 8 + j) * 128, T, lane);
    }

    {
      LANE_IDS();
      constexpr int NT = 10;
      const int nb = kh ? 4 : 0;
      {
        const rsrc_t ruv = mkrs(ws + WS_UV, 2 * 896 * 16);
        const int col = w * 28 + lane, part = col >= 112 ? 1 : 0, cc = col - 112 * part;
        const unsigned off = lane < 28 ? (unsigned)((part * 896 + 112 * j + cc) * 16) : OOB;
        u32x4 gv;
        for (unsigned spins = 0;; ++spins) {
          gv = ld16<SC1>(ruv, off, 0);
          const bool ok = lane >= 28 || (gv.z == (unsigned)T && gv.w == (unsigned)(T >> 32));
          if (__all(ok)) break;
          if (spins > SPIN_MAX) { note_timeout(ws, 0x300, lane); break; }
          __builtin_amdgcn_s_sleep(1); CFENCE();
        }
        if (lane < 28) { *(LAS unsigned*)(lds + L_UV + col * 4) = gv.x; *(LAS unsigned*)(lds + L_UV + (224 + col) * 4) = gv.y; }
      }
      group_wait(lds, 9, ws, WS_FLAG2 + g * 1024, 8, T, w == 0, lane);
      const rsrc_t ra = mkrs(ws + WS_X1C + (size_t)g * 20 * 4 * 1024, 20 * 4 * 1024);
      u32x4 A[NT];
#pragma unroll
      for (int t = 0; t < AW; ++t) A[t] = ld16<SC1>(ra, lane * 16, ((2 * t + kh) * 4 + m) * 1024);
      const rsrc_t rs1 = mkrs(ws + WS_ST1, SEQ * 16 * 8);
      const unsigned s1o = (unsigned)(((64 * g + 16 * m + l15) * 16 + 4 * q) * 8);
      const u32x4 st0 = ld16<SC1>(rs1, s1o, 0), st1 = ld16<SC1>(rs1, s1o + 16, 0);
      f32x4 acc[7];
      bf16x8 B[2][7];
      auto rd = [&](bf16x8(&Bv)[7], int slot) {
#pragma unroll
        for (int n = 0; n < 7; ++n) Bv[n] = ldsfrag(lds + slot * 14336 + kh * 7168 + n * 1024 + lane * 16);
      };
      wait_lgkm0();
      BAR();
      rd(B[0], 0); wait_lgkm0();
      float mean1, rstd1;
      {
        const float sm = xq_sum((__uint_as_float(st0.x) + __uint_as_float(st0.z)) + (__uint_as_float(st1.x) + __uint_as_float(st1.z)));
        const float sq = xq_sum((__uint_as_float(st0.y) + __uint_as_float(st0.w)) + (__uint_as_float(st1.y) + __uint_as_float(st1.w)));
        const f32x2 ls = ln_stats(sm, sq, 1.0f / DM);
        mean1 = ls.x; rstd1 = ls.y;
      }
      LAS char* xo = lds + P3_X + (kh ? 12288 + m * 4096 : m * 3072) + lane * 16;
      LAS char* xi = lds + P3_X + (kh ? m * 3072 : 12288 + m * 4096) + lane * 16;
#pragma unroll
      for (int n = 0; n < 7; ++n) acc[n] = (f32x4){0.f, 0.f, 0.f, 0.f};
#pragma unroll
      for (int t = 0; t < NT; ++t) {
        BAR(); if (t + 1 < NT) rd(B[(t + 1) & 1], (t + 1) % P3_RING);
        if (t + AW < NT) A[t + AW] = ld16<SC1>(ra, lane * 16, ((2 * (t + AW) + kh) * 4 + m) * 1024);
#pragma unroll
        for (int n = 0; n < 7; ++n) acc[n] = mfma(asfrag(A[t]), B[t & 1][n], acc[n]);
        interleave<7, 7, 1>(); SCHED();
      }

      if (kh) {
#pragma unroll
        for (int n = 0; n < 4; ++n) lds_put(xo + n * 1024, acc[n]);
      } else {
#pragma unroll
        for (int n = 0; n < 3; ++n) lds_put(xo + n * 1024, acc[4 + n]);
      }
      wait_lgkm0();
      BAR();
      u32x2 z2p[4];
      {
        float rr[4], c1[4];
#pragma unroll
        for (int r = 0; r < 4; ++r) { rr[r] = __shfl(rstd1, 4 * q + r); c1[r] = __shfl(-rstd1 * mean1, 4 * q + r); }
        float zs[4] = {0.f, 0.f, 0.f, 0.f}, zq[4] = {0.f, 0.f, 0.f, 0.f};
        auto fin = [&](int n, f32x4 own) {
          const f32x4 a = own + lds_get(xi + n * 1024);
          const int col = 16 * (nb + n) + l15;
          const float u = *(LAS float*)(lds + L_UV + col * 4), v = *(LAS float*)(lds + L_UV + (224 + col) * 4);
          float z[4];
#pragma unroll
          for (int r = 0; r < 4; ++r) {
            z[r] = gelu_tanh(__builtin_fmaf(rr[r], a[r], __builtin_fmaf(c1[r], u, v)));
            zs[r] += z[r]; zq[r] = __builtin_fmaf(z[r], z[r], zq[r]);
          }
          z2p[n] = (u32x2){pk2(z[0], z[1]), pk2(z[2], z[3])};
        };
        if (kh) { fin(0, acc[4]); fin(1, acc[5]); fin(2, acc[6]); z2p[3] = (u32x2){0u, 0u}; }
        else { fin(0, acc[0]); fin(1, acc[1]); fin(2, acc[2]); fin(3, acc[3]); }
#pragma unroll
        for (int r = 0; r < 4; ++r) { zs[r] = row16_sum(zs[r]); zq[r] = row16_sum(zq[r]); }
        const float ms = l15 == 0 ? zs[0] : l15 == 1 ? zs[1] : l15 == 2 ? zs[2] : zs[3];
        const float Ms = l15 == 0 ? zq[0] : l15 == 1 ? zq[1] : l15 == 2 ? zq[2] : zq[3];
        const rsrc_t rs2 = mkrs(ws + WS_ST2, SEQ * 16 * 16);
        st16<SC1>((u32x4){__float_as_uint(ms), __float_as_uint(Ms), (unsigned)T, (unsigned)(T >> 32)}, rs2,
                  l15 < 4 ? (unsigned)(((64 * g + 16 * m + 4 * q + l15) * 16 + 2 * j + kh) * 16) : OOB, 0);
      }
      rd(B[0], NT % P3_RING); wait_lgkm0();
      const rsrc_t rs2 = mkrs(ws + WS_ST2, SEQ * 16 * 16);
      const unsigned soff = (unsigned)(((64 * g + 16 * m + l15) * 16 + 4 * q) * 16);
      u32x4 gs[4];
#pragma unroll
      for (int n = 0; n < 7; ++n) acc[n] = (f32x4){0.f, 0.f, 0.f, 0.f};
#pragma unroll
      for (int t = 0; t < NT; ++t) {
        if (t + 1 < NT) { BAR(); rd(B[(t + 1) & 1], (NT + t + 1) % P3_RING); }
        if (t == GST) {
#pragma unroll
          for (int i = 0; i < 4; ++i) gs[i] = ld16<SC1>(rs2, soff + 16 * i, 0);
        }
#pragma unroll
        for (int n = 0; n < 7; ++n) acc[n] = mfma(B[t & 1][n], asfrag(A[t]), acc[n]);
        interleave<7, 7, 1>(); SCHED();
      }

      for (unsigned spins = 0;; ++spins) {
        bool ok = true;
#pragma unroll
        for (int i = 0; i < 4; ++i) ok &= gs[i].z == (unsigned)T && gs[i].w == (unsigned)(T >> 32);
        if (__all(ok)) break;
        if (spins > SPIN_MAX) { note_timeout(ws, 0x400, lane); break; }
        __builtin_amdgcn_s_sleep(1); CFENCE();
#pragma unroll
        for (int i = 0; i < 4; ++i) gs[i] = ld16<SC1>(rs2, soff + 16 * i, 0);
      }
      {
        const float sm = xq_sum((__uint_as_float(gs[0].x) + __uint_as_float(gs[1].x)) + (__uint_as_float(gs[2].x) + __uint_as_float(gs[3].x)));
        const float sq = xq_sum((__uint_as_float(gs[0].y) + __uint_as_float(gs[1].y)) + (__uint_as_float(gs[2].y) + __uint_as_float(gs[3].y)));
        const f32x2 ls = ln_stats(sm, sq, 1.0f / FH);
        float rr[4], c2[4];
#pragma unroll
        for (int r = 0; r < 4; ++r) { rr[r] = __shfl(ls.y, 4 * q + r); c2[r] = __shfl(-ls.x * ls.y, 4 * q + r); }
        u32x2 zn[4];
#pragma unroll
        for (int n = 0; n < 4; ++n)
          zn[n] = (u32x2){pk2(__builtin_fmaf(bflo(z2p[n].x), rr[0], c2[0]), __builtin_fmaf(bfhi(z2p[n].x), rr[1], c2[1])),
                          pk2(__builtin_fmaf(bflo(z2p[n].y), rr[2], c2[2]), __builtin_fmaf(bfhi(z2p[n].y), rr[3], c2[3]))};
        const rsrc_t rz2 = mkrs(ws + WS_Z2T, 8 * 32 * 14336);
        auto z_off = [&](int nf) { return (unsigned)(((j * 32 + g) * 14 + (m >> 1) * 7 + nf) * 1024 + ((2 * (m & 1) + (q >> 1)) * 16 + l15) * 16); };
        const int odd = q & 1;
        st16<SC1>(pair_swap(zn[0], zn[1]), rz2, z_off(nb + odd), 0);
        if (kh) st8<SC1>(zn[2], rz2, z_off(6) + odd * 8, 0);
        else st16<SC1>(pair_swap(zn[2], zn[3]), rz2, z_off(2 + odd), 0);
      }

      BAR();
      publish(lds, 5, 8, ws, WS_FLAG3 + (j * 32 + g) * 128, T, lane);
      if (kh) {
#pragma unroll
        for (int n = 0; n < 4; ++n) lds_put(xo + n * 1024, acc[n]);
      } else {
#pragma unroll
        for (int n = 0; n < 3; ++n) lds_put(xo + n * 1024, acc[4 + n]);
      }
      wait_lgkm0();
      BAR();
      {
        const float c1 = -rstd1 * mean1;
        auto fin = [&](int n, f32x4 own) {
          const f32x4 a = own + lds_get(xi + n * 1024);
          const int col = 112 + 16 * (nb + n) + 4 * q;
          const f32x4 u = lds_get(lds + L_UV + col * 4), v = lds_get(lds + L_UV + (224 + col) * 4);
          const f32x4 pre = a * rstd1 + (u * c1 + v);
          z1p[2 * n] = pk2(gelu_tanh(pre[0]), gelu_tanh(pre[1])); z1p[2 * n + 1] = pk2(gelu_tanh(pre[2]), gelu_tanh(pre[3]));
        };
        if (kh) { fin(0, acc[4]); fin(1, acc[5]); fin(2, acc[6]); z1p[6] = 0u; z1p[7] = 0u; }
        else { fin(0, acc[0]); fin(1, acc[1]); fin(2, acc[2]); fin(3, acc[3]); }
      }
#pragma unroll
      for (int i = 0; i < 8; ++i) asm volatile("" : "+v"(z1p[i]));
    }

    {
      LANE_IDS();
      const int mh = m >> 1, k4 = m & 1, nh = kh;
      f32x4 acc[2][4];
#pragma unroll
      for (int a = 0; a < 2; ++a)
#pragma unroll
        for (int n = 0; n < 4; ++n) acc[a][n] = (f32x4){0.f, 0.f, 0.f, 0.f};
      const unsigned aoff = (mh * 32 + l15) * 128 + (((k4 * 4 + q) ^ (l15 >> 1)) << 4);
      const unsigned boff = P4_DOFF + k4 * 7168 + nh * 4096 + lane * 16;
      const rsrc_t rb = mkrs(p.bsp, SEQ * 2);
      const float bs = bflo(ld2<0>(rb, (64 * g + 16 * m + l15) * 2, 0));
      bf16x8 XA[2], XB[4], YA[2], YB[4];
      int sa = 0, sb = 0;
      if (nh == 0) {
        auto rd = [&](bf16x8(&A)[2], bf16x8(&B)[4]) {
          LAS char* pa = lds + sa * 8192 + aoff;
          LAS char* pb = lds + sb * 14336 + boff;
          A[0] = ldsfrag(pa); A[1] = ldsfrag(pa + 2048);
#pragma unroll
          for (int n = 0; n < 4; ++n) B[n] = ldsfrag(pb + n * 1024);
          sa = (sa + 1 == P4_SA) ? 0 : sa + 1; sb = (sb + 1 == P4_DB) ? 0 : sb + 1;
        };
        auto mm = [&](const bf16x8(&A)[2], const bf16x8(&B)[4]) {
#pragma unroll
          for (int a = 0; a < 2; ++a)
#pragma unroll
            for (int n = 0; n < 4; ++n) acc[a][n] = mfma(B[n], A[a], acc[a][n]);
        };
        BAR();
        rd(XA, XB);
#pragma unroll 1
        for (int i = 0; i < 15; ++i) {
          BAR(); rd(YA, YB); mm(XA, XB); interleave<8, 6, 1>(); SCHED();
          BAR(); rd(XA, XB); mm(YA, YB); interleave<8, 6, 1>(); SCHED();
        }
        BAR(); rd(YA, YB); mm(XA, XB); interleave<8, 6, 1>(); SCHED();
        mm(YA, YB);
      } else {
        auto rd = [&](bf16x8(&A)[2], bf16x8(&B)[4]) {
          LAS char* pa = lds + sa * 8192 + aoff;
          LAS char* pb = lds + sb * 14336 + boff;
          A[0] = ldsfrag(pa); A[1] = ldsfrag(pa + 2048);
#pragma unroll
          for (int n = 0; n < 3; ++n) B[n] = ldsfrag(pb + n * 1024);
          sa = (sa + 1 == P4_SA) ? 0 : sa + 1; sb = (sb + 1 == P4_DB) ? 0 : sb + 1;
        };
        auto mm = [&](const bf16x8(&A)[2], const bf16x8(&B)[4]) {
#pragma unroll
          for (int a = 0; a < 2; ++a)
#pragma unroll
            for (int n = 0; n < 3; ++n) acc[a][n] = mfma(B[n], A[a], acc[a][n]);
        };
        BAR();
        rd(XA, XB);
#pragma unroll 1
        for (int i = 0; i < 15; ++i) {
          BAR(); rd(YA, YB); mm(XA, XB); interleave<6, 5, 1>(); SCHED();
          BAR(); rd(XA, XB); mm(YA, YB); interleave<6, 5, 1>(); SCHED();
        }
        BAR(); rd(YA, YB); mm(XA, XB); interleave<6, 5, 1>(); SCHED();
        mm(YA, YB);
      }

      LAS char* xo = lds + P4_X + w * 4096 + lane * 16;
      LAS char* xi = lds + P4_X + (w ^ 1) * 4096 + lane * 16;
#pragma unroll
      for (int n = 0; n < 4; ++n) lds_put(xo + n * 1024, sel4(k4, acc[0][n], acc[1][n]));
      wait_lgkm0();
      BAR();
      u32x2 up[4];
#pragma unroll
      for (int n = 0; n < 4; ++n) {
        const f32x4 gv = sel4(k4, acc[1][n], acc[0][n]) + lds_get(xi + n * 1024) + bs;
        up[n] = (u32x2){pk2(bflo(z1p[2 * n]) * gv[0], bfhi(z1p[2 * n]) * gv[1]), pk2(bflo(z1p[2 * n + 1]) * gv[2], bfhi(z1p[2 * n + 1]) * gv[3])};
      }
      const rsrc_t ru = mkrs(ws + WS_U, 32 * 28 * 4 * 1024);
      auto u_off = [&](int nf) { const int c = 14 * j + 2 * nf + (q >> 1); return (unsigned)(((g * 28 + (c >> 2)) * 4 + m) * 1024 + ((c & 3) * 16 + l15) * 16); };
      const int odd = q & 1;
      st16<SC1>(pair_swap(up[0], up[1]), ru, u_off(4 * nh + odd), 0);
      if (nh) st8<SC1>(up[2], ru, u_off(6) + odd * 8, 0);
      else st16<SC1>(pair_swap(up[2], up[3]), ru, u_off(2 + odd), 0);

      BAR();
      publish(lds, 6, 8, ws, WS_FLAG4 + (g * 8 + j) * 128, T, lane);
      if (b == 0 && w == 0 && lane == 0) __hip_atomic_store((GAS u64*)(ws + WS_EPOCH), T, __ATOMIC_RELAXED, __HIP_MEMORY_SCOPE_AGENT);
    }

    {
      LANE_IDS();
      constexpr int NT = 14;
      const int nb = kh ? 3 : 0;
      group_wait(lds, 10, ws, WS_FLAG4 + g * 1024, 8, T, w == 0, lane);
      const rsrc_t ra = mkrs(ws + WS_U + (size_t)g * 28 * 4 * 1024, 28 * 4 * 1024);
      u32x4 A[NT];
#pragma unroll
      for (int t = 0; t < AW; ++t) A[t] = ld16<SC1>(ra, lane * 16, ((2 * t + kh) * 4 + m) * 1024);
      f32x4 acc[5];
#pragma unroll
      for (int n = 0; n < 5; ++n) acc[n] = (f32x4){0.f, 0.f, 0.f, 0.f};
      bf16x8 B[2][5];
      auto rd = [&](bf16x8(&Bv)[5], int slot) {
#pragma unroll
        for (int n = 0; n < 5; ++n) Bv[n] = ldsfrag(lds + slot * 10240 + kh * 5120 + n * 1024 + lane * 16);
      };
      BAR();
      rd(B[0], 0);
#pragma unroll
      for (int t = 0; t < NT; ++t) {
        if (t + 1 < NT) { BAR(); rd(B[(t + 1) & 1], t + 1); }
        if (t + AW < NT) A[t + AW] = ld16<SC1>(ra, lane * 16, ((2 * (t + AW) + kh) * 4 + m) * 1024);
#pragma unroll
        for (int n = 0; n < 5; ++n) acc[n] = mfma(B[t & 1][n], asfrag(A[t]), acc[n]);
        interleave<5, 5, 1>(); SCHED();
      }

      LAS char* xo = lds + P5_X + w * 3072 + lane * 16;
      LAS char* xi = lds + P5_X + (w ^ 4) * 3072 + lane * 16;
      if (kh) { lds_put(xo, acc[0]); lds_put(xo + 1024, acc[1]); lds_put(xo + 2048, acc[2]); }
      else { lds_put(xo, acc[3]); lds_put(xo + 1024, acc[4]); }
      wait_lgkm0();
      BAR();
      u32x2 op[3];
#pragma unroll
      for (int n = 0; n < 3; ++n) {
        const u32x2 x1v = *(LAS u32x2*)(lds + L_X1 + (w * 3 + n) * 512 + lane * 8);
        const unsigned x1p[6] = {x1v.x, x1v.y, x1v.x, x1v.y, x1v.x, x1v.y};
        const f32x4 o = sel4(kh, acc[n == 2 ? 4 : 3 + n], acc[n]) + lds_get(xi + n * 1024);
        const int col = (kh && n == 2) ? 0 : 16 * (nb + n) + 4 * q;
        const f32x4 gb = lds_get(lds + L_TAB + (4 * 80 + col) * 4);
        op[n] = (u32x2){pk2(__builtin_fmaf(gb[0], o[0], bflo(x1p[2 * n])), __builtin_fmaf(gb[1], o[1], bfhi(x1p[2 * n]))),
                        pk2(__builtin_fmaf(gb[2], o[2], bflo(x1p[2 * n + 1])), __builtin_fmaf(gb[3], o[3], bfhi(x1p[2 * n + 1])))};
      }
      const rsrc_t ro = mkrs(p.out, SEQ * DM * 2);
      const unsigned rowb = (unsigned)((64 * g + 16 * m + l15) * (DM * 2) + 80 * j * 2);
      const int odd = q & 1;
      st16<SC1>(pair_swap(op[0], op[1]), ro, rowb + (16 * (nb + odd) + 8 * (q >> 1)) * 2, 0);
      st8<SC1>(op[2], ro, kh ? OOB : rowb + (32 + 4 * q) * 2, 0);
    }
  } else if (wid < 10) {
    LANE_IDS();
    const int h = wid - 8;
    const unsigned sw0 = (unsigned)(((lane & 7) ^ (lane >> 4)) * 16), sw1 = sw0 ^ 64;
    wait_lgkm0();
    BAR();
    {
      const rsrc_t r = mkrs((h ? p.wsim : p.wsre) + (size_t)g * 64 * SEQ, 64 * SEQ * 2);
      const unsigned v0 = (lane >> 3) * 4096 + sw0, v1 = (lane >> 3) * 4096 + sw1;
      auto issue = [&](int t, int slot) {
        const unsigned k = (unsigned)((t + rot) & 31) * 128;
#pragma unroll
        for (int pc = 0; pc < 8; ++pc) dma16<0>(r, lds + slot * 16384 + (h * 8 + pc) * 1024, (pc & 1) ? v1 : v0, k + pc * 32768);
      };
      auto touch = [&](int it) {
        __builtin_amdgcn_raw_ptr_buffer_load_lds(r, (LAS void*)(lds + L_SINK), 4, (int)((j * 8 + (lane >> 3)) * 4096 + ((it * 8 + (lane & 7) + rot) & 31) * 128), 0, 0, 0);
      };
      lds_wait(lds, 15); lds_wait(lds, 16);
      if (h == 1) {
        const int cg = b % 60, kq = b / 60;
        const rsrc_t rw = mkrs(p.wmod, b < 240 ? DM * 3840 * 2 : 0), rc = mkrs(p.c, DM * 2);
        u32x4 wv[20]; unsigned cv[20];
#pragma unroll
        for (int it = 0; it < 20; ++it) {
          const int row = (kq & 3) * 160 + it * 8 + (lane >> 3);
          wv[it] = ld16<0>(rw, (unsigned)(row * 7680 + cg * 128 + (lane & 7) * 16), 0);
          cv[it] = ld2<0>(rc, row * 2, 0);
        }
        touch(0);
        float a8[8] = {0.f, 0.f, 0.f, 0.f, 0.f, 0.f, 0.f, 0.f};
#pragma unroll
        for (int it = 0; it < 20; ++it) {
          const float cf = bflo(cv[it]);
          const float sl = rbf(cf * __builtin_amdgcn_rcpf(1.0f + __builtin_amdgcn_exp2f(-1.4426950408889634f * cf)));
#pragma unroll
          for (int i = 0; i < 4; ++i) { a8[2 * i] += sl * bflo(wv[it][i]); a8[2 * i + 1] += sl * bfhi(wv[it][i]); }
        }
#pragma unroll
        for (int i = 0; i < 8; ++i) { a8[i] += __shfl_xor(a8[i], 8); a8[i] = xq_sum(a8[i]); }
        const rsrc_t rm = mkrs(ws + WS_MODP, 4 * 3840 * 4);
        const unsigned mo = (unsigned)((kq * 3840 + cg * 64 + (lane & 7) * 8) * 4);
        const bool on = lane < 8 && b < 240;
        st16<SC1>((u32x4){__float_as_uint(a8[0]), __float_as_uint(a8[1]), __float_as_uint(a8[2]), __float_as_uint(a8[3])}, rm, on ? mo : OOB, 0);
        st16<SC1>((u32x4){__float_as_uint(a8[4]), __float_as_uint(a8[5]), __float_as_uint(a8[6]), __float_as_uint(a8[7])}, rm, on ? mo + 16 : OOB, 0);
        wait_vm<0>();
        if (arrive_last(lds, 7, 3, lane)) set_flag(ws, WS_FLAGB + g * 128 + j * 8, epoch_tag(Ein), lane);
      } else {
        touch(0);
      }
      lds_wait(lds, 14);
      loader_fill<0, 2, 8>(issue);
      loader_run<32, P1_SA, 8, 2, 2, -1>(issue, [&](int i) { if (i == TH1) touch(1); if (i == TH2) touch(2); if (i == TH3) touch(3); });
      BAR(); BAR();
    }
    {
      const unsigned v0 = (lane >> 3) * 1280 + (h ? sw1 : sw0), v1 = (lane >> 3) * 1280 + (h ? sw0 : sw1);
      auto issue = [&](int t, int slot) {
        const int im = t >= 10;
        const rsrc_t r = mkrs((im ? p.wdim : p.wdre) + (size_t)j * 80 * DM, 80 * DM * 2);
        const unsigned k = (unsigned)(t - 10 * im) * 128 + h * 5 * 8 * 1280;
#pragma unroll
        for (int pc = 0; pc < 5; ++pc) dma16<0>(r, lds + slot * 10240 + (h * 5 + pc) * 1024, (pc & 1) ? v1 : v0, k + pc * 8 * 1280);
      };
      loader_fill<0, PF2, 5>(issue);
      loader_run<20, P2_RING, 5, PF2, 2, -1>(issue);
      BAR(); BAR();
    }
    {
      const rsrc_t r = mkrs(ws + WS_WINR + (size_t)j * 20 * 14336, 20 * 14336);
      auto issue = [&](int t, int slot) {
#pragma unroll
        for (int pc = 0; pc < 7; ++pc) dma16<SC1>(r, lds + slot * 14336 + (h * 7 + pc) * 1024, lane * 16, (unsigned)(t * 14336 + (h * 7 + pc) * 1024));
      };
      loader_fill<0, PF3, 7>(issue);
      loader_run<20, P3_RING, 7, PF3, 2, 9>(issue);
      BAR();
    }
    {
      const rsrc_t r = mkrs(p.wsp + (size_t)g * 64 * SEQ, 64 * SEQ * 2);
      const unsigned v0 = (lane >> 3) * 4096 + sw0, v1 = (lane >> 3) * 4096 + sw1;
      auto issue = [&](int t, int slot) {
        const unsigned k = (unsigned)((t + rot) & 31) * 128 + h * 4 * 32768;
#pragma unroll
        for (int pc = 0; pc < 4; ++pc) dma16<0>(r, lds + slot * 8192 + (h * 4 + pc) * 1024, (pc & 1) ? v1 : v0, k + pc * 32768);
      };
      loader_fill<0, PF4, 4>(issue);
      BAR();
      loader_run<32, P4_SA, 4, PF4, 2, -1>(issue);
      BAR(); BAR();
    }
    {
      const rsrc_t r = mkrs(ws + WS_WOUTR + (size_t)j * 14 * 10240, 14 * 10240);
      auto issue = [&](int t, int slot) {
#pragma unroll
        for (int pc = 0; pc < 5; ++pc) dma16<SC1>(r, lds + slot * 10240 + (h * 5 + pc) * 1024, lane * 16, (unsigned)(t * 10240 + (h * 5 + pc) * 1024));
      };
      loader_fill<0, PF5, 5>(issue);
      loader_run<14, P5_RING, 5, PF5, 2, -1>(issue);
      BAR();
    }
  } else {
    LANE_IDS();
    const int h = wid - 10;
    wait_lgkm0();
    BAR();
    {
      LAS char* sc = lds + L_RT + h * 10240;
      const int u = b * 2 + h;
      const int qq = l15 >> 2, pp = l15 & 3;
      if (u < 320) {
        const int jp = u / 20, kf = u - 20 * jp, jj = jp >> 1, part = jp & 1;
        const rsrc_t r = mkrs(p.win, DM * FF * 2);
#pragma unroll
        for (int pc = 0; pc < 7; ++pc) {
          const int idx = pc * 64 + lane, row = idx / 14, ch = idx - 14 * row;
          const bool ok = 112 * jj + 8 * ch < FH;
          dma16<0>(r, sc + pc * 1024, ok ? (unsigned)((32 * kf + row) * (FF * 2) + (part ? 0 : FH * 2) + 224 * jj + ch * 16) : OOB, 0);
        }
      } else if (u < 432) {
        const int uu = u - 320, jj = uu / 14, kp = uu - 14 * jj;
        const rsrc_t r = mkrs(p.wout, FH * DM * 2);
#pragma unroll
        for (int pc = 0; pc < 10; ++pc) {
          const int idx = pc * 64 + lane, row = idx / 10, ch = idx - 10 * row;
          dma16<0>(r, sc + pc * 1024, (unsigned)((64 * kp + row) * (DM * 2) + 160 * jj + ch * 16), 0);
        }
      }
      lds_signal(lds, 15 + h, lane);
      wait_vm<0>();
      CFENCE();
      if (u < 320) {
        const int jp = u / 20, kf = u - 20 * jp, jj = jp >> 1, part = jp & 1;
        const rsrc_t ro = mkrs(ws + WS_WINR, 8 * 20 * 14336);
#pragma unroll
        for (int nf = 0; nf < 7; ++nf) {
          LAS char* a0 = sc + (8 * q + qq) * 224 + (16 * nf + 4 * pp) * 2;
          const u32x2 lo = __builtin_bit_cast(u32x2, __builtin_amdgcn_ds_read_tr16_b64_v4i16((LAS s16x4*)a0));
          const u32x2 hi = __builtin_bit_cast(u32x2, __builtin_amdgcn_ds_read_tr16_b64_v4i16((LAS s16x4*)(a0 + 4 * 224)));
          st16<SC1>((u32x4){lo.x, lo.y, hi.x, hi.y}, ro, (unsigned)((((jj * 2 + part) * 20 + kf) * 7 + nf) * 1024 + lane * 16), 0);
        }
      } else if (u < 432) {
        const int uu = u - 320, jj = uu / 14, kp = uu - 14 * jj;
        const rsrc_t ro = mkrs(ws + WS_WOUTR, 8 * 14 * 10240);
#pragma unroll
        for (int f = 0; f < 10; ++f) {
          const int kk = f / 5, nf = f - 5 * kk;
          LAS char* a0 = sc + (32 * kk + 8 * q + qq) * 160 + (16 * nf + 4 * pp) * 2;
          const u32x2 lo = __builtin_bit_cast(u32x2, __builtin_amdgcn_ds_read_tr16_b64_v4i16((LAS s16x4*)a0));
          const u32x2 hi = __builtin_bit_cast(u32x2, __builtin_amdgcn_ds_read_tr16_b64_v4i16((LAS s16x4*)(a0 + 4 * 160)));
          st16<SC1>((u32x4){lo.x, lo.y, hi.x, hi.y}, ro, (unsigned)(((jj * 14 + kp) * 10 + f) * 1024 + lane * 16), 0);
        }
      }
      wait_lgkm0(); CFENCE();
    }
    const u64 T = epoch_tag(Ein);
    wait_vm<0>();
    if (arrive_last(lds, 7, 3, lane)) set_flag(ws, WS_FLAGB + g * 128 + j * 8, T, lane);

    {
      if (h == 0) {
        lds_wait(lds, 14);
        __builtin_amdgcn_s_sleep(F0DELAY);
        poll_flags256(ws, WS_FLAG0 + xg * 4096, T, lane);
        lds_signal(lds, 12, lane);
      } else {
        lds_wait(lds, 12);
      }
      const rsrc_t r = mkrs(ws + WS_XNT + (size_t)j * 32 * 10240, 32 * 10240);
      auto issue = [&](int t, int slot) {
        const unsigned k = (unsigned)((t + rot) & 31) * 10240;
#pragma unroll
        for (int pc = 0; pc < 5; ++pc) dma16<SC1>(r, lds + P1_DOFF + slot * 10240 + (h * 5 + pc) * 1024, lane * 16, k + (h * 5 + pc) * 1024);
      };
      loader_fill<0, 2, 5>(issue);
      loader_run<32, P1_DB, 5, 2, 2, -1>(issue, [&](int i) {
        if (i == 20 && h == 1) {
          const int bi = g * 8 + j;
          const rsrc_t r3 = mkrs(p.wdre, DM * DM * 2), r4 = mkrs(p.wdim, DM * DM * 2);
          __builtin_amdgcn_raw_ptr_buffer_load_lds(r3, (LAS void*)(lds + L_SINK), 4, lane < 25 ? (int)((bi * 25 + lane) * 128) : (int)OOB, 0, 0, 0);
          __builtin_amdgcn_raw_ptr_buffer_load_lds(r4, (LAS void*)(lds + L_SINK), 4, lane < 25 ? (int)((bi * 25 + lane) * 128) : (int)OOB, 0, 0, 0);
        }
      });
      BAR(); BAR();
    }
    if (h == 0 && b < 112) {
      const int jj = b / 14, pn = b - 14 * jj, part = pn / 7, nf = pn - 7 * part;
      const rsrc_t rw = mkrs(ws + WS_WINR, 8 * 20 * 14336), rm = mkrs(ws + WS_MODP, 4 * 3840 * 4);
      u32x4 wf[20];
#pragma unroll
      for (int k = 0; k < 20; ++k) wf[k] = ld16<SC1>(rw, lane * 16, (unsigned)((((jj * 2 + part) * 20 + k) * 7 + nf) * 1024));
#pragma unroll
      for (int it = 0; it < 5; ++it) {
        const int c4 = (it * 64 + lane) * 4, isS = c4 >= DM, cc = c4 - DM * isS;
        const unsigned o = (unsigned)(((isS ? 3 : 4) * DM + cc) * 4);
        const f32x4 m0 = __builtin_bit_cast(f32x4, ld16<SC1>(rm, o, 0)), m1 = __builtin_bit_cast(f32x4, ld16<SC1>(rm, o, 3840 * 4)),
                    m2 = __builtin_bit_cast(f32x4, ld16<SC1>(rm, o, 2 * 3840 * 4)), m3 = __builtin_bit_cast(f32x4, ld16<SC1>(rm, o, 3 * 3840 * 4));
        f32x4 s = (m0 + m1) + (m2 + m3);
#pragma unroll
        for (int i = 0; i < 4; ++i) { s[i] = rbf(s[i]); if (!isS) s[i] = 1.0f + s[i]; }
        *(LAS u32x2*)(lds + L_TABE + (isS * DM + cc) * 2) = (u32x2){pk2(s[0], s[1]), pk2(s[2], s[3])};
      }
      wait_lgkm0(); CFENCE();
      f32x4 acc = {0.f, 0.f, 0.f, 0.f};
#pragma unroll
      for (int k = 0; k < 20; ++k) {
        u32x4 e = *(LAS u32x4*)(lds + L_TABE + ((l15 & 1) * DM + 32 * k + 8 * q) * 2);
        if (l15 >= 2) e = (u32x4){0u, 0u, 0u, 0u};
        acc = mfma(asfrag(e), asfrag(wf[k]), acc);
      }
      const rsrc_t ruv = mkrs(ws + WS_UV, 2 * 896 * 16);
      st16<SC1>((u32x4){__float_as_uint(acc[0]), __float_as_uint(acc[1]), (unsigned)T, (unsigned)(T >> 32)}, ruv,
                q == 0 ? (unsigned)((part * 896 + 112 * jj + 16 * nf + l15) * 16) : OOB, 0);
    }

    idle_phase<22>();
    idle_phase<12>();
    if (h == 1) {
      const rsrc_t r1 = mkrs(p.wsp + (size_t)(64 * g + 8 * j) * SEQ, 8 * SEQ * 2);
#pragma unroll
      for (int it = 0; it < 4; ++it)
        __builtin_amdgcn_raw_ptr_buffer_load_lds(r1, (LAS void*)(lds + L_SINK), 4, (int)((lane >> 3) * 4096 + ((it * 8 + (lane & 7) + rot) & 31) * 128), 0, 0, 0);
    }
    idle_phase<11>();
    {
      group_wait(lds, 17, ws, WS_FLAG3 + j * 4096, 32, T, h == 0, lane);
      const rsrc_t r = mkrs(ws + WS_Z2T + (size_t)j * 32 * 14336, 32 * 14336);
      auto issue = [&](int t, int slot) {
        const unsigned k = (unsigned)((t + rot) & 31) * 14336;
#pragma unroll
        for (int pc = 0; pc < 7; ++pc) dma16<SC1>(r, lds + P4_DOFF + slot * 14336 + (h * 7 + pc) * 1024, lane * 16, k + (h * 7 + pc) * 1024);
      };
      loader_fill<0, 2, 7>(issue);
      loader_run<32, P4_DB, 7, 2, 2, -1>(issue);
      BAR(); BAR();
    }
    idle_phase<15>();
  }
}

extern "C" void kernel_launch(void* const* d_in, const int* in_sizes, int n_in, void* d_out, int out_size, void* d_ws, size_t ws_size,
                              hipStream_t stream) {
  Params p;
  p.x = (const u16*)d_in[0]; p.wsre = (const u16*)d_in[1]; p.wsim = (const u16*)d_in[2]; p.wdre = (const u16*)d_in[3]; p.wdim = (const u16*)d_in[4];
  p.win = (const u16*)d_in[6]; p.wsp = (const u16*)d_in[7]; p.bsp = (const u16*)d_in[8]; p.wout = (const u16*)d_in[9];
  p.c = (const u16*)d_in[10]; p.wmod = (const u16*)d_in[11];
  p.out = (u16*)d_out; p.ws = (char*)d_ws + ((ws_size - WS_USED) & ~(size_t)0x1fffff);
  (void)hipFuncSetAttribute((const void*)adaLN_cond__spectral_fnet__mixer_gmlp__prompt_kernel, hipFuncAttributeMaxDynamicSharedMemorySize, LDS_BYTES);
  adaLN_cond__spectral_fnet__mixer_gmlp__prompt_kernel<<<256, 768, LDS_BYTES, stream>>>(p);
}
```
